```python
import torch

def _softmax(x, axis=-1):
    return torch.softmax(x, dim=axis)
def _gelu(x):
    return torch.nn.functional.gelu(x, approximate="tanh")
def _silu(x):
    return torch.nn.functional.silu(x)
def _sigmoid(x):
    return torch.sigmoid(x)
def _layernorm(x, axis=-1, eps=1e-5):
    m = x.mean(dim=axis, keepdim=True)
    v = x.var(dim=axis, keepdim=True, unbiased=False)
    return (x - m) / torch.sqrt(v + eps)
def _scatter_add(tgt, idx, src, axis=0):
    return tgt.clone().index_add_(axis, idx, src)
def _segment_sum(src, seg, n):
    out = torch.zeros((n,) + src.shape[1:], dtype=src.dtype)
    return out.index_add_(0, seg, src)
def _pad(x, pw):
    flat = tuple(v for pair in reversed(pw) for v in pair)
    return torch.nn.functional.pad(x, flat)

kernel_name = 'interleave_layers__sliding_block__glu_gated_glu__prompt'

def setup_inputs():

    _w = (lambda a: (lambda t: t.to(torch.bfloat16) if t.is_floating_point() else t)(torch.as_tensor(a)))
    def _A_setup():
        B, S, D = 1, 2048, 512
        return {
            "x": _w((lambda *s: torch.randn(*s, dtype=torch.bfloat16) * 0.05)(B, S, D)),
            "wqkv": _w((lambda *s: torch.randn(*s, dtype=torch.bfloat16) * 0.05)(D, 3 * D)),
            "wo": _w((lambda *s: torch.randn(*s, dtype=torch.bfloat16) * 0.05)(D, D)),
        }
    _A_in = _A_setup()

    def _B_setup():
        B, S, D, F = 1, 2048, 512, 1792
        return {
            "x": _w((lambda *s: torch.randn(*s, dtype=torch.bfloat16) * 0.05)(B, S, D)),
            "w_gate": _w((lambda *s: torch.randn(*s, dtype=torch.bfloat16) * 0.05)(D, F)),
            "w_up": _w((lambda *s: torch.randn(*s, dtype=torch.bfloat16) * 0.05)(D, F)),
            "w_down": _w((lambda *s: torch.randn(*s, dtype=torch.bfloat16) * 0.05)(F, D)),
        }
    _B_in = _B_setup()

    return {
        "A_x": _A_in["x"],
        "A_wqkv": _A_in["wqkv"],
        "A_wo": _A_in["wo"],
        "B_x": _B_in["x"],
        "B_w_gate": _B_in["w_gate"],
        "B_w_up": _B_in["w_up"],
        "B_w_down": _B_in["w_down"],
    }

def reference(A_x, A_wqkv, A_wo, B_x, B_w_gate, B_w_up, B_w_down):
    def _A_ref(x, wqkv, wo):
        B, S, D = x.shape
        H = 32; Dh = D // H; W = 256
        qkv = (lambda x, *d: x.permute(*d))((x @ wqkv).reshape(B, S, 3, H, Dh), 2, 0, 3, 1, 4)
        q, k, v = qkv[0], qkv[1], qkv[2]
        scores = torch.einsum("bhqd,bhkd->bhqk", q, k) * (Dh ** -0.5)
        idx = torch.arange(S)
        mask = (idx[:, None] - idx[None, :] >= 0) & (idx[:, None] - idx[None, :] < W)
        scores = torch.where(mask[None, None], scores, -1e9)
        p = _softmax(scores, axis=-1)
        o = torch.einsum("bhqk,bhkd->bhqd", p, v)
        return (lambda x, *d: x.permute(*d))(o, 0, 2, 1, 3).reshape(B, S, D) @ wo

    def _B_ref(x, w_gate, w_up, w_down):
        g = torch.einsum("bsd,df->bsf", x, w_gate)
        u = torch.einsum("bsd,df->bsf", x, w_up)
        h = _sigmoid(g) * u
        return torch.einsum("bsf,fd->bsd", h, w_down)

    x = A_x
    x = x + _A_ref(_layernorm(x, axis=-1), A_wqkv, A_wo)
    x = x + _B_ref(_layernorm(x, axis=-1), B_w_gate, B_w_up, B_w_down)
    x = x + _A_ref(_layernorm(x, axis=-1), A_wqkv, A_wo)
    x = x + _B_ref(_layernorm(x, axis=-1), B_w_gate, B_w_up, B_w_down)
    return x
```

```cpp
#include <hip/hip_runtime.h>
#include <hip/hip_bf16.h>
typedef unsigned short u16;
static __device__ __forceinline__ float b2f(u16 v){ return __uint_as_float(((unsigned)v)<<16); }
static __device__ __forceinline__ u16 f2b(float f){ unsigned u=__float_as_uint(f); u += 0x7fffu + ((u>>16)&1u); return (u16)(u>>16); }
constexpr int S=2048,D=512,F=1792,H=32,DH=16,W=256;

__global__ void ln_k(const u16* x, u16* y){
  int row=blockIdx.x; int t=threadIdx.x;
  float v[8]; float s=0;
  for(int i=0;i<8;i++){ v[i]=b2f(x[row*D+t*8+i]); s+=v[i]; }
  for(int m=32;m>=1;m>>=1) s+=__shfl_xor(s,m);
  float mean=s/D; float q=0;
  for(int i=0;i<8;i++){ float d=v[i]-mean; q+=d*d; }
  for(int m=32;m>=1;m>>=1) q+=__shfl_xor(q,m);
  float r=1.0f/sqrtf(q/D+1e-5f);
  for(int i=0;i<8;i++) y[row*D+t*8+i]=f2b((v[i]-mean)*r);
}
__global__ void gemm_k(const u16* A,const u16* B,u16* C,const u16* res,int N,int K,int mode){
  __shared__ float As[16][33]; __shared__ float Bs[32][17];
  int tx=threadIdx.x, ty=threadIdx.y; int row=blockIdx.y*16+ty, col=blockIdx.x*16+tx;
  float acc=0;
  for(int k0=0;k0<K;k0+=32){
    As[ty][tx]=b2f(A[row*K+k0+tx]); As[ty][tx+16]=b2f(A[row*K+k0+16+tx]);
    Bs[ty][tx]=b2f(B[(k0+ty)*N+col]); Bs[ty+16][tx]=b2f(B[(k0+16+ty)*N+col]);
    __syncthreads();
    for(int k=0;k<32;k++) acc+=As[ty][k]*Bs[k][tx];
    __syncthreads();
  }
  if(mode==0) C[row*N+col]=f2b(acc);
  else C[row*N+col]=f2b(b2f(res[row*N+col])+b2f(f2b(acc)));
}
__global__ void attn_k(const u16* qkv,u16* o){
  int q=blockIdx.x*64+threadIdx.x; int h=blockIdx.y;
  float qv[16]; for(int d=0;d<16;d++) qv[d]=b2f(qkv[q*1536+h*16+d]);
  int k0=q-255; if(k0<0)k0=0;
  float m=-1e30f;
  for(int k=k0;k<=q;k++){ float s=0; for(int d=0;d<16;d++) s+=qv[d]*b2f(qkv[k*1536+512+h*16+d]); s*=0.25f; m=fmaxf(m,s);}
  float l=0; float acc[16]; for(int d=0;d<16;d++)acc[d]=0;
  for(int k=k0;k<=q;k++){ float s=0; for(int d=0;d<16;d++) s+=qv[d]*b2f(qkv[k*1536+512+h*16+d]); s*=0.25f; float p=__expf(s-m); l+=p; for(int d=0;d<16;d++) acc[d]+=p*b2f(qkv[k*1536+1024+h*16+d]); }
  for(int d=0;d<16;d++) o[q*512+h*16+d]=f2b(acc[d]/l);
}
__global__ void glu_k(const u16* g,const u16* u,u16* h,int n){
  int i=blockIdx.x*256+threadIdx.x; if(i<n){ float gg=b2f(g[i]); float s=1.0f/(1.0f+__expf(-gg)); h[i]=f2b(b2f(f2b(s))*b2f(u[i])); }
}
extern "C" void kernel_launch(void* const* d_in, const int* in_sizes, int n_in, void* d_out, int out_size, void* d_ws, size_t ws_size, hipStream_t stream){
  const u16* x=(const u16*)d_in[0]; const u16* wqkv=(const u16*)d_in[1]; const u16* wo=(const u16*)d_in[2];
  const u16* wg=(const u16*)d_in[4]; const u16* wu=(const u16*)d_in[5]; const u16* wd=(const u16*)d_in[6];
  u16* ws=(u16*)d_ws; u16* xn=ws; u16* qkv=xn+S*D; u16* o=qkv+S*3*D; u16* x1=o+S*D; u16* g=x1+S*D; u16* u=g+S*F; u16* hh=u+S*F; u16* x2=hh+S*F;
  const u16* cur=x;
  for(int l=0;l<2;l++){
    u16* outp = l==0? x2 : (u16*)d_out;
    ln_k<<<S,64,0,stream>>>(cur,xn);
    gemm_k<<<dim3(1536/16,S/16),dim3(16,16),0,stream>>>(xn,wqkv,qkv,nullptr,1536,512,0);
    attn_k<<<dim3(S/64,H),64,0,stream>>>(qkv,o);
    gemm_k<<<dim3(512/16,S/16),dim3(16,16),0,stream>>>(o,wo,x1,cur,512,512,1);
    ln_k<<<S,64,0,stream>>>(x1,xn);
    gemm_k<<<dim3(F/16,S/16),dim3(16,16),0,stream>>>(xn,wg,g,nullptr,F,512,0);
    gemm_k<<<dim3(F/16,S/16),dim3(16,16),0,stream>>>(xn,wu,u,nullptr,F,512,0);
    glu_k<<<(S*F+255)/256,256,0,stream>>>(g,u,hh,S*F);
    gemm_k<<<dim3(512/16,S/16),dim3(16,16),0,stream>>>(hh,wd,outp,x1,512,F,1);
    cur=x2;
  }
}
```

```cpp
#include <hip/hip_runtime.h>
#include <stdint.h>
#include <type_traits>

typedef unsigned short u16;
typedef float f32x16 __attribute__((ext_vector_type(16)));
typedef float f32x4 __attribute__((ext_vector_type(4)));
typedef float f32x2 __attribute__((ext_vector_type(2)));
typedef __bf16 bf16x8 __attribute__((ext_vector_type(8)));
typedef __bf16 bf16x2 __attribute__((ext_vector_type(2)));
typedef short s16x4 __attribute__((ext_vector_type(4)));
typedef unsigned v4u __attribute__((ext_vector_type(4)));
typedef unsigned v2u __attribute__((ext_vector_type(2)));
#define GAS __attribute__((address_space(1)))
#define LAS __attribute__((address_space(3)))
#define DI __device__ __forceinline__
#define RLX_AGENT __ATOMIC_RELAXED, __HIP_MEMORY_SCOPE_AGENT
typedef __amdgpu_buffer_rsrc_t rsrc_t;

constexpr int SEQ = 2048, DM = 512, FF = 1792;
constexpr unsigned KB = 1024u, MB = 1u << 20;
constexpr unsigned OFF_FLAGS = 0;
constexpr unsigned FL_X = 0;
constexpr unsigned FL_O = FL_X + 3 * 64;
constexpr unsigned FL_H = FL_O + 2 * 64 * 2;
constexpr unsigned FL_KV = FL_H + 2 * 64 * 2;
constexpr unsigned FL_LINES = FL_KV + 2 * 32 * 8;
constexpr unsigned OFF_AUG = 256 * KB;
constexpr unsigned AUG_QK = 0, AUG_V = 1024 * 8, AUG_GU = 1536 * 8;
constexpr unsigned OFF_WQK = 1 * MB, OFF_WV = 2 * MB, OFF_WO = 2 * MB + 512 * KB, OFF_WGU = 3 * MB, OFF_WD = 6 * MB + 512 * KB;
constexpr unsigned OFF_X0 = 9 * MB, OFF_X1 = 11 * MB, OFF_X2 = 15 * MB, OFF_O = 17 * MB, OFF_KV = 21 * MB, OFF_H = 29 * MB;
constexpr unsigned WS_BYTES = 43 * MB;
constexpr unsigned R0 = 0, R1 = 32 * KB, R2 = 64 * KB, R3 = 96 * KB, R4V = 128 * KB, R4S = 144 * KB, LDS_BYTES = 160 * KB;
constexpr float NEG_BIG = -1.0e30f;
constexpr float LOG2E = 1.4426950408889634f;
constexpr unsigned SPIN_CAP = 1u << 18;

template <int B, int E, class F> DI void sfor(F&& f) {
  if constexpr (B < E) { f(std::integral_constant<int, B>{}); sfor<B + 1, E>(f); }
}
DI bf16x8 as_bf8(v4u v) { return __builtin_bit_cast(bf16x8, v); }
DI bf16x2 as_bf2(unsigned v) { return __builtin_bit_cast(bf16x2, v); }
DI s16x4 as_s4(v2u v) { return __builtin_bit_cast(s16x4, v); }
DI f32x16 mfma16(v4u a, v4u b, f32x16 c) { return __builtin_amdgcn_mfma_f32_32x32x16_bf16(as_bf8(a), as_bf8(b), c, 0, 0, 0); }
DI f32x16 mfma8(v2u a, v2u b, f32x16 c) { return __builtin_amdgcn_mfma_f32_32x32x8bf16_1k(as_s4(a), as_s4(b), c, 0, 0, 0); }
DI unsigned cvtpk(float lo, float hi) { f32x2 f = {lo, hi}; bf16x2 p = __builtin_convertvector(f, bf16x2); return __builtin_bit_cast(unsigned, p); }
DI float bflo(unsigned v) { return __uint_as_float(v << 16); }
DI float bfhi(unsigned v) { return __uint_as_float(v & 0xffff0000u); }
DI float dot2(unsigned a, unsigned b, float c) { return __builtin_amdgcn_fdot2_f32_bf16(as_bf2(a), as_bf2(b), c, false); }
DI float xhalf_add(float v) { auto r = __builtin_amdgcn_permlane32_swap(__float_as_uint(v), __float_as_uint(v), false, false); return __uint_as_float(r[0]) + __uint_as_float(r[1]); }
DI float xhalf_max(float v) { auto r = __builtin_amdgcn_permlane32_swap(__float_as_uint(v), __float_as_uint(v), false, false); return fmaxf(__uint_as_float(r[0]), __uint_as_float(r[1])); }
DI v4u ld16(rsrc_t rs, unsigned voff, unsigned soff) { return __builtin_amdgcn_raw_buffer_load_b128(rs, voff, soff, 16); }
DI v2u ld8p(rsrc_t rs, unsigned voff, unsigned soff) { return __builtin_bit_cast(v2u, __builtin_amdgcn_raw_buffer_load_b64(rs, voff, soff, 0)); }
DI void st16(v4u v, rsrc_t rs, unsigned voff, unsigned soff) { __builtin_amdgcn_raw_buffer_store_b128(v, rs, voff, soff, 16); }
DI v4u lds16(LAS unsigned char* L, unsigned off) { return *(LAS v4u*)(L + off); }
#define SB() __builtin_amdgcn_sched_barrier(0)
#define OPQV(x) asm volatile("" : "+v"(x))
#define OPQS(x) asm volatile("" : "+s"(x))
#define LANE_CONSTS() int lane = threadIdx.x & 63; OPQV(lane); const int r = lane & 31, h = lane >> 5; const unsigned lane16 = lane * 16; (void)r; (void)h
#define BAR() asm volatile("s_waitcnt lgkmcnt(0)\n\ts_barrier" ::: "memory")
#define VMWAIT(n) asm volatile("s_waitcnt vmcnt(" #n ")" ::: "memory")
DI void dma4(rsrc_t rs, unsigned lds, unsigned soff, unsigned lane16) {
  asm volatile("s_nop 4\n\ts_mov_b32 m0, %0\n\ts_nop 0\n\t"
               "buffer_load_dwordx4 %1, %2, %3 offen lds\n\t"
               "buffer_load_dwordx4 %1, %2, %3 offen offset:1024 lds\n\t"
               "buffer_load_dwordx4 %1, %2, %3 offen offset:2048 lds\n\t"
               "buffer_load_dwordx4 %1, %2, %3 offen offset:3072 lds"
               :: "s"(lds), "v"(lane16), "s"(rs), "s"(soff) : "memory");
}
DI void dma2(rsrc_t rs, unsigned lds, unsigned soff, unsigned lane16) {
  asm volatile("s_nop 4\n\ts_mov_b32 m0, %0\n\ts_nop 0\n\t"
               "buffer_load_dwordx4 %1, %2, %3 offen lds\n\t"
               "buffer_load_dwordx4 %1, %2, %3 offen offset:1024 lds"
               :: "s"(lds), "v"(lane16), "s"(rs), "s"(soff) : "memory");
}
DI void dma2_sc1(rsrc_t rs, unsigned lds, unsigned soff, unsigned lane16) {
  asm volatile("s_nop 4\n\ts_mov_b32 m0, %0\n\ts_nop 0\n\t"
               "buffer_load_dwordx4 %1, %2, %3 offen sc1 lds\n\t"
               "buffer_load_dwordx4 %1, %2, %3 offen offset:1024 sc1 lds"
               :: "s"(lds), "v"(lane16), "s"(rs), "s"(soff) : "memory");
}
DI void dma_tile(rsrc_t rs, unsigned lds, unsigned soff, unsigned lw, unsigned lane16) {
  dma4(rs, lds + lw * 8192u, soff + lw * 8192u, lane16);
  dma4(rs, lds + lw * 8192u + 4096u, soff + lw * 8192u + 4096u, lane16);
}
DI unsigned poll_ld(GAS unsigned* p) { return __hip_atomic_load(p, RLX_AGENT); }
DI void wait_flags(GAS unsigned* p, unsigned target) {
#pragma nounroll
  for (unsigned spins = 0; spins < SPIN_CAP; ++spins) {
    const unsigned f = poll_ld(p);
    if (__all(f >= target)) break;
    __builtin_amdgcn_s_sleep(1);
  }
  asm volatile("" ::: "memory");
}
DI void set_flag(GAS unsigned* p, unsigned v, int lane) { if (lane == 0) __hip_atomic_store(p, v, RLX_AGENT); }

struct Stats { float rstd; v2u aug; };
DI Stats make_stats(float s1, float s2, int h) {
  s1 = xhalf_add(s1); s2 = xhalf_add(s2);
  const float mean = s1 * (1.0f / DM);
  const float var = fmaxf(s2 * (1.0f / DM) - mean * mean, 0.0f);
  Stats st; st.rstd = __builtin_amdgcn_rsqf(var + 1e-5f);
  const float nm = h ? 0.0f : -mean;
  const unsigned hi = cvtpk(nm, nm) & 0xffffu;
  const float lo = nm - __uint_as_float(hi << 16);
  const unsigned w = hi | (cvtpk(lo, lo) << 16);
  st.aug.x = w; st.aug.y = w;
  return st;
}
#define STATS_STEP(xf) do { const unsigned a_ = (xf).x, b_ = (xf).y, c_ = (xf).z, d_ = (xf).w; \
  s1a = dot2(a_, 0x3f803f80u, s1a); s2a = dot2(a_, a_, s2a); s1b = dot2(b_, 0x3f803f80u, s1b); s2b = dot2(b_, b_, s2b); \
  s1a = dot2(c_, 0x3f803f80u, s1a); s2a = dot2(c_, c_, s2a); s1b = dot2(d_, 0x3f803f80u, s1b); s2b = dot2(d_, d_, s2b); } while (0)

DI float max16(const f32x16& s, float m) {
  m = fmaxf(fmaxf(m, s[0]), s[1]);   m = fmaxf(fmaxf(m, s[2]), s[3]);
  float n = fmaxf(s[4], s[5]);       n = fmaxf(fmaxf(n, s[6]), s[7]);
  m = fmaxf(fmaxf(m, s[8]), s[9]);   n = fmaxf(fmaxf(n, s[10]), s[11]);
  m = fmaxf(fmaxf(m, s[12]), s[13]); n = fmaxf(fmaxf(n, s[14]), s[15]);
  return fmaxf(m, n);
}
DI void exp_pv(const f32x16& s, v4u v0, v4u v1, f32x16& o) {
  v4u p0, p1;
  p0.x = cvtpk(__builtin_amdgcn_exp2f(s[0]), __builtin_amdgcn_exp2f(s[1]));
  p0.y = cvtpk(__builtin_amdgcn_exp2f(s[2]), __builtin_amdgcn_exp2f(s[3]));
  p0.z = cvtpk(__builtin_amdgcn_exp2f(s[4]), __builtin_amdgcn_exp2f(s[5]));
  p0.w = cvtpk(__builtin_amdgcn_exp2f(s[6]), __builtin_amdgcn_exp2f(s[7]));
  o = mfma16(v0, p0, o);
  p1.x = cvtpk(__builtin_amdgcn_exp2f(s[8]), __builtin_amdgcn_exp2f(s[9]));
  p1.y = cvtpk(__builtin_amdgcn_exp2f(s[10]), __builtin_amdgcn_exp2f(s[11]));
  p1.z = cvtpk(__builtin_amdgcn_exp2f(s[12]), __builtin_amdgcn_exp2f(s[13]));
  p1.w = cvtpk(__builtin_amdgcn_exp2f(s[14]), __builtin_amdgcn_exp2f(s[15]));
  o = mfma16(v1, p1, o);
}
template <class H>
DI void attn_stage(LAS unsigned char* L, v4u q, const f32x16& cm, int bm, int bf0, int nf, unsigned kaddr, unsigned vbase, unsigned vsel,
                   float& m, f32x16& o, bool first, H&& hook) {
  const f32x16 zero = {};
  f32x16 s = mfma16(lds16(L, kaddr + bm * 2048), q, cm);
  float mx = NEG_BIG;
  for (int b = 0; b < nf; ++b) {
    const f32x16 sn = mfma16(lds16(L, kaddr + (bf0 + b) * 2048), q, zero);
    mx = max16(s, mx);
    s = sn;
  }
  mx = max16(s, mx);
  mx = xhalf_max(mx);
  hook();
  if (!first) {
    mx = fmaxf(mx, m);
    const float f = __builtin_amdgcn_exp2f(m - mx);
#pragma unroll
    for (int i = 0; i < 9; ++i) o[i] *= f;
  }
  m = mx;
  f32x16 nm, cmm;
#pragma unroll
  for (int i = 0; i < 16; ++i) { nm[i] = -mx; cmm[i] = cm[i] - mx; }
  s = mfma16(lds16(L, kaddr + bm * 2048), q, cmm);
  unsigned va = vbase + vsel * (unsigned)(bm * 2048);
  v4u v0 = lds16(L, va), v1 = lds16(L, va + 512);
  for (int b = 0; b < nf; ++b) {
    const f32x16 sn = mfma16(lds16(L, kaddr + (bf0 + b) * 2048), q, nm);
    va = vbase + vsel * (unsigned)((bf0 + b) * 2048);
    const v4u w0 = lds16(L, va), w1 = lds16(L, va + 512);
    exp_pv(s, v0, v1, o);
    s = sn; v0 = w0; v1 = w1;
  }
  exp_pv(s, v0, v1, o);
}

struct Args { unsigned char* ws; u16* out; };

__global__ void __launch_bounds__(512, 2) main_k(Args a) {
  extern __shared__ __attribute__((aligned(16))) unsigned char smem[];
  LAS unsigned char* L = (LAS unsigned char*)smem;
  const unsigned lb = (unsigned)(size_t)L;
  const int w0 = __builtin_amdgcn_readfirstlane(threadIdx.x >> 6);
  const int xcd = blockIdx.x & 7, idx = blockIdx.x >> 3;
  const int sg0 = xcd, j0 = idx;
  const rsrc_t rs = __builtin_amdgcn_make_buffer_rsrc(a.ws, 0, WS_BYTES, 0x00020000);
  GAS unsigned* FL = (GAS unsigned*)a.ws;
#define ROLES() int w = w0, sg = sg0, j = j0, layer = layer0; OPQS(w); OPQS(sg); OPQS(j); OPQS(layer); \
  const int head = j, tbl = w < 4 ? w : 11 - w, Tq = 8 * sg + tbl;              \
  const int grp = 2 * sg + (j >> 4), mem = j & 15, nblk = j & 7;                \
  const int cw = w & 3, th = cw >> 1, kh = cw & 1; \
  const int tb2 = 4 * grp + 2 * ((j >> 3) & 1) + th, tb3 = 4 * grp + cw;        \
  (void)head; (void)Tq; (void)mem; (void)nblk; (void)kh; (void)tb2; (void)tb3
  {
    const int layer0 = 0;
    ROLES(); LANE_CONSTS();
    dma4(rs, lb + R0 + w * 4096u, OFF_WQK + head * 32768u + w * 4096u, lane16);
    dma2(rs, lb + R4V + w * 2048u, OFF_WV + head * 16384u + w * 2048u, lane16);
    VMWAIT(0);
  }

  for (int layer0 = 0; layer0 < 2; ++layer0) {
    BAR();
    {
      ROLES(); LANE_CONSTS();
      if (w == 0 && lane < 2) *(LAS v4u*)(L + R4S + lane * 512) = v4u{0x3f803f80u, 0x3f803f80u, 0x3f803f80u, 0x3f803f80u};
      const unsigned xb = (layer ? OFF_X2 : OFF_X0) + Tq * 32768u;
      const v2u aqk = ld8p(rs, r * 8, OFF_AUG + AUG_QK + head * 256u);
      const v2u av = ld8p(rs, (lane & 15) * 8, OFF_AUG + AUG_V + head * 128u);
      if (layer) wait_flags(FL + (FL_X + 64 + Tq) * 32 + (lane & 15), 1u);
      v4u xr[8];
      sfor<0, 8>([&](auto I) { constexpr int i = I; xr[i] = ld16(rs, lane16 + (i & 3) * 1024, xb + (i >> 2) * 4096); });
      const unsigned vwa = R4V + (h * 16 + (lane & 15)) * 16;
      v4u wq[2], wv[2];
      wq[0] = lds16(L, R0 + lane16); wv[0] = lds16(L, vwa);
      wq[1] = lds16(L, R0 + lane16 + 1024); wv[1] = lds16(L, vwa + 512);
      f32x16 cqk = {}, cv = {};
      float s1a = 0.f, s1b = 0.f, s2a = 0.f, s2b = 0.f;
      SB();
      sfor<0, 32>([&](auto I) {
        constexpr int s = I;
        const v4u xf = xr[s & 7];
        cqk = mfma16(wq[s & 1], xf, cqk);
        cv = mfma16(xf, wv[s & 1], cv);
        if constexpr (s + 2 < 32) { wq[s & 1] = lds16(L, R0 + lane16 + (s + 2) * 1024); wv[s & 1] = lds16(L, vwa + (s + 2) * 512); }
        STATS_STEP(xf);
        if constexpr (s + 8 < 32) xr[s & 7] = ld16(rs, lane16 + ((s + 8) & 3) * 1024, xb + ((s + 8) >> 2) * 4096);
        SB();
      });
      const Stats st = make_stats(s1a + s1b, s2a + s2b, h);
      cqk = mfma8(aqk, st.aug, cqk);
      cv = mfma8(st.aug, av, cv);
      const unsigned own = R1 + (8 + tbl) * 2048;
      if (h == 0) *(LAS float*)(L + own + 1024 + r * 4) = st.rstd;
      f32x4 rr[4];
#pragma unroll
      for (int g = 0; g < 4; ++g) rr[g] = *(LAS f32x4*)(L + own + 1024 + (8 * g + 4 * h) * 4);
      const float qs = st.rstd * (0.25f * LOG2E);
      v4u qf, kf, vf0, vf1;
      qf.x = cvtpk(cqk[0] * qs, cqk[1] * qs); qf.y = cvtpk(cqk[2] * qs, cqk[3] * qs);
      qf.z = cvtpk(cqk[4] * qs, cqk[5] * qs); qf.w = cvtpk(cqk[6] * qs, cqk[7] * qs);
      kf.x = cvtpk(cqk[8] * st.rstd, cqk[9] * st.rstd); kf.y = cvtpk(cqk[10] * st.rstd, cqk[11] * st.rstd);
      kf.z = cvtpk(cqk[12] * st.rstd, cqk[13] * st.rstd); kf.w = cvtpk(cqk[14] * st.rstd, cqk[15] * st.rstd);
      vf0.x = cvtpk(cv[0] * rr[0][0], cv[1] * rr[0][1]); vf0.y = cvtpk(cv[2] * rr[0][2], cv[3] * rr[0][3]);
      vf0.z = cvtpk(cv[4] * rr[1][0], cv[5] * rr[1][1]); vf0.w = cvtpk(cv[6] * rr[1][2], cv[7] * rr[1][3]);
      vf1.x = cvtpk(cv[8] * rr[2][0], cv[9] * rr[2][1]); vf1.y = cvtpk(cv[10] * rr[2][2], cv[11] * rr[2][3]);
      vf1.z = cvtpk(cv[12] * rr[3][0], cv[13] * rr[3][1]); vf1.w = cvtpk(cv[14] * rr[3][2], cv[15] * rr[3][3]);
      const bool up = (lane & 16) != 0;
      const v4u vf = up ? vf1 : vf0;
      const unsigned voff = 1024 + ((lane >> 4) & 1) * 512 + (h * 16 + (lane & 15)) * 16;
      *(LAS v4u*)(L + own + lane16) = kf;
      *(LAS v4u*)(L + own + voff) = vf;
      if (sg < 7) {
        const unsigned kvb = OFF_KV + layer * (4 * MB) + (head * 64 + Tq) * 2048u;
        st16(kf, rs, lane16, kvb); st16(vf, rs, voff, kvb);
      }
      BAR();
      dma4(rs, lb + R2 + w * 8192u, OFF_WO + nblk * 65536u + w * 8192u, lane16);
      dma4(rs, lb + R2 + w * 8192u + 4096u, OFF_WO + nblk * 65536u + w * 8192u + 4096u, lane16);
      f32x16 cm;
#pragma unroll
      for (int i = 0; i < 16; ++i) cm[i] = ((i & 3) + 8 * (i >> 2) + 4 * h <= r) ? 0.0f : NEG_BIG;
      const unsigned kaddr = R1 + lane16;
      const unsigned vsel = up ? 0u : 1u, vbase = up ? R4S : R1 + 1024 + (h * 16 + (lane & 15)) * 16;
      float m = NEG_BIG; f32x16 o = {};
      attn_stage(L, qf, cm, 8 + tbl, 8, tbl, kaddr, vbase, vsel, m, o, true,
                 [&] { if (sg < 7) { VMWAIT(8); set_flag(FL + (FL_KV + (layer * 32 + head) * 8 + sg) * 32 + tbl, 1u, lane); } });
      if (sg > 0) {
        wait_flags(FL + (FL_KV + (layer * 32 + head) * 8 + sg - 1) * 32 + w, 1u);
        dma2_sc1(rs, lb + R1 + w * 2048u, OFF_KV + layer * (4 * MB) + (head * 64 + 8 * (sg - 1) + w) * 2048u, lane16);
      }
      VMWAIT(0);
      BAR();
      dma4(rs, lb + R0 + w * 4096u, OFF_WGU + (mem * 7 + 0) * 32768u + w * 4096u, lane16);
      if (sg > 0) {
#pragma unroll
        for (int i = 0; i < 16; ++i) cm[i] = NEG_BIG - cm[i];
        attn_stage(L, qf, cm, tbl, tbl + 1, 7 - tbl, kaddr, vbase, vsel, m, o, false, [] {});
      }
      const float il = __builtin_amdgcn_rcpf(o[8]);
      v4u of;
      of.x = cvtpk(o[0] * il, o[1] * il); of.y = cvtpk(o[2] * il, o[3] * il); of.z = cvtpk(o[4] * il, o[5] * il); of.w = cvtpk(o[6] * il, o[7] * il);
      st16(of, rs, lane16, OFF_O + layer * (2 * MB) + (Tq * 32 + head) * 1024u);
      VMWAIT(0);
      set_flag(FL + (FL_O + (layer * 64 + Tq) * 2 + (head >> 4)) * 32 + (head & 15), 1u, lane);
    }
    BAR();
    if (w0 >= 4) {
      ROLES(); LANE_CONSTS();
      const unsigned lw = w - 4;
      const unsigned gu = OFF_WGU + mem * 7 * 32768u, wd = OFF_WD + nblk * 7 * 32768u;
      dma_tile(rs, lb + R1, gu + 1 * 32768u, lw, lane16);
      VMWAIT(0); BAR();
      dma_tile(rs, lb + R2, gu + 2 * 32768u, lw, lane16);
      dma_tile(rs, lb + R3, gu + 3 * 32768u, lw, lane16);
      VMWAIT(8); BAR();
      dma_tile(rs, lb + R0, gu + 4 * 32768u, lw, lane16);
      dma_tile(rs, lb + R1, gu + 5 * 32768u, lw, lane16);
      VMWAIT(16); BAR();
      dma_tile(rs, lb + R2, gu + 6 * 32768u, lw, lane16);
      VMWAIT(16); BAR();
      dma_tile(rs, lb + R3, wd + 0 * 32768u, lw, lane16);
      VMWAIT(16); BAR();
      dma_tile(rs, lb + R0, wd + 1 * 32768u, lw, lane16);
      VMWAIT(16); BAR();
      dma_tile(rs, lb + R1, wd + 2 * 32768u, lw, lane16);
      VMWAIT(16); BAR();
      dma_tile(rs, lb + R2, wd + 3 * 32768u, lw, lane16);
      VMWAIT(16); BAR();
      dma_tile(rs, lb + R3, wd + 4 * 32768u, lw, lane16);
      VMWAIT(16); BAR();
      dma_tile(rs, lb + R0, wd + 5 * 32768u, lw, lane16);
      VMWAIT(16); BAR();
      dma_tile(rs, lb + R1, wd + 6 * 32768u, lw, lane16);
      VMWAIT(16); BAR();
      if (layer == 0) {
        dma4(rs, lb + R4V + lw * 4096u, OFF_WV + head * 16384u + lw * 4096u, lane16);
        VMWAIT(12); BAR();
        VMWAIT(4); BAR();
        dma_tile(rs, lb + R0, OFF_WQK + head * 32768u, lw, lane16);
      } else {
        VMWAIT(8); BAR();
        VMWAIT(0); BAR();
      }
      BAR();
      VMWAIT(0);
    } else {
      {
        ROLES(); LANE_CONSTS();
        const unsigned xfo = (tb2 * 32 + 4 * nblk + 2 * kh) * 1024u;
        const unsigned sown = R4S + cw * 4096 + lane16, spart = R4S + (cw ^ 1) * 4096 + lane16;
        const unsigned xres = (layer ? OFF_X2 : OFF_X0) + xfo;
        const v4u r0 = ld16(rs, lane16, xres), r1 = ld16(rs, lane16 + 1024, xres);
        wait_flags(FL + (FL_O + (layer * 64 + tb2) * 2 + kh) * 32 + (lane & 15), 1u);
        const unsigned ob = OFF_O + layer * (2 * MB) + (tb2 * 32 + 16 * kh) * 1024u;
        v4u ofr[16];
        sfor<0, 16>([&](auto I) { constexpr int i = I; ofr[i] = ld16(rs, lane16 + (i & 3) * 1024, ob + (i >> 2) * 4096); });
        const unsigned wa = R2 + kh * 16384 + lane16;
        v4u ar[6];
        sfor<0, 6>([&](auto I) { constexpr int u = I; ar[u] = lds16(L, wa + (u & 1) * 32768 + (u >> 1) * 1024); });
        f32x16 c0 = {}, c1 = {};
        SB();
        sfor<0, 32>([&](auto I) {
          constexpr int u = I;
          if constexpr (u & 1) c1 = mfma16(ar[u % 6], ofr[u >> 1], c1); else c0 = mfma16(ar[u % 6], ofr[u >> 1], c0);
          if constexpr (u + 6 < 32) ar[u % 6] = lds16(L, wa + ((u + 6) & 1) * 32768 + ((u + 6) >> 1) * 1024);
          SB();
        });
        const f32x16 give = kh ? c0 : c1;
        f32x16 keep = kh ? c1 : c0;
#pragma unroll
        for (int g = 0; g < 4; ++g) *(LAS f32x4*)(L + sown + g * 1024) = f32x4{give[4 * g], give[4 * g + 1], give[4 * g + 2], give[4 * g + 3]};
        BAR();
#pragma unroll
        for (int g = 0; g < 4; ++g) { const f32x4 t = *(LAS f32x4*)(L + spart + g * 1024); keep[4 * g] += t[0]; keep[4 * g + 1] += t[1]; keep[4 * g + 2] += t[2]; keep[4 * g + 3] += t[3]; }
        v4u y0, y1;
        y0.x = cvtpk(keep[0] + bflo(r0.x), keep[1] + bfhi(r0.x)); y0.y = cvtpk(keep[2] + bflo(r0.y), keep[3] + bfhi(r0.y));
        y0.z = cvtpk(keep[4] + bflo(r0.z), keep[5] + bfhi(r0.z)); y0.w = cvtpk(keep[6] + bflo(r0.w), keep[7] + bfhi(r0.w));
        y1.x = cvtpk(keep[8] + bflo(r1.x), keep[9] + bfhi(r1.x)); y1.y = cvtpk(keep[10] + bflo(r1.y), keep[11] + bfhi(r1.y));
        y1.z = cvtpk(keep[12] + bflo(r1.z), keep[13] + bfhi(r1.z)); y1.w = cvtpk(keep[14] + bflo(r1.w), keep[15] + bfhi(r1.w));
        const unsigned xo = OFF_X1 + layer * (2 * MB) + xfo;
        st16(y0, rs, lane16, xo); st16(y1, rs, lane16 + 1024, xo);
        VMWAIT(0);
        set_flag(FL + (FL_X + (layer ? 128 : 0) + tb2) * 32 + 2 * nblk + kh, 1u, lane);
      }
      {
        ROLES(); LANE_CONSTS();
        unsigned fpre;
        v4u ar[6];
        const unsigned augb = OFF_AUG + AUG_GU + mem * 7 * 256u;
        v2u ag[3];
        ag[0] = ld8p(rs, r * 8, augb); ag[1] = ld8p(rs, r * 8 + 256, augb); ag[2] = ld8p(rs, r * 8 + 512, augb);
        wait_flags(FL + (FL_X + (layer ? 128 : 0) + tb3) * 32 + (lane & 15), 1u);
        const unsigned xb = OFF_X1 + layer * (2 * MB) + tb3 * 32768u;
        v4u x[32];
        sfor<0, 32>([&](auto I) { constexpr int i = I; x[i] = ld16(rs, lane16 + (i & 3) * 1024, xb + (i >> 2) * 4096); });
        const unsigned hb = OFF_H + layer * (7 * MB) + (tb3 * 112 + mem * 7) * 1024u;
        GAS unsigned* fh_mine = FL + (FL_H + (layer * 64 + tb3) * 2 + (mem >> 3)) * 32 + (mem & 7);
        auto aaddr = [&](auto U) -> unsigned {
          constexpr int u = U;
          if constexpr (u < 64) return ((u & 1) ? R1 : R0) + (u >> 1) * 1024 + lane16;
          else if constexpr (u < 224) { constexpr int T = 2 + (u - 64) / 32, s = (u - 64) % 32; constexpr unsigned sl[7] = {R0, R1, R2, R3, R0, R1, R2}; return sl[T] + s * 1024 + lane16; }
          else return R3 + kh * 16384 + (u - 224) * 1024 + lane16;
        };
        sfor<0, 6>([&](auto I) { constexpr int u = I; ar[u] = lds16(L, aaddr(I)); });
        f32x16 acc[3] = {};
        float s1a = 0.f, s1b = 0.f, s2a = 0.f, s2b = 0.f;
        float cg = 0.f, cu = 0.f; v2u augx = {0u, 0u};
        float hv[2]; v4u hf;
        auto epi = [&](auto P_, auto E_) {
          constexpr int p = P_, e = E_;
          const float ex = __builtin_amdgcn_exp2f(acc[p % 3][e] * cg);
          hv[e & 1] = __builtin_amdgcn_rcpf(1.0f + ex) * (acc[p % 3][8 + e] * cu);
          if constexpr (e & 1) hf[e >> 1] = cvtpk(hv[0], hv[1]);
          if constexpr (e == 7) st16(hf, rs, lane16 + (p & 3) * 1024, hb + (p >> 2) * 4096);
        };
        SB();
        sfor<0, 224>([&](auto I) {
          constexpr int u = I;
          constexpr int T = u < 64 ? (u & 1) : 2 + (u - 64) / 32, s = u < 64 ? (u >> 1) : (u - 64) % 32;
          constexpr int left = u < 64 ? 64 - u : 32 - s;
          if constexpr (left == 4) {
            BAR();
            ar[(u + 4) % 6] = lds16(L, aaddr(std::integral_constant<int, u + 4>{}));
            ar[(u + 5) % 6] = lds16(L, aaddr(std::integral_constant<int, u + 5>{}));
          }
          if constexpr (u >= 96 && s == 0) { const f32x16 z = {}; acc[T % 3] = mfma8(ag[T % 3], augx, z); }
          if constexpr (u == 64) { const f32x16 z = {}; acc[2] = mfma16(ar[u % 6], x[s], z); }
          else acc[T % 3] = mfma16(ar[u % 6], x[s], acc[T % 3]);
          if constexpr (left > 6 || left <= 4) ar[u % 6] = lds16(L, aaddr(std::integral_constant<int, u + 6>{}));
          if constexpr (u < 64 && (u & 1)) STATS_STEP(x[s]);
          if constexpr (u == 64) {
            const Stats st = make_stats(s1a + s1b, s2a + s2b, h);
            cg = st.rstd * -LOG2E; cu = st.rstd; augx = st.aug;
          }
          if constexpr (u == 66) acc[0] = mfma8(ag[0], augx, acc[0]);
          if constexpr (u == 67) acc[1] = mfma8(ag[1], augx, acc[1]);
          if constexpr (u == 68) { acc[2] = mfma8(ag[2], augx, acc[2]); ag[0] = ld8p(rs, r * 8 + 768, augb); }
          if constexpr (u >= 70 && u < 94 && (u - 70) % 3 != 2) {
            constexpr int q = (u - 70) / 3 * 2 + (u - 70) % 3;
            epi(std::integral_constant<int, q / 8>{}, std::integral_constant<int, q % 8>{});
          }
          if constexpr (u >= 96) {
            if constexpr (s == 1 && T < 6) ag[(T + 1) % 3] = ld8p(rs, r * 8 + ((T + 1) & 3) * 256, augb + ((T + 1) >> 2) * 1024);
            if constexpr (s >= 2 && s < 26 && (s - 2) % 3 == 0) epi(std::integral_constant<int, T - 1>{}, std::integral_constant<int, (s - 2) / 3>{});
            if constexpr (s == 27 && T == 5) { VMWAIT(1); set_flag(fh_mine, 4u, lane); }
            if constexpr (s == 27 && T == 6) { VMWAIT(1); set_flag(fh_mine, 5u, lane); fpre = poll_ld(FL + (FL_H + (layer * 64 + tb2) * 2 + kh) * 32 + (lane & 7)); }
          }
          SB();
        });
        sfor<0, 8>([&](auto E) { epi(std::integral_constant<int, 6>{}, E); });
        GAS unsigned* fh_poll = FL + (FL_H + (layer * 64 + tb2) * 2 + kh) * 32 + (lane & 7);
        const unsigned xfo = (tb2 * 32 + 4 * nblk + 2 * kh) * 1024u;
        if (!__all(fpre >= 4u)) wait_flags(fh_poll, 4u);
        const unsigned hb4 = OFF_H + layer * (7 * MB) + (tb2 * 112 + 56 * kh) * 1024u;
        v4u hr[16];
        sfor<0, 16>([&](auto I) { constexpr int q = I; hr[q] = ld16(rs, lane16, hb4 + (7 * (q & 7) + (q >> 3)) * 1024); });
        const unsigned xr1 = OFF_X1 + layer * (2 * MB) + xfo;
        const v4u r0 = ld16(rs, lane16, xr1), r1 = ld16(rs, lane16 + 1024, xr1);
        VMWAIT(18);
        set_flag(fh_mine, 7u, lane);
        f32x16 c0 = {}, c1 = {};
        const unsigned wb = kh * 16384 + lane16;
        SB();
        sfor<0, 112>([&](auto I) {
          constexpr int u = I, c = u / 16, v = u % 16, q = u >> 1;
          constexpr unsigned sl[7] = {R3, R0, R1, R2, R3, R0, R1};
          if constexpr (v == 12 && c < 6) {
            BAR();
            ar[(224 + u + 4) % 6] = lds16(L, sl[c + 1] + wb);
            ar[(224 + u + 5) % 6] = lds16(L, sl[c + 1] + wb + 1024);
          }
          if constexpr (v == 0 && (c == 1 || c == 2)) fpre = poll_ld(fh_poll);
          if constexpr (v == 0 && c == 2) { }
          if constexpr (u & 1) c1 = mfma16(ar[(224 + u) % 6], hr[q & 15], c1); else c0 = mfma16(ar[(224 + u) % 6], hr[q & 15], c0);
          if constexpr ((v < 10 || v >= 12) && u + 6 < 112) ar[(224 + u) % 6] = lds16(L, sl[(u + 6) / 16] + wb + ((u + 6) % 16) * 1024);
          if constexpr (u == 31) { if (!__all(fpre >= 5u)) wait_flags(fh_poll, 5u); }
          if constexpr (u == 47) { if (!__all(fpre >= 7u)) wait_flags(fh_poll, 7u); }
          if constexpr ((u & 1) && q + 16 < 56) hr[q & 15] = ld16(rs, lane16, hb4 + (7 * (q & 7) + (q >> 3) + 2) * 1024);
          SB();
        });
        const unsigned sown = R4S + cw * 4096 + lane16, spart = R4S + (cw ^ 1) * 4096 + lane16;
        const f32x16 give = kh ? c0 : c1;
        f32x16 keep = kh ? c1 : c0;
#pragma unroll
        for (int g = 0; g < 4; ++g) *(LAS f32x4*)(L + sown + g * 1024) = f32x4{give[4 * g], give[4 * g + 1], give[4 * g + 2], give[4 * g + 3]};
        BAR();
#pragma unroll
        for (int g = 0; g < 4; ++g) { const f32x4 t = *(LAS f32x4*)(L + spart + g * 1024); keep[4 * g] += t[0]; keep[4 * g + 1] += t[1]; keep[4 * g + 2] += t[2]; keep[4 * g + 3] += t[3]; }
        v4u y0, y1;
        y0.x = cvtpk(keep[0] + bflo(r0.x), keep[1] + bfhi(r0.x)); y0.y = cvtpk(keep[2] + bflo(r0.y), keep[3] + bfhi(r0.y));
        y0.z = cvtpk(keep[4] + bflo(r0.z), keep[5] + bfhi(r0.z)); y0.w = cvtpk(keep[6] + bflo(r0.w), keep[7] + bfhi(r0.w));
        y1.x = cvtpk(keep[8] + bflo(r1.x), keep[9] + bfhi(r1.x)); y1.y = cvtpk(keep[10] + bflo(r1.y), keep[11] + bfhi(r1.y));
        y1.z = cvtpk(keep[12] + bflo(r1.z), keep[13] + bfhi(r1.z)); y1.w = cvtpk(keep[14] + bflo(r1.w), keep[15] + bfhi(r1.w));
        if (layer == 0) {
          st16(y0, rs, lane16, OFF_X2 + xfo); st16(y1, rs, lane16 + 1024, OFF_X2 + xfo);
          VMWAIT(0);
          set_flag(FL + (FL_X + 64 + tb2) * 32 + 2 * nblk + kh, 1u, lane);
        } else {
          GAS v2u* op = (GAS v2u*)(a.out + (size_t)(tb2 * 32 + r) * DM + 64 * nblk + 32 * kh + 4 * h);
          op[0] = v2u{y0.x, y0.y}; op[2] = v2u{y0.z, y0.w}; op[4] = v2u{y1.x, y1.y}; op[6] = v2u{y1.z, y1.w};
        }
      }
    }
  }
}

struct PrepArgs { const u16 *x, *wqkv, *wo, *wg, *wu, *wd; unsigned char* ws; };
DI v4u gather8(const u16* p, int ld, int k0, int h) {
  unsigned e[8];
#pragma unroll
  for (int jj = 0; jj < 8; ++jj) e[jj] = p[(size_t)(k0 + 8 * (jj >> 2) + 4 * h + (jj & 3)) * ld];
  return v4u{e[0] | (e[1] << 16), e[2] | (e[3] << 16), e[4] | (e[5] << 16), e[6] | (e[7] << 16)};
}
constexpr int PB_X = 256, PB_QK = 128, PB_V = 64, PB_O = 64, PB_GU = 448, PB_D = 224, PB_CS = 80, PB_FL = 19;
__global__ void __launch_bounds__(512) prep_k(PrepArgs p) {
  __shared__ float red[8][64];
  int b = blockIdx.x; const int t = threadIdx.x;
  if (b < PB_CS) {
    const int c = b * 64 + (t & 63), g = t >> 6;
    const u16* src; int ld;
    if (c < 1024) { const int hd = c >> 5, rr = c & 31; src = p.wqkv + (rr < 16 ? 16 * hd + rr : 512 + 16 * hd + rr - 16); ld = 1536; }
    else if (c < 1536) { src = p.wqkv + 1024 + (c - 1024); ld = 1536; }
    else { const int cc = c - 1536, tl = cc >> 5, rr = cc & 31; src = (rr < 16 ? p.wg : p.wu) + 16 * tl + (rr & 15); ld = FF; }
    float s = 0.f;
#pragma unroll 16
    for (int k = 0; k < 64; ++k) s += __uint_as_float((unsigned)src[(size_t)(g * 64 + k) * ld] << 16);
    red[g][t & 63] = s;
    __syncthreads();
    if (t < 64) {
      float cs = 0.f;
      for (int i = 0; i < 8; ++i) cs += red[i][t];
      const unsigned hi = cvtpk(cs, cs), lo = cvtpk(cs - __uint_as_float(hi << 16), cs - __uint_as_float(hi << 16));
      *(v2u*)(p.ws + OFF_AUG + c * 8) = v2u{hi, lo};
    }
    return;
  }
  b -= PB_CS;
  if (b < PB_FL) { *(v4u*)(p.ws + OFF_FLAGS + (b * 512 + t) * 16) = v4u{0u, 0u, 0u, 0u}; return; }
  b -= PB_FL;
  const int lane = t & 63, r = lane & 31, h = lane >> 5;
  if (b < PB_X) {
    const int f = b * 8 + (t >> 6), tb = f >> 5, s = f & 31;
    const u16* src = p.x + (size_t)(tb * 32 + r) * DM + 16 * s + 4 * h;
    const v2u a = *(const v2u*)src, c = *(const v2u*)(src + 8);
    *(v4u*)(p.ws + OFF_X0 + f * 1024 + lane * 16) = v4u{a.x, a.y, c.x, c.y};
    return;
  }
  b -= PB_X;
  if (b < PB_QK) {
    const int f = b * 8 + (t >> 6), hd = f >> 5, s = f & 31;
    *(v4u*)(p.ws + OFF_WQK + f * 1024 + lane * 16) = gather8(p.wqkv + (r < 16 ? 16 * hd + r : 512 + 16 * hd + r - 16), 1536, 16 * s, h);
    return;
  }
  b -= PB_QK;
  if (b < PB_V) {
    const int c = b * 512 + t, hd = c >> 10, s = (c >> 5) & 31, l = c & 31;
    *(v4u*)(p.ws + OFF_WV + c * 16) = gather8(p.wqkv + 1024 + 16 * hd + (l & 15), 1536, 16 * s, l >> 4);
    return;
  }
  b -= PB_V;
  if (b < PB_O) {
    const int f = b * 8 + (t >> 6), tl = f >> 5, s = f & 31;
    *(v4u*)(p.ws + OFF_WO + f * 1024 + lane * 16) = gather8(p.wo + 32 * tl + r, DM, 16 * s, h);
    return;
  }
  b -= PB_O;
  if (b < PB_GU) {
    const int f = b * 8 + (t >> 6), tl = f >> 5, s = f & 31;
    *(v4u*)(p.ws + OFF_WGU + f * 1024 + lane * 16) = gather8((r < 16 ? p.wg : p.wu) + 16 * tl + (r & 15), FF, 16 * s, h);
    return;
  }
  b -= PB_GU;
  {
    const int f = b * 8 + (t >> 6);
    const int n = f / 224, rem = f % 224, c = rem >> 5, kh = (rem >> 4) & 1, i = (rem >> 1) & 7, tl = rem & 1;
    const int s = 7 * (8 * kh + i) + c;
    *(v4u*)(p.ws + OFF_WD + f * 1024 + lane * 16) = gather8(p.wd + 64 * n + 32 * tl + r, DM, 16 * s, h);
  }
}

extern "C" void kernel_launch(void* const* d_in, const int* in_sizes, int n_in, void* d_out, int out_size, void* d_ws, size_t ws_size,
                              hipStream_t stream) {
  PrepArgs p;
  p.x = (const u16*)d_in[0]; p.wqkv = (const u16*)d_in[1]; p.wo = (const u16*)d_in[2];
  p.wg = (const u16*)d_in[4]; p.wu = (const u16*)d_in[5]; p.wd = (const u16*)d_in[6]; p.ws = (unsigned char*)d_ws;
  prep_k<<<PB_CS + PB_FL + PB_X + PB_QK + PB_V + PB_O + PB_GU + PB_D, 512, 0, stream>>>(p);
  Args a; a.ws = (unsigned char*)d_ws; a.out = (u16*)d_out;
  (void)hipFuncSetAttribute((const void*)main_k, hipFuncAttributeMaxDynamicSharedMemorySize, LDS_BYTES);
  main_k<<<256, 512, LDS_BYTES, stream>>>(a);
}
```

```cpp
#include <hip/hip_runtime.h>
#include <stdint.h>
#include <type_traits>

typedef unsigned short u16;
typedef float f32x16 __attribute__((ext_vector_type(16)));
typedef float f32x4 __attribute__((ext_vector_type(4)));
typedef float f32x2 __attribute__((ext_vector_type(2)));
typedef __bf16 bf16x8 __attribute__((ext_vector_type(8)));
typedef __bf16 bf16x2 __attribute__((ext_vector_type(2)));
typedef short s16x4 __attribute__((ext_vector_type(4)));
typedef unsigned v4u __attribute__((ext_vector_type(4)));
typedef unsigned v2u __attribute__((ext_vector_type(2)));
#define GAS __attribute__((address_space(1)))
#define LAS __attribute__((address_space(3)))
#define DI __device__ __forceinline__
#define RLX_AGENT __ATOMIC_RELAXED, __HIP_MEMORY_SCOPE_AGENT
#define IC(n) std::integral_constant<int, (n)>{}
typedef __amdgpu_buffer_rsrc_t rsrc_t;

constexpr int SEQ = 2048, DM = 512, FF = 1792;
constexpr unsigned KB = 1024u, MB = 1u << 20;
constexpr unsigned OFF_FLAGS = 0;
constexpr unsigned FL_X = 0;
constexpr unsigned FL_O = FL_X + 4 * 64;
constexpr unsigned FL_H = FL_O + 2 * 64 * 2;
constexpr unsigned FL_KV = FL_H + 2 * 64 * 2;
constexpr unsigned FL_LINES = FL_KV + 2 * 32 * 8;
constexpr unsigned OFF_AUG = 256 * KB;
constexpr unsigned AUG_QK = 0, AUG_V = 1024 * 16, AUG_GU = 1536 * 16;
constexpr unsigned OFF_WQK = 1 * MB, OFF_WV = 2 * MB, OFF_WO = 2 * MB + 512 * KB, OFF_WGU = 3 * MB, OFF_WD = 6 * MB + 512 * KB;
constexpr unsigned OFF_X0 = 9 * MB, OFF_X1 = 11 * MB, OFF_X2 = 15 * MB, OFF_O = 17 * MB, OFF_KV = 21 * MB, OFF_H = 29 * MB;
constexpr unsigned OFF_ST = 43 * MB;
constexpr unsigned WS_BYTES = 44 * MB;
constexpr unsigned OOB = 0x40000000u;
constexpr unsigned R0 = 0, R1 = 32 * KB, R2 = 64 * KB, R3 = 96 * KB, R4V = 128 * KB, R4S = 144 * KB, LDS_BYTES = 160 * KB;
constexpr float NEG_BIG = -1.0e30f;
constexpr float LOG2E = 1.4426950408889634f;
constexpr float SAFE2 = 2500.0f;
constexpr unsigned SPIN_CAP = 1u << 18;
#define D1V 5
#define D2V 8
#define D3V 8
#define D4V 10
#define ROT 1
constexpr int D1 = D1V, D2 = D2V, D3 = D3V, D4 = D4V;

template <int B, int E, class F> DI void sfor(F&& f) {
  if constexpr (B < E) { f(std::integral_constant<int, B>{}); sfor<B + 1, E>(f); }
}
DI bf16x8 as_bf8(v4u v) { return __builtin_bit_cast(bf16x8, v); }
DI bf16x2 as_bf2(unsigned v) { return __builtin_bit_cast(bf16x2, v); }
DI s16x4 as_s4(v2u v) { return __builtin_bit_cast(s16x4, v); }
DI f32x16 mfma16(v4u a, v4u b, f32x16 c) { return __builtin_amdgcn_mfma_f32_32x32x16_bf16(as_bf8(a), as_bf8(b), c, 0, 0, 0); }
DI f32x16 mfma8(v2u a, v2u b, f32x16 c) { return __builtin_amdgcn_mfma_f32_32x32x8bf16_1k(as_s4(a), as_s4(b), c, 0, 0, 0); }
DI unsigned cvtpk(float lo, float hi) { f32x2 f = {lo, hi}; bf16x2 p = __builtin_convertvector(f, bf16x2); return __builtin_bit_cast(unsigned, p); }
DI float bflo(unsigned v) { return __uint_as_float(v << 16); }
DI float bfhi(unsigned v) { return __uint_as_float(v & 0xffff0000u); }
DI float dot2(unsigned a, unsigned b, float c) { return __builtin_amdgcn_fdot2_f32_bf16(as_bf2(a), as_bf2(b), c, false); }
DI float ex2(float v) { return __builtin_amdgcn_exp2f(v); }
DI float xhalf_add(float v) { auto r = __builtin_amdgcn_permlane32_swap(__float_as_uint(v), __float_as_uint(v), false, false); return __uint_as_float(r[0]) + __uint_as_float(r[1]); }
DI float xhalf_max(float v) { auto r = __builtin_amdgcn_permlane32_swap(__float_as_uint(v), __float_as_uint(v), false, false); return fmaxf(__uint_as_float(r[0]), __uint_as_float(r[1])); }
DI unsigned umax(unsigned a, unsigned b) { return a > b ? a : b; }
DI unsigned row_umax(unsigned v) {
  v = umax(v, (unsigned)__builtin_amdgcn_update_dpp(0, (int)v, 0x121, 0xf, 0xf, true));
  v = umax(v, (unsigned)__builtin_amdgcn_update_dpp(0, (int)v, 0x122, 0xf, 0xf, true));
  v = umax(v, (unsigned)__builtin_amdgcn_update_dpp(0, (int)v, 0x124, 0xf, 0xf, true));
  v = umax(v, (unsigned)__builtin_amdgcn_update_dpp(0, (int)v, 0x128, 0xf, 0xf, true));
  return v;
}
DI v4u ld16(rsrc_t rs, unsigned voff, unsigned soff) { return __builtin_amdgcn_raw_buffer_load_b128(rs, voff, soff, 16); }
DI v4u ld16p(rsrc_t rs, unsigned voff, unsigned soff) { return __builtin_amdgcn_raw_buffer_load_b128(rs, voff, soff, 0); }
DI v2u ld8(rsrc_t rs, unsigned voff, unsigned soff) { return __builtin_bit_cast(v2u, __builtin_amdgcn_raw_buffer_load_b64(rs, voff, soff, 16)); }
DI void st16(v4u v, rsrc_t rs, unsigned voff, unsigned soff) { __builtin_amdgcn_raw_buffer_store_b128(v, rs, voff, soff, 16); }
DI void st8(v2u v, rsrc_t rs, unsigned voff, unsigned soff) { __builtin_amdgcn_raw_buffer_store_b64(v, rs, voff, soff, 16); }
DI v4u lds16(LAS unsigned char* L, unsigned off) { return *(LAS v4u*)(L + off); }
#define SB() __builtin_amdgcn_sched_barrier(0)
#define OPQV(x) asm volatile("" : "+v"(x))
#define OPQS(x) asm volatile("" : "+s"(x))
#define LANE_CONSTS() int lane = threadIdx.x & 63; OPQV(lane); const int r = lane & 31, h = lane >> 5; const unsigned lane16 = lane * 16; (void)r; (void)h
#define BAR() asm volatile("s_waitcnt lgkmcnt(0)\n\ts_barrier" ::: "memory")
#define VMWAIT(n) asm volatile("s_waitcnt vmcnt(" #n ")" ::: "memory")
#define DMA_HEAD "s_nop 4\n\ts_mov_b32 m0, %0\n\ts_nop 0\n\t"
#define DMA_LD(o) "buffer_load_dwordx4 %1, %2, %3 offen offset:" #o " lds\n\t"
#define DMA_ARGS :: "s"(lds), "v"(voff), "s"(rs), "s"(soff) : "memory"
DI void dma4(rsrc_t rs, unsigned lds, unsigned soff, unsigned voff) { asm volatile(DMA_HEAD DMA_LD(0) DMA_LD(1024) DMA_LD(2048) DMA_LD(3072) DMA_ARGS); }
DI void dma2(rsrc_t rs, unsigned lds, unsigned soff, unsigned voff) { asm volatile(DMA_HEAD DMA_LD(0) DMA_LD(1024) DMA_ARGS); }
DI void dma1(rsrc_t rs, unsigned lds, unsigned soff, unsigned voff) { asm volatile(DMA_HEAD DMA_LD(0) DMA_ARGS); }
#define PACE 3
#define STR2(x) #x
#define STR(x) STR2(x)
#define DMA_W "s_waitcnt vmcnt(" STR(PACE) ")\n\t"
DI void dma4_paced(rsrc_t rs, unsigned lds, unsigned soff, unsigned voff) {
  asm volatile(DMA_HEAD DMA_LD(0) DMA_W DMA_LD(1024) DMA_W DMA_LD(2048) DMA_W DMA_LD(3072) DMA_W DMA_ARGS);
}
DI void dma4_sc1(rsrc_t rs, unsigned lds, unsigned soff, unsigned voff) {
  asm volatile(DMA_HEAD "buffer_load_dwordx4 %1, %2, %3 offen sc1 lds\n\tbuffer_load_dwordx4 %1, %2, %3 offen offset:1024 sc1 lds\n\t"
               "buffer_load_dwordx4 %1, %2, %3 offen offset:2048 sc1 lds\n\tbuffer_load_dwordx4 %1, %2, %3 offen offset:3072 sc1 lds" DMA_ARGS);
}
DI void dma_tile(rsrc_t rs, unsigned lds, unsigned soff, unsigned lw, unsigned o0, unsigned o1, unsigned lane16) {
  dma4(rs, lds + lw * 8192u, soff + o0, lane16);
  dma4(rs, lds + lw * 8192u + 4096u, soff + o1, lane16);
}
DI void dma_tile_paced(rsrc_t rs, unsigned lds, unsigned soff, unsigned lw, unsigned o0, unsigned o1, unsigned lane16) {
  dma4_paced(rs, lds + lw * 8192u, soff + o0, lane16);
  dma4_paced(rs, lds + lw * 8192u + 4096u, soff + o1, lane16);
}
DI unsigned poll_ld(rsrc_t rs, unsigned line, int lane, int n) {
  int z = 0; OPQV(z);
  return (unsigned)__builtin_amdgcn_raw_ptr_buffer_atomic_add_i32(z, rs, lane < n ? lane * 4 : OOB, line, 0);
}
DI bool flags_ok(unsigned f, unsigned target, int lane, int n) { return __all(f >= target || lane >= n); }
DI unsigned wait_flags(rsrc_t rs, unsigned line, unsigned target, int lane, int n) {
  unsigned f = 0;
#pragma nounroll
  for (unsigned spins = 0; spins < SPIN_CAP; ++spins) {
    f = poll_ld(rs, line, lane, n);
    if (flags_ok(f, target, lane, n)) break;
  }
  asm volatile("" ::: "memory");
  return f;
}
DI void set_flag(rsrc_t rs, unsigned off, unsigned v, int lane) {
  const unsigned vo = lane == 0 ? 0u : OOB;
  asm volatile("s_nop 4\n\tbuffer_atomic_swap %0, %1, %2, %3 offen" :: "v"(v), "v"(vo), "s"(rs), "s"(off) : "memory");
}

struct Stats { float rstd; v2u aug; };
DI Stats make_stats(const v2u (&sp)[8], int h) {
  float s1 = 0.f, s2 = 0.f;
#pragma unroll
  for (int i = 0; i < 8; ++i) { const unsigned a = sp[i].x, b = sp[i].y; s1 += __uint_as_float(a); s2 += __uint_as_float(b); }
  s1 = xhalf_add(s1); s2 = xhalf_add(s2);
  const float mean = s1 * (1.0f / DM);
  const float var = fmaxf(s2 * (1.0f / DM) - mean * mean, 0.0f);
  Stats st; st.rstd = __builtin_amdgcn_rsqf(var + 1e-5f);
  const float nm = h ? 0.0f : -mean;
  const unsigned hi = cvtpk(nm, nm) & 0xffffu;
  const float lo = nm - __uint_as_float(hi << 16);
  const unsigned w = hi | (cvtpk(lo, lo) << 16);
  st.aug.x = w; st.aug.y = w;
  return st;
}
DI v2u make_aug(v4u p) {
  const unsigned a = p.x, b = p.y, c = p.z, d = p.w;
  const float cs = (__uint_as_float(a) + __uint_as_float(b)) + (__uint_as_float(c) + __uint_as_float(d));
  const unsigned hi = cvtpk(cs, cs);
  const float lo = cs - __uint_as_float(hi << 16);
  return v2u{hi, cvtpk(lo, lo)};
}

DI float max16(const f32x16& s, float m) {
  m = fmaxf(fmaxf(m, s[0]), s[1]);   m = fmaxf(fmaxf(m, s[2]), s[3]);
  float n = fmaxf(s[4], s[5]);       n = fmaxf(fmaxf(n, s[6]), s[7]);
  m = fmaxf(fmaxf(m, s[8]), s[9]);   n = fmaxf(fmaxf(n, s[10]), s[11]);
  m = fmaxf(fmaxf(m, s[12]), s[13]); n = fmaxf(fmaxf(n, s[14]), s[15]);
  return fmaxf(m, n);
}
DI void exp_pv(const f32x16& s, v4u v0, v4u v1, f32x16& o) {
  v4u p0, p1;
  p0.x = cvtpk(ex2(s[0]), ex2(s[1])); p0.y = cvtpk(ex2(s[2]), ex2(s[3])); p0.z = cvtpk(ex2(s[4]), ex2(s[5])); p0.w = cvtpk(ex2(s[6]), ex2(s[7]));
  o = mfma16(v0, p0, o);
  p1.x = cvtpk(ex2(s[8]), ex2(s[9])); p1.y = cvtpk(ex2(s[10]), ex2(s[11])); p1.z = cvtpk(ex2(s[12]), ex2(s[13])); p1.w = cvtpk(ex2(s[14]), ex2(s[15]));
  o = mfma16(v1, p1, o);
}
DI void attn_general(LAS unsigned char* L, v4u q, const f32x16& cm, int bm, int bf0, int nf, unsigned kaddr, unsigned vbase, unsigned vstep,
                     float& m, f32x16& o) {
  const f32x16 zero = {};
  float mx = max16(mfma16(lds16(L, kaddr + bm * 2048), q, cm), m);
#pragma nounroll
  for (int b = 0; b < nf; ++b) mx = max16(mfma16(lds16(L, kaddr + (bf0 + b) * 2048), q, zero), mx);
  mx = xhalf_max(mx);
  const float f = ex2(m - mx);
#pragma unroll
  for (int i = 0; i < 16; ++i) o[i] *= f;
  m = mx;
  f32x16 c;
#pragma unroll
  for (int i = 0; i < 16; ++i) c[i] = cm[i] - mx;
  unsigned va = vbase + vstep * bm;
  exp_pv(mfma16(lds16(L, kaddr + bm * 2048), q, c), lds16(L, va), lds16(L, va + 512), o);
#pragma unroll
  for (int i = 0; i < 16; ++i) c[i] = -mx;
#pragma nounroll
  for (int b = 0; b < nf; ++b) {
    va = vbase + vstep * (bf0 + b);
    exp_pv(mfma16(lds16(L, kaddr + (bf0 + b) * 2048), q, c), lds16(L, va), lds16(L, va + 512), o);
  }
}
template <int K, int S0, int DIR, class H>
DI void attn_chain(LAS unsigned char* L, v4u q, int n, unsigned kaddr, unsigned va, unsigned vstep, const f32x16& s, v4u v0, v4u v1, v4u kn,
                   f32x16& o, H&& hook) {
  if constexpr (K == 7) { hook(IC(K)); exp_pv(s, v0, v1, o); }
  else {
    const f32x16 zero = {};
    f32x16 sn = mfma16(kn, q, zero);
    v4u w0 = lds16(L, va), w1 = lds16(L, va + 512);
    if constexpr (K < 6) kn = lds16(L, kaddr + (S0 + DIR * (K + 1)) * 2048);
    hook(IC(K));
    SB();
    exp_pv(s, v0, v1, o);
    asm volatile("" : "+v"(sn), "+v"(w0), "+v"(w1), "+v"(kn), "+v"(o));
    if (n > K) attn_chain<K + 1, S0, DIR>(L, q, n, kaddr, DIR > 0 ? va + vstep : va - vstep, vstep, sn, w0, w1, kn, o, hook);
  }
}
template <int S0, int DIR, class H>
DI void attn_bounded(LAS unsigned char* L, v4u q, const f32x16& cm, int bm, int n, unsigned kaddr, unsigned vbase, unsigned vstep, f32x16& o, H&& hook) {
  const unsigned vm = vbase + vstep * bm;
  const v4u km = lds16(L, kaddr + bm * 2048), kn = lds16(L, kaddr + S0 * 2048), v0 = lds16(L, vm), v1 = lds16(L, vm + 512);
  attn_chain<0, S0, DIR>(L, q, n, kaddr, vbase + vstep * S0, vstep, mfma16(km, q, cm), v0, v1, kn, o, hook);
}
template <int S0, class H>
DI void attn_full(LAS unsigned char* L, v4u q, int n, unsigned kaddr, unsigned vbase, unsigned vstep, f32x16& o, H&& hook) {
  const f32x16 zero = {};
  const unsigned va = vbase + vstep * S0;
  const v4u k0 = lds16(L, kaddr + S0 * 2048), kn = lds16(L, kaddr + (S0 + 1) * 2048), v0 = lds16(L, va), v1 = lds16(L, va + 512);
  attn_chain<1, S0, 1>(L, q, n, kaddr, va + vstep, vstep, mfma16(k0, q, zero), v0, v1, kn, o, hook);
}

DI f32x16 unpack_tile(v4u r0, v4u r1) {
  f32x16 k;
  k[0] = bflo(r0.x); k[1] = bfhi(r0.x); k[2] = bflo(r0.y); k[3] = bfhi(r0.y); k[4] = bflo(r0.z); k[5] = bfhi(r0.z); k[6] = bflo(r0.w); k[7] = bfhi(r0.w);
  k[8] = bflo(r1.x); k[9] = bfhi(r1.x); k[10] = bflo(r1.y); k[11] = bfhi(r1.y); k[12] = bflo(r1.z); k[13] = bfhi(r1.z); k[14] = bflo(r1.w); k[15] = bfhi(r1.w);
  return k;
}
DI void pack_tile(const f32x16& k, v4u& y0, v4u& y1) {
  y0.x = cvtpk(k[0], k[1]); y0.y = cvtpk(k[2], k[3]); y0.z = cvtpk(k[4], k[5]); y0.w = cvtpk(k[6], k[7]);
  y1.x = cvtpk(k[8], k[9]); y1.y = cvtpk(k[10], k[11]); y1.z = cvtpk(k[12], k[13]); y1.w = cvtpk(k[14], k[15]);
}
DI v2u tile_stats(const f32x16& k) {
  f32x2 a = {k[0], k[1]}, b = a * a;
#pragma unroll
  for (int i = 1; i < 8; ++i) { const f32x2 t = {k[2 * i], k[2 * i + 1]}; a += t; b += t * t; }
  return v2u{__float_as_uint(xhalf_add(a[0] + a[1])), __float_as_uint(xhalf_add(b[0] + b[1]))};
}

struct Args { unsigned char* ws; u16* out; };
#define STAMP(id) do {} while (0)
#define STAMP_DECL() do {} while (0)
#define STAMP_OUT() do {} while (0)

__global__ void __launch_bounds__(512, 2) main_k(Args a) {
  extern __shared__ __attribute__((aligned(16))) unsigned char smem[];
  LAS unsigned char* L = (LAS unsigned char*)smem;
  const unsigned lb = (unsigned)(size_t)L;
  const int w0 = __builtin_amdgcn_readfirstlane(threadIdx.x >> 6);
  STAMP_DECL();
  const int xcd = blockIdx.x & 7, idx = blockIdx.x >> 3;
  const int sg0 = (xcd & ~1) | (idx >> 4), i4 = idx & 15, j0 = ((i4 >> 2) << 3) | ((xcd & 1) << 2) | (i4 & 3);
  const rsrc_t rs = __builtin_amdgcn_make_buffer_rsrc(a.ws, 0, WS_BYTES, 0x00020000);
#define ROLES() int w = w0, sg = sg0, j = j0, layer = layer0; OPQS(w); OPQS(sg); OPQS(j); OPQS(layer); \
  const int head = j, tbl = w < 4 ? w : 11 - w, Tq = 8 * sg + tbl;              \
  const int grp = 2 * sg + (j >> 4), mem = j & 15, nblk = j & 7;                \
  const int cw = w & 3, th = cw >> 1, kh = cw & 1; \
  const int tb2 = 4 * grp + 2 * ((j >> 3) & 1) + th, tb3 = 4 * grp + cw;        \
  const unsigned rg = ROT * ((j & 3) | ((j >> 1) & 4)), rn = ROT * (j & 3);     \
  (void)head; (void)Tq; (void)mem; (void)nblk; (void)kh; (void)tb2; (void)tb3; (void)rg; (void)rn
  {
    const int layer0 = 0;
    STAMP(31);
    ROLES(); LANE_CONSTS();
    dma4(rs, lb + R0 + w * 4096u, OFF_WQK + head * 32768u + ((w + rg) & 7) * 4096u, lane16);
    dma2(rs, lb + R4V + w * 2048u, OFF_WV + head * 16384u + ((w + rg) & 7) * 2048u, lane16);
  }

  for (int layer0 = 0; layer0 < 2; ++layer0) {
    {
      ROLES(); LANE_CONSTS();
      STAMP(0);
      const unsigned xb = (layer ? OFF_X2 : OFF_X0) + Tq * 32768u;
      const v4u pqk = ld16p(rs, r * 16, OFF_AUG + AUG_QK + head * 512u);
      const v4u pv = ld16p(rs, (lane & 15) * 16, OFF_AUG + AUG_V + head * 256u);
      if (layer) wait_flags(rs, (FL_X + 128 + Tq) * 128u, 1u, lane, 16);
      STAMP(1);
      auto xload = [&](auto I) -> v4u { constexpr int i = I; return ld16(rs, lane16 + (i & 3) * 1024, xb + (((i >> 2) + rg) & 7) * 4096u); };
      v4u xr[D1];
      sfor<0, D1>([&](auto I) { constexpr int i = I; xr[i] = xload(I); });
      v2u sp[8];
      asm volatile("s_waitcnt vmcnt(%0)" :: "i"(D1) : "memory");
      BAR();
      STAMP(2);
      if (w == 0 && lane < 2) { unsigned o2 = 0x3f803f80u; OPQV(o2); *(LAS v4u*)(L + R4S + lane * 512) = v4u{o2, o2, o2, o2}; }
      unsigned vwa = R4V + (h * 16 + (lane & 15)) * 16; OPQV(vwa);
      const unsigned own = R1 + (8 + tbl) * 2048;
      v4u wq[2], wv[2];
      wq[0] = lds16(L, R0 + lane16); wv[0] = lds16(L, vwa);
      wq[1] = lds16(L, R0 + lane16 + 1024); wv[1] = lds16(L, vwa + 512);
      f32x16 cqk = {}, cv = {};
      Stats st; v2u aqk, av;
      f32x16 cm;
      f32x4 rr[4];
      SB();
      sfor<0, 32>([&](auto I) {
        constexpr int s = I;
        const v4u xf = xr[s % D1];
        cqk = mfma16(wq[s & 1], xf, cqk); OPQV(cqk);
        cv = mfma16(xf, wv[s & 1], cv); OPQV(cv);
        if constexpr (s + 2 < 32) { wq[s & 1] = lds16(L, R0 + lane16 + (s + 2) * 1024); wv[s & 1] = lds16(L, vwa + (s + 2) * 512); }
        if constexpr (s + D1 < 32) xr[s % D1] = xload(IC(s + D1));
        if constexpr (s >= 4 && s < 12) sp[s - 4] = ld8(rs, h * 2048 + r * 8 + (s - 4) * 256, OFF_ST + (layer * 128 + Tq) * 4096u);
        if constexpr (s == 2) { aqk = make_aug(pqk); av = make_aug(pv); OPQV(aqk); OPQV(av); }
        if constexpr (s == 20) { st = make_stats(sp, h); OPQV(st.aug); *(LAS float*)(L + own + 1024 + r * 4) = st.rstd; }
        if constexpr (s == 22) {
          cqk = mfma8(aqk, st.aug, cqk); cv = mfma8(st.aug, av, cv); OPQV(cqk); OPQV(cv);
#pragma unroll
          for (int g = 0; g < 4; ++g) rr[g] = *(LAS f32x4*)(L + own + 1024 + (8 * g + 4 * h) * 4);
        }
        if constexpr (s >= 23 && s < 31) {
          constexpr int i = 2 * (s - 23);
          float a0 = ((i & 3) + 8 * (i >> 2) + 4 * h <= r) ? 0.0f : NEG_BIG, a1 = (((i + 1) & 3) + 8 * (i >> 2) + 4 * h <= r) ? 0.0f : NEG_BIG;
          OPQV(a0); OPQV(a1); cm[i] = a0; cm[i + 1] = a1;
        }
        SB();
      });
      STAMP(3);
      const bool up = (lane & 16) != 0;
      const unsigned voff = 1024 + ((lane >> 4) & 1) * 512 + (h * 16 + (lane & 15)) * 16;
      const unsigned kvb = OFF_KV + layer * (4 * MB) + (head * 64 + Tq) * 2048u;
      v4u qf, kf, vf0, vf1;
      float q2 = 0.f, k2 = 0.f;
#pragma unroll
      for (int i = 0; i < 8; ++i) cqk[8 + i] *= st.rstd;
      kf.x = cvtpk(cqk[8], cqk[9]); kf.y = cvtpk(cqk[10], cqk[11]); kf.z = cvtpk(cqk[12], cqk[13]); kf.w = cvtpk(cqk[14], cqk[15]);
      st16(kf, rs, sg < 7 ? lane16 : OOB, kvb);
#pragma unroll
      for (int i = 0; i < 16; ++i) cv[i] *= rr[i >> 2][i & 3];
      pack_tile(cv, vf0, vf1);
      const v4u vf = up ? vf1 : vf0;
      st16(vf, rs, sg < 7 ? voff : OOB, kvb);
      SB();
      *(LAS v4u*)(L + own + lane16) = kf;
      *(LAS v4u*)(L + own + voff) = vf;
      const float qs = st.rstd * (0.25f * LOG2E);
#pragma unroll
      for (int i = 0; i < 8; ++i) { cqk[i] *= qs; q2 += cqk[i] * cqk[i]; k2 += cqk[8 + i] * cqk[8 + i]; }
      qf.x = cvtpk(cqk[0], cqk[1]); qf.y = cvtpk(cqk[2], cqk[3]); qf.z = cvtpk(cqk[4], cqk[5]); qf.w = cvtpk(cqk[6], cqk[7]);
      const f32x16 sd = mfma16(kf, qf, cm);
      q2 = xhalf_add(q2) * 1.02f;
      const unsigned kr = row_umax(__float_as_uint(xhalf_add(k2) * 1.02f + 1e-30f));
      const unsigned kmaxb = umax(__builtin_amdgcn_readlane(kr, 0), __builtin_amdgcn_readlane(kr, 16));
      *(LAS unsigned*)(L + R4S + 1024 + (8 + tbl) * 4) = kmaxb;
      const unsigned fkv = (FL_KV + (layer * 32 + head) * 8 + sg) * 128u;
      f32x16 o = {};
      bool ok = __all(q2 * __uint_as_float(kmaxb) <= SAFE2);
      {
        unsigned one2 = 0x3f803f80u; OPQV(one2);
        const v4u ones = {one2, one2, one2, one2};
        exp_pv(sd, up ? ones : vf0, up ? ones : vf1, o);
      }
      if (w < 4) {
#pragma unroll
        for (int i = 0; i < 4; ++i) dma4(rs, lb + R2 + w * 16384u + i * 4096u, OFF_WO + nblk * 65536u + w * 16384u + ((i + rn) & 3) * 4096u, lane16);
        VMWAIT(16);
        if (sg < 7) set_flag(rs, fkv + tbl * 4, kmaxb, lane);
      }
      STAMP(4);
      BAR();
      STAMP(5);
      unsigned kaddr = R1 + lane16; OPQV(kaddr);
      const unsigned vstep = up ? 0u : 2048u, vbase = up ? R4S : R1 + 1024 + (h * 16 + (lane & 15)) * 16;
      ok = ok && __all(q2 * __uint_as_float(row_umax(*(LAS unsigned*)(L + R4S + 1024 + (8 + (lane & 7)) * 4))) <= SAFE2);
      if (ok && tbl > 0)
        attn_full<8>(L, qf, tbl, kaddr, vbase, vstep, o, [&](auto K) { if (K == 2 && w >= 4 && sg < 7) { VMWAIT(0); set_flag(rs, fkv + tbl * 4, kmaxb, lane); } });
      if (!ok && w >= 4 && sg < 7) { VMWAIT(0); set_flag(rs, fkv + tbl * 4, kmaxb, lane); }
      STAMP(6);
      if (w < 4) {
        if (sg > 0) {
          const unsigned f = wait_flags(rs, fkv - 128 + 8 * w, 1u, lane, 2);
          dma4_sc1(rs, lb + R1 + w * 4096u, OFF_KV + layer * (4 * MB) + (head * 64 + 8 * (sg - 1) + 2 * w) * 2048u, lane16);
          if (lane < 2) *(LAS unsigned*)(L + R4S + 1024 + (2 * w + lane) * 4) = f;
        }
        STAMP(7);
        VMWAIT(0);
      }
      STAMP(8);
      BAR();
      STAMP(9);
      if (sg > 0) ok = ok && __all(q2 * __uint_as_float(row_umax(*(LAS unsigned*)(L + R4S + 1024 + (lane & 7) * 4))) <= SAFE2);
      if (ok) {
        if (sg > 0) {
#pragma unroll
          for (int i = 0; i < 16; ++i) cm[i] = NEG_BIG - cm[i];
          attn_bounded<7, -1>(L, qf, cm, tbl, 7 - tbl, kaddr, vbase, vstep, o, [&](auto) {});
        }
      } else {
        float m = NEG_BIG;
#pragma unroll
        for (int i = 0; i < 16; ++i) o[i] = 0.f;
        attn_general(L, qf, cm, 8 + tbl, 8, tbl, kaddr, vbase, vstep, m, o);
        if (sg > 0) {
#pragma unroll
          for (int i = 0; i < 16; ++i) cm[i] = NEG_BIG - cm[i];
          attn_general(L, qf, cm, tbl, tbl + 1, 7 - tbl, kaddr, vbase, vstep, m, o);
        }
      }
      STAMP(10);
      const float il = __builtin_amdgcn_rcpf(o[8]);
      v4u of;
      of.x = cvtpk(o[0] * il, o[1] * il); of.y = cvtpk(o[2] * il, o[3] * il); of.z = cvtpk(o[4] * il, o[5] * il); of.w = cvtpk(o[6] * il, o[7] * il);
      st16(of, rs, lane16, OFF_O + layer * (2 * MB) + (Tq * 32 + head) * 1024u);
      if (w >= 4) {
        dma4(rs, lb + R0 + (w - 4) * 8192u, OFF_WGU + mem * 7 * 32768u + ((2 * (w - 4) + rg) & 7) * 4096u, lane16);
        VMWAIT(4);
      } else VMWAIT(0);
      set_flag(rs, (FL_O + (layer * 64 + Tq) * 2 + (head >> 4)) * 128u + (head & 15) * 4, 1u, lane);
      STAMP(11);
    }
    BAR();
    STAMP(12);
    if (w0 >= 4) {
      ROLES(); LANE_CONSTS();
      const unsigned lw = w - 4;
      const unsigned gu = OFF_WGU + mem * 7 * 32768u, wd = OFF_WD + nblk * 7 * 32768u;
      const unsigned g0 = ((2 * lw + rg) & 7) * 4096u, g1 = ((2 * lw + 1 + rg) & 7) * 4096u;
      const unsigned d0 = (lw >> 1) * 16384u + ((2 * lw + rn) & 3) * 4096u, d1 = (lw >> 1) * 16384u + ((2 * lw + 1 + rn) & 3) * 4096u;
      dma4_paced(rs, lb + R0 + lw * 8192u + 4096u, gu + g1, lane16);
      dma4_paced(rs, lb + R1 + lw * 8192u, gu + 32768u + g0, lane16);
      STAMP(14); BAR();
      dma4_paced(rs, lb + R1 + lw * 8192u + 4096u, gu + 32768u + g1, lane16);
      dma_tile_paced(rs, lb + R2, gu + 2 * 32768u, lw, g0, g1, lane16);
      STAMP(17); BAR();
      dma_tile_paced(rs, lb + R3, gu + 3 * 32768u, lw, g0, g1, lane16);
      STAMP(18); BAR();
      dma_tile(rs, lb + R0, gu + 4 * 32768u, lw, g0, g1, lane16);
      dma_tile(rs, lb + R1, gu + 5 * 32768u, lw, g0, g1, lane16);
      VMWAIT(16); STAMP(19); BAR();
      dma_tile(rs, lb + R2, gu + 6 * 32768u, lw, g0, g1, lane16);
      VMWAIT(16); STAMP(20); BAR();
      dma_tile(rs, lb + R3, wd + 0 * 32768u, lw, d0, d1, lane16);
      VMWAIT(16); BAR();
      dma_tile(rs, lb + R0, wd + 1 * 32768u, lw, d0, d1, lane16);
      VMWAIT(16); STAMP(21); BAR();
      dma_tile(rs, lb + R1, wd + 2 * 32768u, lw, d0, d1, lane16);
      VMWAIT(16); STAMP(22); BAR();
      dma_tile_paced(rs, lb + R2, wd + 3 * 32768u, lw, d0, d1, lane16);
      STAMP(25); BAR();
      dma_tile(rs, lb + R3, wd + 4 * 32768u, lw, d0, d1, lane16);
      BAR();
      dma_tile(rs, lb + R0, wd + 5 * 32768u, lw, d0, d1, lane16);
      VMWAIT(16); STAMP(26); BAR();
      dma_tile(rs, lb + R1, wd + 6 * 32768u, lw, d0, d1, lane16);
      VMWAIT(16); BAR();
      if (layer == 0) {
        dma2(rs, lb + R4V + lw * 4096u, OFF_WV + head * 16384u + (g0 >> 1), lane16);
        dma2(rs, lb + R4V + lw * 4096u + 2048u, OFF_WV + head * 16384u + (g1 >> 1), lane16);
        VMWAIT(12); STAMP(27); BAR();
        VMWAIT(4); BAR();
        dma4(rs, lb + R0 + lw * 8192u, OFF_WQK + head * 32768u + g0, lane16);
        STAMP(28); BAR();
        dma4_paced(rs, lb + R0 + lw * 8192u + 4096u, OFF_WQK + head * 32768u + g1, lane16);
      } else {
        VMWAIT(8); STAMP(27); BAR();
        VMWAIT(0); BAR();
        STAMP(28); BAR();
      }
    } else {
      {
        ROLES(); LANE_CONSTS();
        const unsigned xfo = (tb2 * 32 + 4 * nblk + 2 * kh) * 1024u;
        const unsigned xres = (layer ? OFF_X2 : OFF_X0) + xfo;
        const v4u r0 = ld16(rs, lane16, xres), r1 = ld16(rs, lane16 + 1024, xres);
        unsigned wa0 = R2 + kh * (16384 + 32768) + lane16, wa1 = wa0 ^ 32768u; OPQV(wa0); OPQV(wa1);
        auto wread = [&](auto U) -> v4u { constexpr int u = U; return lds16(L, ((u & 1) ? wa1 : wa0) + (u >> 1) * 1024); };
        v4u ar[6];
        sfor<0, 6>([&](auto I) { constexpr int u = I; ar[u] = wread(I); });
        f32x16 c0 = unpack_tile(r0, r1), c1 = {};
        OPQV(c0);
        wait_flags(rs, (FL_O + (layer * 64 + tb2) * 2 + kh) * 128u, 1u, lane, 16);
        STAMP(13);
        const unsigned ob = OFF_O + layer * (2 * MB) + (tb2 * 32 + 16 * kh) * 1024u;
        auto oload = [&](auto I) -> v4u { constexpr int i = I; return ld16(rs, lane16 + (i & 3) * 1024, ob + (((i >> 2) + rn) & 3) * 4096u); };
        v4u ofr[16];
        sfor<0, D2>([&](auto I) { constexpr int i = I; ofr[i] = oload(I); });
        SB();
        sfor<0, 32>([&](auto I) {
          constexpr int u = I;
          if constexpr (u & 1) { c1 = mfma16(ar[u % 6], ofr[u >> 1], c1); OPQV(c1); } else { c0 = mfma16(ar[u % 6], ofr[u >> 1], c0); OPQV(c0); }
          if constexpr (u + 6 < 32) ar[u % 6] = wread(IC(u + 6));
          if constexpr (!(u & 1) && (u >> 1) + D2 < 16) ofr[(u >> 1) + D2] = oload(IC((u >> 1) + D2));
          SB();
        });
        unsigned sown = R4S + cw * 4096 + lane16; OPQV(sown);
#pragma unroll
        for (int g = 0; g < 4; ++g) *(LAS f32x4*)(L + sown + g * 1024) = f32x4{c1[4 * g], c1[4 * g + 1], c1[4 * g + 2], c1[4 * g + 3]};
        OPQV(c0);
        STAMP(14);
        BAR();
        STAMP(15);
#pragma unroll
        for (int g = 0; g < 4; ++g) { const f32x4 t = *(LAS f32x4*)(L + (sown ^ 4096u) + g * 1024); c0[4 * g] += t[0]; c0[4 * g + 1] += t[1]; c0[4 * g + 2] += t[2]; c0[4 * g + 3] += t[3]; }
        v4u y0, y1;
        pack_tile(c0, y0, y1);
        const unsigned xo = OFF_X1 + layer * (2 * MB) + xfo;
        st16(y0, rs, lane16, xo); st16(y1, rs, lane16 + 1024, xo);
        st8(tile_stats(c0), rs, h ? OOB : r * 8, OFF_ST + (((1 + 2 * layer) * 64 + tb2) * 16 + 2 * nblk + kh) * 256u);
        VMWAIT(0);
        set_flag(rs, (FL_X + (1 + 2 * layer) * 64 + tb2) * 128u + (2 * nblk + kh) * 4, 1u, lane);
      }
      {
        ROLES(); LANE_CONSTS();
        STAMP(16);
        unsigned fpre;
        v4u ar[6];
        unsigned lhi = lane16 + R2; OPQV(lhi);
        unsigned lp0 = lane16 + kh * (16384 + 1024), lp1 = lp0 ^ 1024u;
        OPQV(lp0); OPQV(lp1);
        auto aread = [&](auto U) -> v4u {
          constexpr int u = U;
          if constexpr (u < 64) return lds16(L, ((u & 1) ? R1 : R0) + (u >> 1) * 1024 + lane16);
          else if constexpr (u < 224) {
            constexpr int T = 2 + (u - 64) / 32, s = (u - 64) % 32;
            if constexpr (T == 4 || T == 5) return lds16(L, (T - 4) * 32768 + s * 1024 + lane16);
            else return lds16(L, (T == 3 ? 32768 : 0) + s * 1024 + lhi);
          } else return lds16(L, R3 + ((u - 224) >> 1) * 2048 + ((u & 1) ? lp1 : lp0));
        };
        const unsigned augb = OFF_AUG + AUG_GU + mem * 7 * 512u;
        v4u pg0 = ld16p(rs, r * 16, augb), pg1 = ld16p(rs, r * 16 + 512, augb);
        v2u agn = make_aug(pg0), ag1 = make_aug(pg1);
        OPQV(agn); OPQV(ag1);
        wait_flags(rs, (FL_X + (1 + 2 * layer) * 64 + tb3) * 128u, 1u, lane, 16);
        STAMP(17);
        v2u sp[8];
        const unsigned xb = OFF_X1 + layer * (2 * MB) + tb3 * 32768u;
        auto xload = [&](auto I) -> v4u { constexpr int i = I; return ld16(rs, lane16 + (i & 3) * 1024, xb + (((i >> 2) + rg) & 7) * 4096u); };
        v4u x[32];
        sfor<0, D3>([&](auto I) { constexpr int i = I; x[i] = xload(I); });
        STAMP(23);
        BAR();
        STAMP(24);
        sfor<0, 6>([&](auto I) { constexpr int u = I; ar[u] = aread(I); });
        SB();
        const unsigned hb = OFF_H + layer * (7 * MB) + (tb3 * 112 + mem * 7) * 1024u;
        const unsigned fh_mine = (FL_H + (layer * 64 + tb3) * 2 + (mem >> 3)) * 128u + (mem & 7) * 4;
        const unsigned fh_poll = (FL_H + (layer * 64 + tb2) * 2 + kh) * 128u;
        Stats st;
        float cg, cu;
        f32x16 acc[3] = {};
        float ee[4], uu[4]; v4u hf;
        auto epi = [&](auto P_, auto E_, auto G_) {
          constexpr int p = P_, e = E_, g = G_;
          if constexpr (g == 0) { ee[e & 3] = ex2(acc[p % 3][e] * cg); OPQV(ee[e & 3]); }
          if constexpr (g == 1) { ee[e & 3] = __builtin_amdgcn_rcpf(1.0f + ee[e & 3]); uu[e & 3] = acc[p % 3][8 + e] * cu; OPQV(ee[e & 3]); OPQV(uu[e & 3]); }
          if constexpr (g == 2) {
            uu[e & 3] *= ee[e & 3]; OPQV(uu[e & 3]);
            if constexpr (e & 1) hf[e >> 1] = cvtpk(uu[(e - 1) & 3], uu[e & 3]);
            if constexpr (e == 7) st16(hf, rs, lane16 + (p & 3) * 1024, hb + (p >> 2) * 4096);
          }
        };
        SB();
        sfor<0, 224>([&](auto I) {
          constexpr int u = I;
          constexpr int T = u < 64 ? (u & 1) : 2 + (u - 64) / 32, s = u < 64 ? (u >> 1) : (u - 64) % 32;
          constexpr int left = u < 64 ? 64 - u : 32 - s;
          if constexpr (left == 4) {
            BAR();
            ar[(u + 4) % 6] = aread(IC(u + 4));
            ar[(u + 5) % 6] = aread(IC(u + 5));
          }
          if constexpr (u == 64) STAMP(18);
          if constexpr (u == 96) STAMP(19);
          if constexpr (u == 128) STAMP(20);
          if constexpr (u == 192) STAMP(21);
          if constexpr (u >= 64 && s == 0) { const f32x16 z = {}; acc[T % 3] = mfma8(agn, st.aug, z); }
          acc[T % 3] = mfma16(ar[u % 6], x[s], acc[T % 3]); OPQV(acc[T % 3]);
          if constexpr (left > 6 || left <= 4) ar[u % 6] = aread(IC(u + 6));
          if constexpr (u < 64 && !(u & 1) && s + D3 < 32) x[s + D3] = xload(IC(s + D3));
          if constexpr (u < 16 && (u & 1)) sp[s] = ld8(
rs, h * 2048 + r * 8 + s * 256, OFF_ST + ((1 + 2 * layer) * 64 + tb3) * 4096u);
          if constexpr (u == 31) { st = make_stats(sp, h); cg = st.rstd * -LOG2E; cu = st.rstd; OPQV(cg); OPQV(st.aug); }
          if constexpr (u == 36) { acc[1] = mfma8(ag1, st.aug, acc[1]); OPQV(acc[1]); }
          if constexpr (u == 37) { acc[0] = mfma8(agn, st.aug, acc[0]); OPQV(acc[0]); pg0 = ld16p(rs, r * 16 + 1024, augb); }
          if constexpr (left == 5 && u < 200) { agn = make_aug(pg0); OPQV(agn); }
          if constexpr (left == 3 && u < 168) { constexpr int n2 = (u < 64 ? 3 : T + 2); pg0 = ld16p(rs, r * 16 + (n2 & 1) * 512, augb + (n2 >> 1) * 1024); }
          if constexpr (T == 2 && u >= 64) {
            sfor<0, 16>([&](auto Q) { constexpr int q = Q, g = s - 2 - (3 * q) / 2; if constexpr (g >= 0 && g < 3) epi(IC(q / 8), IC(q % 8), IC(g)); });
          } else if constexpr (T > 2) {
            sfor<0, 8>([&](auto Q) { constexpr int q = Q, g = s - 2 - 3 * q; if constexpr (g >= 0 && g < 3) epi(IC(T - 1), Q, IC(g)); });
            if constexpr (s == 27 && T == 5) { VMWAIT(1); set_flag(rs, fh_mine, 4u, lane); }
            if constexpr (s == 14 && T == 6) fpre = poll_ld(rs, fh_poll, lane, 8);
            if constexpr (s == 27 && T == 6) { VMWAIT(1); set_flag(rs, fh_mine, 5u, lane); }
          }
          SB();
        });
        STAMP(22);
        const unsigned xfo = (tb2 * 32 + 4 * nblk + 2 * kh) * 1024u;
        if (!flags_ok(fpre, 4u, lane, 8)) wait_flags(rs, fh_poll, 4u, lane, 8);
        const unsigned hb4 = OFF_H + layer * (7 * MB) + (tb2 * 112 + 56 * kh) * 1024u;
        v4u hr[D4];
        unsigned lane16b = lane16 + 4096; OPQV(lane16b);
        auto hload = [&](auto Q) {
          constexpr int q = Q, c = q >> 3;
          hr[q % D4] = ld16(rs, (c < 4 ? lane16 : lane16b) + (c & 3) * 1024, hb4 + (((q & 7) + 2 * rn) & 7) * 7168u);
        };
        const unsigned xr1 = OFF_X1 + layer * (2 * MB) + xfo;
        const v4u r0 = ld16(rs, lane16, xr1), r1 = ld16(rs, lane16 + 1024, xr1);
        sfor<0, 6>(hload);
        SB();
        sfor<0, 10>([&](auto S_) { sfor<0, 8>([&](auto Q) { constexpr int g = S_ - Q; if constexpr (g >= 0 && g < 3) epi(IC(6), Q, IC(g)); }); });
        sfor<6, D4>(hload);
        f32x16 c0 = unpack_tile(r0, r1), c1 = {};
        unsigned lp0h = lp0 + R2, lp1h = lp1 + R2; OPQV(lp0h); OPQV(lp1h);
        auto dread = [&](auto U) -> v4u {
          constexpr int u = U, c = u / 16, i = (u % 16) >> 1;
          if constexpr (c == 0 || c == 4) return lds16(L, 32768 + i * 2048 + ((u & 1) ? lp1h : lp0h));
          else if constexpr (c == 3) return lds16(L, i * 2048 + ((u & 1) ? lp1h : lp0h));
          else return lds16(L, ((c == 2 || c == 6) ? 32768 : 0) + i * 2048 + ((u & 1) ? lp1 : lp0));
        };
        SB();
        sfor<0, 112>([&](auto I) {
          constexpr int u = I, c = u / 16, v = u % 16, q = u >> 1;
          if constexpr (v == 12 && c < 6) {
            BAR();
            ar[(224 + u + 4) % 6] = dread(IC(u + 4));
            ar[(224 + u + 5) % 6] = dread(IC(u + 5));
          }
          constexpr int U5 = 2 * (32 - D4) - 1, U7 = 2 * (40 - D4) - 1;
          if constexpr (u == U5 - 15 || u == U7 - 15) fpre = poll_ld(rs, fh_poll, lane, 8);
          if constexpr (u == 16) STAMP(25);
          if constexpr (u == 48) STAMP(26);
          if constexpr (u == 80) STAMP(27);
          if constexpr (u & 1) { c1 = mfma16(ar[(224 + u) % 6], hr[q % D4], c1); OPQV(c1); } else { c0 = mfma16(ar[(224 + u) % 6], hr[q % D4], c0); OPQV(c0); }
          if constexpr ((v < 10 || v >= 12) && u + 6 < 112) ar[(224 + u) % 6] = dread(IC(u + 6));
          if constexpr (u == 11) { asm volatile("s_waitcnt vmcnt(%0)" :: "i"(D4 - 6 + 5) : "memory"); set_flag(rs, fh_mine, 7u, lane); }
          if constexpr (u == U5) { if (!flags_ok(fpre, 5u, lane, 8)) wait_flags(rs, fh_poll, 5u, lane, 8); }
          if constexpr (u == U7) { if (!flags_ok(fpre, 7u, lane, 8)) wait_flags(rs, fh_poll, 7u, lane, 8); }
          if constexpr ((u & 1) && q + D4 < 56) hload(IC(q + D4));
          SB();
        });
        unsigned sown = R4S + cw * 4096 + lane16; OPQV(sown);
#pragma unroll
        for (int g = 0; g < 4; ++g) *(LAS f32x4*)(L + sown + g * 1024) = f32x4{c1[4 * g], c1[4 * g + 1], c1[4 * g + 2], c1[4 * g + 3]};
        OPQV(c0);
        STAMP(28);
        BAR();
        STAMP(29);
#pragma unroll
        for (int g = 0; g < 4; ++g) { const f32x4 t = *(LAS f32x4*)(L + (sown ^ 4096u) + g * 1024); c0[4 * g] += t[0]; c0[4 * g + 1] += t[1]; c0[4 * g + 2] += t[2]; c0[4 * g + 3] += t[3]; }
        v4u y0, y1;
        pack_tile(c0, y0, y1);
        if (layer == 0) {
          st16(y0, rs, lane16, OFF_X2 + xfo); st16(y1, rs, lane16 + 1024, OFF_X2 + xfo);
          st8(tile_stats(c0), rs, h ? OOB : r * 8, OFF_ST + ((128 + tb2) * 16 + 2 * nblk + kh) * 256u);
          VMWAIT(0);
          set_flag(rs, (FL_X + 128 + tb2) * 128u + (2 * nblk + kh) * 4, 1u, lane);
        } else {
          GAS v2u* op = (GAS v2u*)(a.out + (size_t)(tb2 * 32 + r) * DM + 64 * nblk + 32 * kh + 4 * h);
          op[0] = v2u{y0.x, y0.y}; op[2] = v2u{y0.z, y0.w}; op[4] = v2u{y1.x, y1.y}; op[6] = v2u{y1.z, y1.w};
        }
        STAMP(30);
      }
    }
  }
  STAMP_OUT();
}

struct PrepArgs { const u16 *x, *wqkv, *wo, *wg, *wu, *wd; unsigned char* ws; };
DI v2u trd(LAS unsigned char* L, unsigned off) { return __builtin_bit_cast(v2u, __builtin_amdgcn_ds_read_tr16_b64_v4i16((LAS s16x4*)(L + off))); }
DI v4u trfrag(LAS unsigned char* L, unsigned off) { const v2u a = trd(L, off), b = trd(L, off + 8 * 128); return v4u{a.x, a.y, b.x, b.y}; }
DI float colpart(v4u f) {
  const unsigned a = f.x, b = f.y, c = f.z, d = f.w;
  return xhalf_add(dot2(a, 0x3f803f80u, dot2(b, 0x3f803f80u, 0.f)) + dot2(c, 0x3f803f80u, dot2(d, 0x3f803f80u, 0.f)));
}
constexpr int PU_GU = 112, PU_QK = 32, PU_V = 32, PU_D = 112, PU_O = 32, PU_X = 64;
__global__ void __launch_bounds__(512) prep_k(PrepArgs p) {
  extern __shared__ __attribute__((aligned(16))) unsigned char smem[];
  LAS unsigned char* L = (LAS unsigned char*)smem;
  const unsigned lb = (unsigned)(size_t)L;
  int b = blockIdx.x;
  const int t = threadIdx.x, lane = t & 63, w = __builtin_amdgcn_readfirstlane(t >> 6), r = lane & 31, h = lane >> 5;
  const int g = lane >> 4, q = (lane & 15) >> 2, pp = lane & 3;
  const rsrc_t rs = __builtin_amdgcn_make_buffer_rsrc(p.ws, 0, WS_BYTES, 0x00020000);
  if (t < 27) { const unsigned o = (b * 27 + t) * 16; if (o < FL_LINES * 128) *(v4u*)(p.ws + OFF_FLAGS + o) = v4u{0u, 0u, 0u, 0u}; }
  LAS float* red = (LAS float*)(L + 34 * 1024);
  if (b < PU_GU + PU_QK) {
    const bool gu = b < PU_GU; const int u = gu ? b : b - PU_GU, cb = u >> 2, kq = u & 3, s = 8 * kq + w;
    const unsigned ld = gu ? FF * 2 : 3072;
    const rsrc_t ra = __builtin_amdgcn_make_buffer_rsrc((void*)(gu ? p.wg : p.wqkv), 0, gu ? DM * FF * 2 : DM * 3072, 0x00020000);
    const rsrc_t rb = __builtin_amdgcn_make_buffer_rsrc((void*)(gu ? p.wu : p.wqkv + 512), 0, gu ? DM * FF * 2 : DM * 3072 - 1024, 0x00020000);
    const unsigned vo = (lane >> 3) * ld + (lane & 7) * 16, so = 16 * s * ld + cb * 128;
    dma1(ra, lb + w * 4096u, so, vo); dma1(ra, lb + w * 4096u + 1024, so + 8 * ld, vo);
    dma1(rb, lb + w * 4096u + 2048, so, vo); dma1(rb, lb + w * 4096u + 3072, so + 8 * ld, vo);
    VMWAIT(0);
    const unsigned ta = w * 4096 + (g & 1) * 2048 + (4 * (g >> 1) + q) * 128 + pp * 8;
    unsigned char* dst = p.ws + (gu ? OFF_WGU : OFF_WQK);
#pragma unroll
    for (int tl = 0; tl < 4; ++tl) {
      const v4u f = trfrag(L, ta + tl * 32);
      *(v4u*)(dst + ((4 * cb + tl) * 32 + s) * 1024 + lane * 16) = f;
      const float cp = colpart(f);
      if (h == 0) red[(w * 4 + tl) * 32 + r] = cp;
    }
    __syncthreads();
    if (t < 128) {
      float cs = 0.f;
#pragma unroll
      for (int i = 0; i < 8; ++i) cs += red[i * 128 + t];
      *(float*)(p.ws + OFF_AUG + (gu ? AUG_GU : AUG_QK) + (cb * 128 + t) * 16 + kq * 4) = cs;
    }
    return;
  }
  b -= PU_GU + PU_QK;
  if (b < PU_V + PU_D + PU_O) {
    const int ty = b < PU_V ? 0 : (b < PU_V + PU_D ? 1 : 2);
    int cb, kr;
    if (ty == 0) { cb = b >> 2; kr = b & 3; } else if (ty == 1) { cb = (b - PU_V) / 14; kr = (b - PU_V) % 14; } else { cb = (b - PU_V - PU_D) >> 2; kr = (b - PU_V - PU_D) & 3; }
    const int s = 8 * kr + w;
    const unsigned ld = ty == 0 ? 3072 : 1024;
    const rsrc_t ra = __builtin_amdgcn_make_buffer_rsrc((void*)(ty == 0 ? p.wqkv + 1024 : (ty == 1 ? p.wd : p.wo)), 0,
                                                        ty == 0 ? DM * 3072 - 2048 : (ty == 1 ? FF * 1024 : DM * 1024), 0x00020000);
    const unsigned vo = (lane >> 3) * ld + (lane & 7) * 16, so = 16 * s * ld + cb * 128;
    dma1(ra, lb + w * 2048u, so, vo); dma1(ra, lb + w * 2048u + 1024, so + 8 * ld, vo);
    VMWAIT(0);
    const unsigned ta = w * 2048 + (4 * (g >> 1) + q) * 128 + (g & 1) * 32 + pp * 8;
#pragma unroll
    for (int tl = 0; tl < 2; ++tl) {
      const v4u f = trfrag(L, ta + tl * 64);
      if (ty == 0) {
        *(v4u*)(p.ws + OFF_WV + (4 * cb + 2 * tl + (g & 1)) * 16384 + s * 512 + (h * 16 + (lane & 15)) * 16) = f;
        const float cp = colpart(f);
        if (h == 0) red[(w * 2 + tl) * 32 + r] = cp;
      } else if (ty == 1) {
        const int mm = s / 7, c = s % 7;
        *(v4u*)(p.ws + OFF_WD + (cb * 224 + c * 32 + (mm >> 3) * 16 + (mm & 7) * 2 + tl) * 1024 + lane * 16) = f;
      } else {
        *(v4u*)(p.ws + OFF_WO + ((cb * 2 + tl) * 32 + s) * 1024 + lane * 16) = f;
      }
    }
    if (ty == 0) {
      __syncthreads();
      if (t < 64) {
        float cs = 0.f;
#pragma unroll
        for (int i = 0; i < 8; ++i) cs += red[i * 64 + t];
        *(float*)(p.ws + OFF_AUG + AUG_V + (cb * 64 + t) * 16 + kr * 4) = cs;
      }
    }
    return;
  }
  b -= PU_V + PU_D + PU_O;
  {
    const rsrc_t rx = __builtin_amdgcn_make_buffer_rsrc((void*)p.x, 0, SEQ * DM * 2, 0x00020000);
#pragma unroll
    for (int i = 0; i < 4; ++i) dma1(rx, lb + (4 * w + i) * 1040u, (b * 32 + 4 * w + i) * 1024u, lane * 16);
    VMWAIT(0);
    __syncthreads();
#pragma unroll
    for (int pr = 0; pr < 2; ++pr) {
      float s1 = 0.f, s2 = 0.f;
#pragma unroll
      for (int i = 0; i < 2; ++i) {
        const int s = 4 * w + 2 * pr + i;
        const v2u a = *(LAS v2u*)(L + r * 1040 + 32 * s + 8 * h), c = *(LAS v2u*)(L + r * 1040 + 32 * s + 16 + 8 * h);
        *(v4u*)(p.ws + OFF_X0 + (b * 32 + s) * 1024 + lane * 16) = v4u{a.x, a.y, c.x, c.y};
        const unsigned e0 = a.x, e1 = a.y, e2 = c.x, e3 = c.y;
        s1 = dot2(e0, 0x3f803f80u, dot2(e1, 0x3f803f80u, dot2(e2, 0x3f803f80u, dot2(e3, 0x3f803f80u, s1))));
        s2 = dot2(e0, e0, dot2(e1, e1, dot2(e2, e2, dot2(e3, e3, s2))));
      }
      s1 = xhalf_add(s1); s2 = xhalf_add(s2);
      if (h == 0) *(v2u*)(p.ws + OFF_ST + (b * 16 + 2 * w + pr) * 256 + r * 8) = v2u{__float_as_uint(s1), __float_as_uint(s2)};
    }
  }
}

extern "C" void kernel_launch(void* const* d_in, const int* in_sizes, int n_in, void* d_out, int out_size, void* d_ws, size_t ws_size,
                              hipStream_t stream) {
  PrepArgs p;
  p.x = (const u16*)d_in[0]; p.wqkv = (const u16*)d_in[1]; p.wo = (const u16*)d_in[2];
  p.wg = (const u16*)d_in[4]; p.wu = (const u16*)d_in[5]; p.wd = (const u16*)d_in[6]; p.ws = (unsigned char*)d_ws;
  prep_k<<<PU_GU + PU_QK + PU_V + PU_D + PU_O + PU_X, 512, 40 * 1024, stream>>>(p);
  Args a; a.ws = (unsigned char*)d_ws; a.out = (u16*)d_out;
  (void)hipFuncSetAttribute((const void*)main_k, hipFuncAttributeMaxDynamicSharedMemorySize, LDS_BYTES);
  main_k<<<256, 512, LDS_BYTES, stream>>>(a);
}
```

```cpp
#include <hip/hip_runtime.h>
#include <stdint.h>
#include <type_traits>

typedef unsigned short u16;
typedef float f32x16 __attribute__((ext_vector_type(16)));
typedef float f32x4 __attribute__((ext_vector_type(4)));
typedef float f32x2 __attribute__((ext_vector_type(2)));
typedef __bf16 bf16x8 __attribute__((ext_vector_type(8)));
typedef __bf16 bf16x2 __attribute__((ext_vector_type(2)));
typedef short s16x4 __attribute__((ext_vector_type(4)));
typedef unsigned v4u __attribute__((ext_vector_type(4)));
typedef unsigned v2u __attribute__((ext_vector_type(2)));
#define GAS __attribute__((address_space(1)))
#define LAS __attribute__((address_space(3)))
#define DI __device__ __forceinline__
#define RLX_AGENT __ATOMIC_RELAXED, __HIP_MEMORY_SCOPE_AGENT
#define IC(n) std::integral_constant<int, (n)>{}
typedef __amdgpu_buffer_rsrc_t rsrc_t;

constexpr int SEQ = 2048, DM = 512, FF = 1792;
constexpr unsigned KB = 1024u, MB = 1u << 20;
constexpr unsigned FL_EPOCH = 0;
constexpr unsigned FL_SEEN = 2;
constexpr unsigned FL_P = 32;
constexpr unsigned FL_Q = FL_P + 44;
constexpr unsigned FL_X = FL_Q + 32;
constexpr unsigned FL_O = FL_X + 3 * 64;
constexpr unsigned FL_H = FL_O + 2 * 64 * 2;
constexpr unsigned FL_KV = FL_H + 2 * 64 * 2;
constexpr unsigned FL_LINES = FL_KV + 2 * 32 * 8;
static_assert(FL_LINES * 128 <= 256 * KB, "flags");
constexpr unsigned OFF_AUG = 256 * KB;
constexpr unsigned OFF_ST0 = 384 * KB;
constexpr unsigned OFF_CSQ = 512 * KB;
constexpr unsigned OFF_WQK = 1 * MB, OFF_WV = 2 * MB, OFF_WO = 2 * MB + 512 * KB, OFF_WGU = 3 * MB, OFF_WD = 6 * MB + 512 * KB;
constexpr unsigned OFF_ST = 9 * MB;
constexpr unsigned OFF_X1 = 11 * MB, OFF_X2 = 15 * MB, OFF_O = 17 * MB, OFF_KV = 21 * MB, OFF_H = 29 * MB;
constexpr unsigned WS_BYTES = 43 * MB;
constexpr unsigned OOB = 0x40000000u;
constexpr unsigned R0 = 0, R1 = 32 * KB, R2 = 64 * KB, R3 = 96 * KB, R4V = 128 * KB, R4S = 144 * KB, LDS_BYTES = 160 * KB;
constexpr float NEG_BIG = -1.0e30f;
constexpr float LOG2E = 1.4426950408889634f;
constexpr float SAFE2 = 2500.0f;
constexpr unsigned SPIN_CAP = 1u << 18;
#define D1V 6
#define D2V 8
#define D3V 8
#define D4V 10
constexpr int D1 = D1V, D2 = D2V, D3 = D3V, D4 = D4V;

template <int B, int E, class F> DI void sfor(F&& f) {
  if constexpr (B < E) { f(std::integral_constant<int, B>{}); sfor<B + 1, E>(f); }
}
DI bf16x8 as_bf8(v4u v) { return __builtin_bit_cast(bf16x8, v); }
DI bf16x2 as_bf2(unsigned v) { return __builtin_bit_cast(bf16x2, v); }
DI s16x4 as_s4(v2u v) { return __builtin_bit_cast(s16x4, v); }
DI f32x16 mfma16(v4u a, v4u b, f32x16 c) { return __builtin_amdgcn_mfma_f32_32x32x16_bf16(as_bf8(a), as_bf8(b), c, 0, 0, 0); }
DI f32x16 mfma8(v2u a, v2u b, f32x16 c) { return __builtin_amdgcn_mfma_f32_32x32x8bf16_1k(as_s4(a), as_s4(b), c, 0, 0, 0); }
DI f32x4 mfma16s(v4u a, v4u b, f32x4 c) { return __builtin_amdgcn_mfma_f32_16x16x32_bf16(as_bf8(a), as_bf8(b), c, 0, 0, 0); }
DI unsigned cvtpk(float lo, float hi) { f32x2 f = {lo, hi}; bf16x2 p = __builtin_convertvector(f, bf16x2); return __builtin_bit_cast(unsigned, p); }
DI float bflo(unsigned v) { return __uint_as_float(v << 16); }
DI float bfhi(unsigned v) { return __uint_as_float(v & 0xffff0000u); }
DI float dot2(unsigned a, unsigned b, float c) { return __builtin_amdgcn_fdot2_f32_bf16(as_bf2(a), as_bf2(b), c, false); }
DI float ex2(float v) { return __builtin_amdgcn_exp2f(v); }
DI float xhalf_add(float v) { auto r = __builtin_amdgcn_permlane32_swap(__float_as_uint(v), __float_as_uint(v), false, false); return __uint_as_float(r[0]) + __uint_as_float(r[1]); }
DI float xhalf_max(float v) { auto r = __builtin_amdgcn_permlane32_swap(__float_as_uint(v), __float_as_uint(v), false, false); return fmaxf(__uint_as_float(r[0]), __uint_as_float(r[1])); }
DI unsigned umax(unsigned a, unsigned b) { return a > b ? a : b; }
DI unsigned row_umax(unsigned v) {
  v = umax(v, (unsigned)__builtin_amdgcn_update_dpp(0, (int)v, 0x121, 0xf, 0xf, true));
  v = umax(v, (unsigned)__builtin_amdgcn_update_dpp(0, (int)v, 0x122, 0xf, 0xf, true));
  v = umax(v, (unsigned)__builtin_amdgcn_update_dpp(0, (int)v, 0x124, 0xf, 0xf, true));
  v = umax(v, (unsigned)__builtin_amdgcn_update_dpp(0, (int)v, 0x128, 0xf, 0xf, true));
  return v;
}
DI v4u ld16(rsrc_t rs, unsigned voff, unsigned soff) { return __builtin_amdgcn_raw_buffer_load_b128(rs, voff, soff, 16); }
DI v4u ld16p(rsrc_t rs, unsigned voff, unsigned soff) { return __builtin_amdgcn_raw_buffer_load_b128(rs, voff, soff, 0); }
DI v2u ld8p(rsrc_t rs, unsigned voff, unsigned soff) { return __builtin_bit_cast(v2u, __builtin_amdgcn_raw_buffer_load_b64(rs, voff, soff, 0)); }
DI v2u ld8(rsrc_t rs, unsigned voff, unsigned soff) { return __builtin_bit_cast(v2u, __builtin_amdgcn_raw_buffer_load_b64(rs, voff, soff, 16)); }
DI void st16(v4u v, rsrc_t rs, unsigned voff, unsigned soff) {
  __builtin_amdgcn_raw_buffer_store_b128(v, rs, voff, soff, 16);
  asm volatile("s_nop 1" :: "v"(v) : "memory");
}
DI void st8(v2u v, rsrc_t rs, unsigned voff, unsigned soff) { __builtin_amdgcn_raw_buffer_store_b64(v, rs, voff, soff, 16); }
DI v4u lds16(LAS unsigned char* L, unsigned off) { return *(LAS v4u*)(L + off); }
#define SB() __builtin_amdgcn_sched_barrier(0)
#define OPQV(x) asm volatile("" : "+v"(x))
#define OPQS(x) asm volatile("" : "+s"(x))
#define LANE_CONSTS() int lane = threadIdx.x & 63; OPQV(lane); const int r = lane & 31, h = lane >> 5; const unsigned lane16 = lane * 16; (void)r; (void)h
#define BAR() asm volatile("s_waitcnt lgkmcnt(0)\n\ts_barrier" ::: "memory")
#define VMWAIT(n) asm volatile("s_waitcnt vmcnt(" #n ")" ::: "memory")
#define DMA_HEAD "s_nop 4\n\ts_mov_b32 m0, %0\n\ts_nop 0\n\t"
#define DMA_LD(o) "buffer_load_dwordx4 %1, %2, %3 offen offset:" #o " sc1 lds\n\t"
#define DMA_ARGS :: "s"(lds), "v"(voff), "s"(rs), "s"(soff) : "memory"
DI void dma4(rsrc_t rs, unsigned lds, unsigned soff, unsigned voff) { asm volatile(DMA_HEAD DMA_LD(0) DMA_LD(1024) DMA_LD(2048) DMA_LD(3072) DMA_ARGS); }
DI void dma1p(rsrc_t rs, unsigned lds, unsigned soff, unsigned voff) { asm volatile(DMA_HEAD "buffer_load_dwordx4 %1, %2, %3 offen lds" DMA_ARGS); }
#define PACE 3
#define STR2(x) #x
#define STR(x) STR2(x)
#define DMA_W "s_waitcnt vmcnt(" STR(PACE) ")\n\t"
DI void dma4_paced(rsrc_t rs, unsigned lds, unsigned soff, unsigned voff) {
  asm volatile(DMA_HEAD DMA_LD(0) DMA_W DMA_LD(1024) DMA_W DMA_LD(2048) DMA_W DMA_LD(3072) DMA_W DMA_ARGS);
}
DI void dma_tile(rsrc_t rs, unsigned lds, unsigned soff, unsigned lw, unsigned lane16) {
  dma4(rs, lds + lw * 8192u, soff + lw * 8192u, lane16);
  dma4(rs, lds + lw * 8192u + 4096u, soff + lw * 8192u + 4096u, lane16);
}
extern "C" __device__ long long wsatomic_add64(long long, rsrc_t, int, int, int) __asm("llvm.amdgcn.raw.ptr.buffer.atomic.add.i64");
struct Tag { unsigned lo, hi; };
DI v2u poll_ld(rsrc_t rs, unsigned voff, unsigned soff) {
  long long z = 0; OPQV(z);
  return __builtin_bit_cast(v2u, wsatomic_add64(z, rs, voff, soff, 0));
}
DI bool word_ok(v2u f, Tag t, unsigned target) { const unsigned a = f.x, b = f.y; return b == t.hi && (a - t.lo - target) <= 0xffffu - target; }
DI bool flags_ok(v2u f, Tag t, unsigned target, int lane, int n) { return __all(word_ok(f, t, target) || lane >= n); }
DI v2u wait_words(rsrc_t rs, unsigned vo, unsigned soff, Tag t, unsigned target, bool act) {
  v2u f = {0u, 0u};
#pragma nounroll
  for (unsigned spins = 0; spins < SPIN_CAP; ++spins) {
    f = poll_ld(rs, vo, soff);
    if (__all(word_ok(f, t, target) || !act)) break;
  }
  asm volatile("" ::: "memory");
  return f;
}
DI v2u wait_flags(rsrc_t rs, unsigned soff, Tag t, unsigned target, int lane, int n) {
  return wait_words(rs, lane < n ? lane * 8 : OOB, soff, t, target, lane < n);
}
DI void swap64(rsrc_t rs, unsigned voff, unsigned soff, v2u v) {
  asm volatile("s_nop 4\n\tbuffer_atomic_swap_x2 %0, %1, %2, %3 offen\n\ts_nop 1" :: "v"(v), "v"(voff), "s"(rs), "s"(soff) : "memory");
}
DI void set_flag(rsrc_t rs, unsigned off, Tag t, unsigned payload, int lane) { swap64(rs, lane == 0 ? 0u : OOB, off, v2u{t.lo | payload, t.hi}); }

struct Stats { float rstd, mean; v2u aug; };
DI Stats finish_stats(float s1, float s2, int h) {
  const float mean = s1 * (1.0f / DM);
  const float var = fmaxf(s2 * (1.0f / DM) - mean * mean, 0.0f);
  Stats st; st.rstd = __builtin_amdgcn_rsqf(var + 1e-5f); st.mean = mean;
  const float nm = h ? 0.0f : -mean;
  const unsigned hi = cvtpk(nm, nm) & 0xffffu;
  const float lo = nm - __uint_as_float(hi << 16);
  const unsigned w = hi | (cvtpk(lo, lo) << 16);
  st.aug.x = w; st.aug.y = w;
  return st;
}
DI Stats make_stats(const v2u (&sp)[8], int h) {
  float s1 = 0.f, s2 = 0.f;
#pragma unroll
  for (int i = 0; i < 8; ++i) { const unsigned a = sp[i].x, b = sp[i].y; s1 += __uint_as_float(a); s2 += __uint_as_float(b); }
  return finish_stats(xhalf_add(s1), xhalf_add(s2), h);
}
DI float xrow_add(float v) { auto r = __builtin_amdgcn_permlane16_swap(__float_as_uint(v), __float_as_uint(v), false, false); return __uint_as_float(r[0]) + __uint_as_float(r[1]); }
DI float wave_add(float v) {
  v += __int_as_float(__builtin_amdgcn_update_dpp(0, __float_as_int(v), 0x121, 0xf, 0xf, true));
  v += __int_as_float(__builtin_amdgcn_update_dpp(0, __float_as_int(v), 0x122, 0xf, 0xf, true));
  v += __int_as_float(__builtin_amdgcn_update_dpp(0, __float_as_int(v), 0x124, 0xf, 0xf, true));
  v += __int_as_float(__builtin_amdgcn_update_dpp(0, __float_as_int(v), 0x128, 0xf, 0xf, true));
  return xhalf_add(xrow_add(v));
}
DI v2u trd(LAS unsigned char* L, unsigned off) { return __builtin_bit_cast(v2u, __builtin_amdgcn_ds_read_tr16_b64_v4i16((LAS s16x4*)(L + off))); }
DI v4u trfrag(LAS unsigned char* L, unsigned off, unsigned second) { const v2u a = trd(L, off), b = trd(L, off + second); return v4u{a.x, a.y, b.x, b.y}; }
DI float sum8(v4u f, float c) {
  const unsigned a = f.x, b = f.y, d = f.z, e = f.w;
  return dot2(a, 0x3f803f80u, dot2(b, 0x3f803f80u, dot2(d, 0x3f803f80u, dot2(e, 0x3f803f80u, c))));
}
DI v2u split_aug(float cs) {
  const unsigned hi = cvtpk(cs, cs);
  const float lo = cs - __uint_as_float(hi << 16);
  return v2u{hi, cvtpk(lo, lo)};
}
DI float sum4(v4u p) {
  const unsigned a = p.x, b = p.y, c = p.z, d = p.w;
  return (__uint_as_float(a) + __uint_as_float(b)) + (__uint_as_float(c) + __uint_as_float(d));
}
DI void swap16(v4u& a, v4u& b) {
#pragma unroll
  for (int i = 0; i < 4; ++i) { const unsigned x = a[i], y = b[i]; const auto t = __builtin_amdgcn_permlane16_swap(x, y, false, false); a[i] = t[0]; b[i] = t[1]; }
}
DI v2u make_aug(v4u p) { return split_aug(sum4(p)); }

DI float max16(const f32x16& s, float m) {
  m = fmaxf(fmaxf(m, s[0]), s[1]);   m = fmaxf(fmaxf(m, s[2]), s[3]);
  float n = fmaxf(s[4], s[5]);       n = fmaxf(fmaxf(n, s[6]), s[7]);
  m = fmaxf(fmaxf(m, s[8]), s[9]);   n = fmaxf(fmaxf(n, s[10]), s[11]);
  m = fmaxf(fmaxf(m, s[12]), s[13]); n = fmaxf(fmaxf(n, s[14]), s[15]);
  return fmaxf(m, n);
}
DI void exp_pv(const f32x16& s, v4u v0, v4u v1, f32x16& o) {
  v4u p0, p1;
  p0.x = cvtpk(ex2(s[0]), ex2(s[1])); p0.y = cvtpk(ex2(s[2]), ex2(s[3])); p0.z = cvtpk(ex2(s[4]), ex2(s[5])); p0.w = cvtpk(ex2(s[6]), ex2(s[7]));
  o = mfma16(v0, p0, o);
  p1.x = cvtpk(ex2(s[8]), ex2(s[9])); p1.y = cvtpk(ex2(s[10]), ex2(s[11])); p1.z = cvtpk(ex2(s[12]), ex2(s[13])); p1.w = cvtpk(ex2(s[14]), ex2(s[15]));
  o = mfma16(v1, p1, o);
}
DI void attn_general(LAS unsigned char* L, v4u q, const f32x16& cm, int bm, int bf0, int nf, unsigned kaddr, unsigned vbase, unsigned vstep,
                     float& m, f32x16& o) {
  const f32x16 zero = {};
  float mx = max16(mfma16(lds16(L, kaddr + bm * 2048), q, cm), m);
#pragma nounroll
  for (int b = 0; b < nf; ++b) mx = max16(mfma16(lds16(L, kaddr + (bf0 + b) * 2048), q, zero), mx);
  mx = xhalf_max(mx);
  const float f = ex2(m - mx);
#pragma unroll
  for (int i = 0; i < 16; ++i) o[i] *= f;
  m = mx;
  f32x16 c;
#pragma unroll
  for (int i = 0; i < 16; ++i) c[i] = cm[i] - mx;
  unsigned va = vbase + vstep * bm;
  exp_pv(mfma16(lds16(L, kaddr + bm * 2048), q, c), lds16(L, va), lds16(L, va + 512), o);
#pragma unroll
  for (int i = 0; i < 16; ++i) c[i] = -mx;
#pragma nounroll
  for (int b = 0; b < nf; ++b) {
    va = vbase + vstep * (bf0 + b);
    exp_pv(mfma16(lds16(L, kaddr + (bf0 + b) * 2048), q, c), lds16(L, va), lds16(L, va + 512), o);
  }
}
template <int K, int S0, int DIR, class H>
DI void attn_chain(LAS unsigned char* L, v4u q, int n, unsigned kaddr, unsigned va, unsigned vstep, const f32x16& s, v4u v0, v4u v1, v4u kn,
                   f32x16& o, H&& hook) {
  if constexpr (K == 7) { hook(IC(K)); exp_pv(s, v0, v1, o); }
  else {
    const f32x16 zero = {};
    f32x16 sn = mfma16(kn, q, zero);
    v4u w0 = lds16(L, va), w1 = lds16(L, va + 512);
    if constexpr (K < 6) kn = lds16(L, kaddr + (S0 + DIR * (K + 1)) * 2048);
    hook(IC(K));
    SB();
    exp_pv(s, v0, v1, o);
    asm volatile("" : "+v"(sn), "+v"(w0), "+v"(w1), "+v"(kn), "+v"(o));
    if (n > K) attn_chain<K + 1, S0, DIR>(L, q, n, kaddr, DIR > 0 ? va + vstep : va - vstep, vstep, sn, w0, w1, kn, o, hook);
  }
}
template <int S0, int DIR, class H>
DI void attn_bounded(LAS unsigned char* L, v4u q, const f32x16& cm, int bm, int n, unsigned kaddr, unsigned vbase, unsigned vstep, f32x16& o, H&& hook) {
  const unsigned vm = vbase + vstep * bm;
  const v4u km = lds16(L, kaddr + bm * 2048), kn = lds16(L, kaddr + S0 * 2048), v0 = lds16(L, vm), v1 = lds16(L, vm + 512);
  attn_chain<0, S0, DIR>(L, q, n, kaddr, vbase + vstep * S0, vstep, mfma16(km, q, cm), v0, v1, kn, o, hook);
}
template <int S0, class H>
DI void attn_full(LAS unsigned char* L, v4u q, int n, unsigned kaddr, unsigned vbase, unsigned vstep, f32x16& o, H&& hook) {
  const f32x16 zero = {};
  const unsigned va = vbase + vstep * S0;
  const v4u k0 = lds16(L, kaddr + S0 * 2048), kn = lds16(L, kaddr + (S0 + 1) * 2048), v0 = lds16(L, va), v1 = lds16(L, va + 512);
  attn_chain<1, S0, 1>(L, q, n, kaddr, va + vstep, vstep, mfma16(k0, q, zero), v0, v1, kn, o, hook);
}

DI f32x16 unpack_tile(v4u r0, v4u r1) {
  f32x16 k;
  k[0] = bflo(r0.x); k[1] = bfhi(r0.x); k[2] = bflo(r0.y); k[3] = bfhi(r0.y); k[4] = bflo(r0.z); k[5] = bfhi(r0.z); k[6] = bflo(r0.w); k[7] = bfhi(r0.w);
  k[8] = bflo(r1.x); k[9] = bfhi(r1.x); k[10] = bflo(r1.y); k[11] = bfhi(r1.y); k[12] = bflo(r1.z); k[13] = bfhi(r1.z); k[14] = bflo(r1.w); k[15] = bfhi(r1.w);
  return k;
}
DI void pack_tile(const f32x16& k, v4u& y0, v4u& y1) {
  y0.x = cvtpk(k[0], k[1]); y0.y = cvtpk(k[2], k[3]); y0.z = cvtpk(k[4], k[5]); y0.w = cvtpk(k[6], k[7]);
  y1.x = cvtpk(k[8], k[9]); y1.y = cvtpk(k[10], k[11]); y1.z = cvtpk(k[12], k[13]); y1.w = cvtpk(k[14], k[15]);
}
DI v2u tile_stats(const f32x16& k) {
  f32x2 a = {k[0], k[1]}, b = a * a;
#pragma unroll
  for (int i = 1; i < 8; ++i) { const f32x2 t = {k[2 * i], k[2 * i + 1]}; a += t; b += t * t; }
  return v2u{__float_as_uint(xhalf_add(a[0] + a[1])), __float_as_uint(xhalf_add(b[0] + b[1]))};
}

struct Args { unsigned char* ws; u16* out; const u16 *x, *wqkv, *wo, *wg, *wu, *wd; };
#define STAMP(id) do {} while (0)
#define STAMP_DECL() do {} while (0)
#define STAMP_OUT() do {} while (0)

typedef const __attribute__((address_space(4))) Args* kargs_t;
DI kargs_t kargs() { kargs_t p = (kargs_t)__builtin_amdgcn_kernarg_segment_ptr(); OPQS(p); return p; }
__global__ void __launch_bounds__(512, 2) main_k(Args a) {
  extern __shared__ __attribute__((aligned(16))) unsigned char smem[];
  LAS unsigned char* L = (LAS unsigned char*)smem;
  const unsigned lb = (unsigned)(size_t)L;
  const int w0 = __builtin_amdgcn_readfirstlane(threadIdx.x >> 6);
  STAMP_DECL();
  const int xcd = blockIdx.x & 7, idx = blockIdx.x >> 3;
  const int sg0 = (xcd & ~1) | (idx >> 4), i4 = idx & 15, j0 = ((i4 >> 2) << 3) | ((xcd & 1) << 2) | (i4 & 3);
  const rsrc_t rs = __builtin_amdgcn_make_buffer_rsrc(a.ws, 0, WS_BYTES, 0x00020000);
#define ROLES() int w = w0, sg = sg0, j = j0, layer = layer0; OPQS(w); OPQS(sg); OPQS(j); OPQS(layer); Tag tg = tg0; OPQS(tg.lo); OPQS(tg.hi); \
  const int head = j, tbl = w < 4 ? w : 11 - w, Tq = 8 * sg + tbl;              \
  const int grp = 2 * sg + (j >> 4), mem = j & 15, nblk = j & 7;                \
  const int cw = w & 3, th = cw >> 1, kh = cw & 1; \
  const int tb2 = 4 * grp + 2 * ((j >> 3) & 1) + th, tb3 = 4 * grp + cw;        \
  (void)head; (void)Tq; (void)mem; (void)nblk; (void)kh; (void)tb2; (void)tb3
#define XROUND(rho) do { _Pragma("unroll") for (int p_ = 0; p_ < 4; ++p_) \
    dma1p(rx, lb + R2 + w * 8192u + ((rho) & 1) * 4096u + p_ * 1024u, (Tq * 32 + 8 * p_) * 1024u + (rho) * 128u, (p_ & 1) ? (xve ^ 64u) : xve); } while (0)
  Tag tg0 = {0u, 0u};
  {
    const int layer0 = 0;
    STAMP(31);
    ROLES(); LANE_CONSTS();
    const rsrc_t rx = __builtin_amdgcn_make_buffer_rsrc((void*)kargs()->x, 0, SEQ * DM * 2, 0x00020000);
    const rsrc_t rw = __builtin_amdgcn_make_buffer_rsrc((void*)kargs()->wqkv, 0, DM * 3072, 0x00020000);
    const unsigned token = 256 * sg + 8 * j + w;
    v2u ep; v4u row;
    asm volatile("s_nop 4\n\tbuffer_load_dwordx2 %0, off, %2, 0 sc1\n\tbuffer_load_dwordx4 %1, %3, %4, %5 offen"
                 : "=&v"(ep), "=&v"(row) : "s"(rs), "v"(lane16), "s"(rx), "s"(token * 1024u) : "memory");
    const unsigned vq = (lane >> 2) * 3072 + (lane & 1) * 16 + ((lane >> 1) & 1) * 1024 + head * 32;
    const unsigned vv = (lane >> 1) * 3072 + (lane & 1) * 16 + 2048 + head * 32;
#pragma unroll
    for (int i = 0; i < 4; ++i) dma1p(rw, lb + R0 + (4 * w + i) * 1024u, (4 * w + i) * 16u * 3072u, vq);
#pragma unroll
    for (int i = 0; i < 2; ++i) dma1p(rw, lb + R4V + (2 * w + i) * 1024u, (2 * w + i) * 32u * 3072u, vv);
    const unsigned xve = (lane >> 3) * 1024 + (((lane & 7) ^ (lane >> 4)) * 16);
    XROUND(0); XROUND(1);
    asm volatile("s_waitcnt vmcnt(14)" : "+v"(ep), "+v"(row) :: "memory");
    const unsigned e0 = __builtin_amdgcn_readfirstlane(ep.x) & 0xffff0000u, e1 = __builtin_amdgcn_readfirstlane(ep.y);
    tg.lo = e0 + 0x10000u; tg.hi = e1 + (tg.lo == 0u ? 1u : 0u);
    tg0 = tg;
    const unsigned a0 = row.x, a1 = row.y, a2 = row.z, a3 = row.w;
    const float s1 = wave_add(sum8(row, 0.f)), s2 = wave_add(dot2(a0, a0, dot2(a1, a1, dot2(a2, a2, dot2(a3, a3, 0.f)))));
    swap64(rs, lane < 2 ? lane * 8 : OOB, OFF_ST0 + token * 16u, v2u{__float_as_uint(lane ? s2 : s1), (tg.lo >> 16) | (tg.hi << 16)});
    swap64(rs, (w | lane) == 0 ? 0u : OOB, FL_SEEN * 128u + blockIdx.x * 8u, v2u{tg.lo | 1u, tg.hi});
  }

  for (int layer0 = 0; layer0 < 2; ++layer0) {
    {
      ROLES(); LANE_CONSTS();
      STAMP(0);
      const unsigned own = R1 + (8 + tbl) * 2048;
      f32x16 cqk = {};
      f32x4 cva = {}, cvb = {};
      Stats st; float csv;
      f32x16 cm;
      f32x4 rra, rrb, mma, mmb;
      auto mask2 = [&](auto I) {
        constexpr int i = I;
        float a0 = ((i & 3) + 8 * (i >> 2) + 4 * h <= r) ? 0.0f : NEG_BIG, a1 = (((i + 1) & 3) + 8 * (i >> 2) + 4 * h <= r) ? 0.0f : NEG_BIG;
        OPQV(a0); OPQV(a1); cm[i] = a0; cm[i + 1] = a1;
      };
      auto bounce_rd = [&] {
        const unsigned ba = own + 1024 + (lane >> 4) * 16;
        rra = *(LAS f32x4*)(L + ba); rrb = *(LAS f32x4*)(L + ba + 64); mma = *(LAS f32x4*)(L + ba + 128); mmb = *(LAS f32x4*)(L + ba + 192);
      };
      if (layer == 0) {
        const rsrc_t rx = __builtin_amdgcn_make_buffer_rsrc((void*)kargs()->x, 0, SEQ * DM * 2, 0x00020000);
        const int g = lane >> 4, q = (lane & 15) >> 2, pp = lane & 3;
        VMWAIT(10);
        BAR();
        STAMP(2);
        { unsigned o2 = 0x3f803f80u; OPQV(o2); *(LAS v4u*)(L + R4S + (lane & 1) * 512) = v4u{o2, o2, o2, o2}; }
        {
          const unsigned tq = R0 + (64 * w + 4 * (g >> 1) + q) * 64 + (g & 1) * 32 + pp * 8;
          const unsigned tv = R4V + (64 * w + 16 * (g >> 1) + 4 * (g & 1) + q) * 32 + pp * 8;
          const unsigned so = w == sg ? lane16 : OOB;
          float cq = 0.f, cvp = 0.f;
#pragma unroll
          for (int i = 0; i < 4; ++i) { const v4u f = trfrag(L, tq + i * 1024, 512); cq = sum8(f, cq); st16(f, rs, so + i * 1024, OFF_WQK + (head * 32 + 4 * w) * 1024u); }
#pragma unroll
          for (int i = 0; i < 2; ++i) { const v4u f = trfrag(L, tv + i * 1024, 256); cvp = sum8(f, cvp); st16(f, rs, so + i * 1024, OFF_WV + head * 16384u + 4 * w * 512u); }
          *(LAS f32x2*)(L + R4S + 2048 + w * 512 + lane * 8) = f32x2{cq, cvp};
          *(LAS v2u*)(L + R4S + 1536) = v2u{0u, 0u};
        }
        BAR();
        f32x2 cs = {0.f, 0.f};
#pragma unroll
        for (int i = 0; i < 8; ++i) cs += *(LAS f32x2*)(L + R4S + 2048 + i * 512 + lane * 8);
        const v2u aqk = split_aug(xhalf_add(cs[0]));
        csv = xhalf_add(xrow_add(cs[1]));
        st16(v4u{aqk.x, aqk.y, __float_as_uint(csv), 0u}, rs, w == 0 ? lane16 : OOB, OFF_CSQ + (sg * 32 + j) * 1024u);
        sfor<0, 8>([&](auto I) { mask2(IC(2 * I)); });
        const unsigned xve = (lane >> 3) * 1024 + (((lane & 7) ^ (lane >> 4)) * 16);
        unsigned xa[4];
#pragma unroll
        for (int i = 0; i < 4; ++i) { xa[i] = R2 + w * 8192 + (r >> 3) * 1024 + (r & 7) * 128 + (((2 * i + h) ^ ((r >> 1) & 7)) << 4); OPQV(xa[i]); }
        unsigned tq = R0 + (8 * (g >> 1) + q) * 64 + (g & 1) * 32 + pp * 8, tv = R4V + (16 * (g & 1) + 8 * (g >> 1) + q) * 32 + pp * 8;
        OPQV(tq); OPQV(tv);
        v2u sgr = {0u, 0u};
        SB();
        sfor<0, 8>([&](auto RHO) {
          constexpr int rho = RHO;
          v4u wq[4], wv[2], xf[4];
#pragma unroll
          for (int i = 0; i < 4; ++i) wq[i] = trfrag(L, tq + (4 * rho + i) * 1024, 256);
#pragma unroll
          for (int i = 0; i < 2; ++i) wv[i] = trfrag(L, tv + (2 * rho + i) * 1024, 128);
          if constexpr (rho < 2) VMWAIT(13);
          else if constexpr (rho == 4) VMWAIT(5);
          else if constexpr (rho == 5) asm volatile("s_waitcnt vmcnt(4)" : "+v"(sgr) :: "memory");
          else if constexpr (rho == 7) VMWAIT(0);
          else VMWAIT(4);
#pragma unroll
          for (int i = 0; i < 4; ++i) xf[i] = lds16(L, xa[i] + (rho & 1) * 4096);
          asm volatile("s_waitcnt lgkmcnt(0)" : "+v"(xf[0]), "+v"(xf[1]), "+v"(xf[2]), "+v"(xf[3]) :: "memory");
          if constexpr (rho == 3)
            asm volatile("s_nop 4\n\tbuffer_atomic_add_x2 %0, %1, %2, %3 offen sc0" : "+v"(sgr) : "v"(r * 16 + h * 8), "s"(rs), "s"(OFF_ST0 + Tq * 512u) : "memory");
          if constexpr (rho + 2 < 8) XROUND(rho + 2);
#pragma unroll
          for (int i = 0; i < 4; ++i) { cqk = mfma16(wq[i], xf[i], cqk); OPQV(cqk); }
#pragma unroll
          for (int i = 0; i < 2; ++i) {
            swap16(xf[2 * i], xf[2 * i + 1]);
            cva = mfma16s(xf[2 * i], wv[i], cva); OPQV(cva);
            cvb = mfma16s(xf[2 * i + 1], wv[i], cvb); OPQV(cvb);
          }
          if constexpr (rho == 5) {
            const unsigned t32 = (tg.lo >> 16) | (tg.hi << 16);
            if (!__all(sgr.y == t32)) {
#pragma nounroll
              for (unsigned spins = 0; spins < SPIN_CAP; ++spins) { sgr = poll_ld(rs, r * 16 + h * 8, OFF_ST0 + Tq * 512u); if (__all(sgr.y == t32)) break; }
            }
            const unsigned sv = sgr.x;
            const auto t = __builtin_amdgcn_permlane32_swap(sv, sv, false, false);
            st = finish_stats(__uint_as_float(t[0]), __uint_as_float(t[1]), h); OPQV(st.aug);
            *(LAS float*)(L + own + 1024 + h * 128 + r * 4) = h ? st.mean : st.rstd;
          }
          if constexpr (rho == 6) { cqk = mfma8(aqk, st.aug, cqk); OPQV(cqk); bounce_rd(); }
          SB();
        });
      } else {
        const unsigned xb = OFF_X2 + Tq * 32768u;
        const v4u pcs = ld16(rs, lane16, OFF_CSQ + (sg * 32 + j) * 1024u);
        wait_flags(rs, (FL_X + 64 + Tq) * 128u, tg, 1u, lane, 16);
        STAMP(1);
        auto xload = [&](auto I) -> v4u { constexpr int i = I; return ld16(rs, lane16 + (i & 3) * 1024, xb + (i >> 2) * 4096u); };
        v4u xr[D1];
        sfor<0, D1>([&](auto I) { constexpr int i = I; xr[i] = xload(I); });
        v2u sp[8];
        asm volatile("s_waitcnt vmcnt(%0)" :: "i"(D1) : "memory");
        BAR();
        STAMP(2);
        { unsigned o2 = 0x3f803f80u; OPQV(o2); *(LAS v4u*)(L + R4S + (lane & 1) * 512) = v4u{o2, o2, o2, o2}; }
        unsigned vwa = R4V + ((lane >> 4) & 1) * 512 + (h * 16 + (lane & 15)) * 16; OPQV(vwa);
        v4u wq[3], wv[2];
        wq[0] = lds16(L, R0 + lane16); wq[1] = lds16(L, R0 + lane16 + 1024); wv[0] = lds16(L, vwa);
        csv = __uint_as_float(pcs.z);
        v4u xprev;
        SB();
        sfor<0, 32>([&](auto I) {
          constexpr int s = I;
          v4u xf = xr[s % D1];
          cqk = mfma16(wq[s % 3], xf, cqk); OPQV(cqk);
          if constexpr (s + 2 < 32) wq[(s + 2) % 3] = lds16(L, R0 + lane16 + (s + 2) * 1024);
          if constexpr (!(s & 1) && s + 2 < 32) wv[((s >> 1) + 1) & 1] = lds16(L, vwa + (s + 2) * 512);
          if constexpr (s & 1) {
            swap16(xprev, xf);
            cva = mfma16s(xprev, wv[(s >> 1) & 1], cva); OPQV(cva);
            cvb = mfma16s(xf, wv[(s >> 1) & 1], cvb); OPQV(cvb);
            if constexpr (s - 1 + D1 < 32) xr[(s - 1) % D1] = xload(IC(s - 1 + D1));
            if constexpr (s + D1 < 32) xr[s % D1] = xload(IC(s + D1));
          } else xprev = xf;
          if constexpr (s >= 4 && s < 12) sp[s - 4] = ld8(rs, h * 2048 + r * 8 + (s - 4) * 256, OFF_ST + (64 + Tq) * 4096u);
          if constexpr (s == 20) { st = make_stats(sp, h); OPQV(st.aug); *(LAS float*)(L + own + 1024 + h * 128 + r * 4) = h ? st.mean : st.rstd; }
          if constexpr (s == 22) { cqk = mfma8(v2u{pcs.x, pcs.y}, st.aug, cqk); OPQV(cqk); bounce_rd(); }
          if constexpr (s >= 23 && s < 31) mask2(IC(2 * (s - 23)));
          SB();
        });
      }
      STAMP(3);
      const bool up = (lane & 16) != 0;
      const unsigned kvb = OFF_KV + layer * (4 * MB) + (head * 64 + Tq) * 2048u;
      const unsigned kvo = sg < 7 ? lane16 : OOB;
      v4u qf, kf, vf;
      float q2 = 0.f, k2 = 0.f;
#pragma unroll
      for (int i = 0; i < 8; ++i) cqk[8 + i] *= st.rstd;
      kf.x = cvtpk(cqk[8], cqk[9]); kf.y = cvtpk(cqk[10], cqk[11]); kf.z = cvtpk(cqk[12], cqk[13]); kf.w = cvtpk(cqk[14], cqk[15]);
      st16(kf, rs, kvo, kvb);
#pragma unroll
      for (int i = 0; i < 4; ++i) { cva[i] = (cva[i] - mma[i] * csv) * rra[i]; cvb[i] = (cvb[i] - mmb[i] * csv) * rrb[i]; }
      {
        const auto t0 = __builtin_amdgcn_permlane32_swap(cvtpk(cva[0], cva[1]), cvtpk(cvb[0], cvb[1]), false, false);
        const auto t1 = __builtin_amdgcn_permlane32_swap(cvtpk(cva[2], cva[3]), cvtpk(cvb[2], cvb[3]), false, false);
        vf = v4u{t0[0], t1[0], t0[1], t1[1]};
      }
      st16(vf, rs, kvo + 1024, kvb);
      SB();
      *(LAS v4u*)(L + own + lane16) = kf;
      *(LAS v4u*)(L + own + 1024 + lane16) = vf;
      const unsigned vstep = up ? 0u : 2048u, vbase = up ? R4S : R1 + 1024 + (h * 16 + (lane & 15)) * 16;
      const v4u vd0 = lds16(L, vbase + vstep * (8 + tbl)), vd1 = lds16(L, vbase + vstep * (8 + tbl) + 512);
      const float qs = st.rstd * (0.25f * LOG2E);
#pragma unroll
      for (int i = 0; i < 8; ++i) { cqk[i] *= qs; q2 += cqk[i] * cqk[i]; k2 += cqk[8 + i] * cqk[8 + i]; }
      qf.x = cvtpk(cqk[0], cqk[1]); qf.y = cvtpk(cqk[2], cqk[3]); qf.z = cvtpk(cqk[4], cqk[5]); qf.w = cvtpk(cqk[6], cqk[7]);
      const f32x16 sd = mfma16(kf, qf, cm);
      q2 = xhalf_add(q2) * 1.02f;
      const unsigned kr = row_umax(__float_as_uint(xhalf_add(k2) * 1.02f));
      const unsigned km0 = (umax(__builtin_amdgcn_readlane(kr, 0), __builtin_amdgcn_readlane(kr, 16)) >> 16) + 1u;
      const unsigned kmax16 = km0 < 0x7fc0u ? km0 : 0x7fc0u;
      *(LAS unsigned*)(L + R4S + 1024 + (8 + tbl) * 4) = kmax16 << 16;
      const unsigned fkv = (FL_KV + (layer * 32 + head) * 8 + sg) * 128u;
      f32x16 o = {};
      bool ok = __all(q2 * __uint_as_float(kmax16 << 16) <= SAFE2);
      exp_pv(sd, vd0, vd1, o);
      int un = blockIdx.x; OPQS(un);
      const int uty = un < 112 ? 0 : (un < 224 ? 1 : 2);
      const int ucb = uty == 0 ? un >> 2 : (uty == 1 ? (un - 112) / 14 : (un - 224) >> 2);
      const int uks = 8 * (uty == 1 ? (un - 112) % 14 : un & 3) + w;
      auto prep_load = [&] {
        const kargs_t ka = kargs();
        const unsigned ld = uty == 0 ? FF * 2 : 1024;
        const rsrc_t ra = __builtin_amdgcn_make_buffer_rsrc((void*)(uty == 0 ? ka->wg : (uty == 1 ? ka->wd : ka->wo)), 0, uty == 2 ? DM * 1024 : FF * 1024, 0x00020000);
        const rsrc_t rb = __builtin_amdgcn_make_buffer_rsrc((void*)ka->wu, 0, uty == 0 ? FF * 1024 : 0, 0x00020000);
        const unsigned vo = (lane >> 3) * ld + (lane & 7) * 16, so = 16 * uks * ld + ucb * 128;
        dma1p(ra, lb + R0 + w * 4096u, so, vo); dma1p(ra, lb + R0 + w * 4096u + 1024, so + 8 * ld, vo);
        dma1p(rb, lb + R0 + w * 4096u + 2048, so, vo); dma1p(rb, lb + R0 + w * 4096u + 3072, so + 8 * ld, vo);
      };
      if (w < 4) {
        if (layer) {
#pragma unroll
          for (int i = 0; i < 4; ++i) dma4(rs, lb + R2 + w * 16384u + i * 4096u, OFF_WO + nblk * 65536u + w * 16384u + i * 4096u, lane16);
          VMWAIT(16);
        } else VMWAIT(0);
        if (sg < 7) set_flag(rs, fkv + tbl * 8, tg, kmax16, lane);
      }
      STAMP(4);
      BAR();
      STAMP(5);
      if (w < 4 && layer == 0) prep_load();
      unsigned kaddr = R1 + lane16; OPQV(kaddr);
      ok = ok && __all(q2 * __uint_as_float(row_umax(*(LAS unsigned*)(L + R4S + 1024 + (8 + (lane & 7)) * 4))) <= SAFE2);
      auto late_kv = [&] { VMWAIT(0); if (sg < 7) set_flag(rs, fkv + tbl * 8, tg, kmax16, lane); if (layer == 0) prep_load(); };
      if (ok && tbl > 0) attn_full<8>(L, qf, tbl, kaddr, vbase, vstep, o, [&](auto K) { if (K == 2 && w >= 4) late_kv(); });
      if (!ok && w >= 4) late_kv();
      STAMP(6);
      if (w < 4) {
        if (layer) dma_tile(rs, lb + R0, OFF_WGU + mem * 7 * 32768u, w, lane16);
        if (sg > 0) {
          const v2u f = wait_flags(rs, fkv - 128 + 16 * w, tg, 1u, lane, 2);
          dma4(rs, lb + R1 + w * 4096u, OFF_KV + layer * (4 * MB) + (head * 64 + 8 * (sg - 1) + 2 * w) * 2048u, lane16);
          if (lane < 2) *(LAS unsigned*)(L + R4S + 1024 + (2 * w + lane) * 4) = f.x << 16;
        }
        STAMP(7);
        VMWAIT(0);
      }
      if (layer == 0) {
        VMWAIT(0);
        const int g = lane >> 4, q = (lane & 15) >> 2, pp = lane & 3;
        const unsigned ta = R0 + w * 4096 + (4 * (g >> 1) + q) * 128 + pp * 8 + (g & 1) * (uty == 0 ? 2048 : 32), tstep = uty == 0 ? 32 : 64;
        const int mm = uks / 7, cc = uks % 7;
        const unsigned dst = uty == 0 ? OFF_WGU + (4 * ucb * 32 + uks) * 1024u
                           : (uty == 1 ? OFF_WD + (ucb * 224 + cc * 32 + (mm >> 3) * 16 + (mm & 7) * 2) * 1024u : OFF_WO + (ucb * 64 + uks) * 1024u);
        const unsigned dstep = uty == 1 ? 1024u : 32768u;
#pragma unroll
        for (int tl = 0; tl < 4; ++tl) {
          if (tl < 2 || uty == 0) {
            const v4u f = trfrag(L, ta + tl * tstep, 1024);
            st16(f, rs, lane16, dst + tl * dstep);
            *(LAS float*)(L + R4S + 2048 + ((w * 4 + tl) * 32 + r) * 4) = xhalf_add(sum8(f, 0.f));
          }
        }
      }
      STAMP(8);
      BAR();
      STAMP(9);
      bool pdone = layer != 0;
      if (layer == 0 && uty == 0 && w < 2) {
        float cs = 0.f;
#pragma unroll
        for (int i = 0; i < 8; ++i) cs += *(LAS float*)(L + R4S + 2048 + (i * 128 + w * 64 + lane) * 4);
        __builtin_amdgcn_raw_buffer_store_b32(__float_as_uint(cs), rs, lane16, OFF_AUG + (ucb * 128 + w * 64) * 16u + (un & 3) * 4u, 16);
      }
      auto prep_flag = [&] {
        unsigned old = 0;
        if (lane == 0) old = __hip_atomic_fetch_add((LAS unsigned*)(L + R4S + 1536), 1u, __ATOMIC_RELAXED, __HIP_MEMORY_SCOPE_WORKGROUP);
        if (__builtin_amdgcn_readfirstlane(old) == 7u)
          set_flag(rs, (FL_P + (uty == 0 ? 0 : (uty == 1 ? 28 : 36)) + ucb) * 128u + (uks >> 3) * 8u, tg, 1u, lane);
        pdone = true;
      };
      if (sg > 0) ok = ok && __all(q2 * __uint_as_float(row_umax(*(LAS unsigned*)(L + R4S + 1024 + (lane & 7) * 4))) <= SAFE2);
      if (ok) {
        if (sg > 0) {
#pragma unroll
          for (int i = 0; i < 16; ++i) cm[i] = NEG_BIG - cm[i];
          attn_bounded<7, -1>(L, qf, cm, tbl, 7 - tbl, kaddr, vbase, vstep, o, [&](auto K) { if (K == 2 && !pdone) { VMWAIT(0); prep_flag(); } });
        }
      } else {
        float m = NEG_BIG;
#pragma unroll
        for (int i = 0; i < 16; ++i) o[i] = 0.f;
        attn_general(L, qf, cm, 8 + tbl, 8, tbl, kaddr, vbase, vstep, m, o);
        if (sg > 0) {
#pragma unroll
          for (int i = 0; i < 16; ++i) cm[i] = NEG_BIG - cm[i];
          attn_general(L, qf, cm, tbl, tbl + 1, 7 - tbl, kaddr, vbase, vstep, m, o);
        }
      }
      STAMP(10);
      const float il = __builtin_amdgcn_rcpf(o[8]);
      v4u of;
      of.x = cvtpk(o[0] * il, o[1] * il); of.y = cvtpk(o[2] * il, o[3] * il); of.z = cvtpk(o[4] * il, o[5] * il); of.w = cvtpk(o[6] * il, o[7] * il);
      st16(of, rs, lane16, OFF_O + layer * (2 * MB) + (Tq * 32 + head) * 1024u);
      VMWAIT(0);
      set_flag(rs, (FL_O + (layer * 64 + Tq) * 2 + (head >> 4)) * 128u + (head & 15) * 8, tg, 1u, lane);
      if (!pdone) prep_flag();
      if (layer == 0 && w == sg) set_flag(rs, (FL_Q + head) * 128u + sg * 8, tg, 1u, lane);
      STAMP(11);
    }
    if (w0 >= 4) {
      ROLES(); LANE_CONSTS();
      const unsigned lw = w - 4;
      const unsigned gu = OFF_WGU + mem * 7 * 32768u, wd = OFF_WD + nblk * 7 * 32768u;
      unsigned wlo = lw * 8192u + lane16, whi = wlo + R2; OPQV(wlo); OPQV(whi);
      v4u sa[4][8];
      auto stage = [&](auto B, unsigned src, auto PACED) {
        constexpr int b = B;
        sfor<0, 8>([&](auto I) {
          constexpr int i = I;
          sa[b][i] = ld16(rs, lane16 + (i & 3) * 1024, src + lw * 8192u + (i >> 2) * 4096u);
          if constexpr (PACED) asm volatile(DMA_W ::: "memory"); else SB();
        });
      };
      auto put = [&](auto B, unsigned base) { constexpr int b = B; sfor<0, 8>([&](auto I) { constexpr int i = I; *(LAS v4u*)(L + base + i * 1024) = sa[b][i]; }); };
      if (layer) {
        stage(IC(0), gu + 1 * 32768u, IC(0));
        BAR();
        stage(IC(1), gu + 2 * 32768u, IC(1));
        put(IC(0), wlo + R1);
        stage(IC(2), gu + 3 * 32768u, IC(1));
        STAMP(14); BAR();
      } else {
        BAR();
        volatile LAS unsigned* relay = (volatile LAS unsigned*)(L + R4S + 1540);
        if (lw == 0) { wait_flags(rs, (FL_P + 36 + nblk) * 128u, tg, 1u, lane, 4); *relay = 1u; }
        else {
#pragma nounroll
          for (unsigned spins = 0; spins < 64 * SPIN_CAP && *relay == 0u; ++spins) __builtin_amdgcn_s_sleep(1);
          asm volatile("" ::: "memory");
        }
        dma_tile(rs, lb + R2, OFF_WO + nblk * 65536u, lw, lane16);
        dma_tile(rs, lb + R3, OFF_WO + nblk * 65536u + 32768u, lw, lane16);
        if (lw == 0) {
          const unsigned c0 = (7 * mem) >> 2, nw = 4 * (((7 * mem + 6) >> 2) - c0 + 1);
          wait_words(rs, (unsigned)lane < nw ? (lane >> 2) * 128 + (lane & 3) * 8 : OOB, (FL_P + c0) * 128u, tg, 1u, (unsigned)lane < nw);
        }
        VMWAIT(0);
        BAR();
        dma_tile(rs, lb + R0, gu, lw, lane16);
        stage(IC(0), gu + 1 * 32768u, IC(1));
        STAMP(14); BAR();
        stage(IC(1), gu + 2 * 32768u, IC(1));
        put(IC(0), wlo + R1);
        stage(IC(2), gu + 3 * 32768u, IC(1));
      }
      put(IC(1), whi); put(IC(2), whi + 32768u);
      STAMP(17); BAR();
      if (layer == 0) {
        if (lw == 0) wait_flags(rs, (FL_P + 28 + nblk) * 128u, tg, 1u, lane, 14);
        if (lw == 1) wait_flags(rs, (FL_Q + head) * 128u, tg, 1u, lane, 8);
      }
      STAMP(18); BAR();
      dma_tile(rs, lb + R0, gu + 4 * 32768u, lw, lane16);
      dma_tile(rs, lb + R1, gu + 5 * 32768u, lw, lane16);
      STAMP(19); BAR();
      dma_tile(rs, lb + R2, gu + 6 * 32768u, lw, lane16);
      stage(IC(0), wd + 3 * 32768u, IC(0));
      VMWAIT(24); STAMP(20); BAR();
      dma_tile(rs, lb + R3, wd + 0 * 32768u, lw, lane16);
      stage(IC(1), wd + 4 * 32768u, IC(0));
      VMWAIT(32); BAR();
      dma_tile(rs, lb + R0, wd + 1 * 32768u, lw, lane16);
      stage(IC(2), wd + 5 * 32768u, IC(0));
      VMWAIT(40); STAMP(21); BAR();
      dma_tile(rs, lb + R1, wd + 2 * 32768u, lw, lane16);
      stage(IC(3), wd + 6 * 32768u, IC(0));
      VMWAIT(40); STAMP(22); BAR();
      put(IC(0), whi);
      VMWAIT(24); STAMP(25); BAR();
      put(IC(1), whi + 32768u);
      VMWAIT(8); BAR();
      put(IC(2), wlo);
      STAMP(26); BAR();
      put(IC(3), wlo + R1);
      BAR();
      STAMP(27); BAR();
      BAR();
      STAMP(28); BAR();
      if (layer == 0) {
        dma4_paced(rs, lb + R0 + lw * 8192u, OFF_WQK + head * 32768u + lw * 8192u, lane16);
        dma4_paced(rs, lb + R0 + lw * 8192u + 4096u, OFF_WQK + head * 32768u + lw * 8192u + 4096u, lane16);
        dma4(rs, lb + R4V + lw * 4096u, OFF_WV + head * 16384u + lw * 4096u, lane16);
      } else if (blockIdx.x == 0 && w == 4) {
#pragma unroll
        for (int i = 0; i < 4; ++i) wait_flags(rs, FL_SEEN * 128u + i * 512u, tg, 1u, lane, 64);
        set_flag(rs, FL_EPOCH * 128u, tg, 0u, lane);
      }
    } else {
      BAR();
      STAMP(12);
      {
        ROLES(); LANE_CONSTS();
        const unsigned xfo = (tb2 * 32 + 4 * nblk + 2 * kh) * 1024u;
        v4u r0, r1;
        if (layer) { r0 = ld16(rs, lane16, OFF_X2 + xfo); r1 = ld16(rs, lane16 + 1024, OFF_X2 + xfo); }
        else {
          const rsrc_t rx = __builtin_amdgcn_make_buffer_rsrc((void*)kargs()->x, 0, SEQ * DM * 2, 0x00020000);
          const unsigned vo = r * 1024 + h * 8, so = tb2 * 32768u + nblk * 128u + kh * 64u;
          const v2u t0 = ld8p(rx, vo, so), t1 = ld8p(rx, vo + 16, so), t2 = ld8p(rx, vo + 32, so), t3 = ld8p(rx, vo + 48, so);
          r0 = v4u{t0.x, t0.y, t1.x, t1.y}; r1 = v4u{t2.x, t2.y, t3.x, t3.y};
        }
        unsigned wa0 = R2 + kh * (16384 + 32768) + lane16, wa1 = wa0 ^ 32768u; OPQV(wa0); OPQV(wa1);
        auto wread = [&](auto U) -> v4u { constexpr int u = U; return lds16(L, ((u & 1) ? wa1 : wa0) + (u >> 1) * 1024); };
        v4u ar[6];
        f32x16 c0 = unpack_tile(r0, r1), c1 = {};
        OPQV(c0);
        wait_flags(rs, (FL_O + (layer * 64 + tb2) * 2 + kh) * 128u, tg, 1u, lane, 16);
        STAMP(13);
        const unsigned ob = OFF_O + layer * (2 * MB) + (tb2 * 32 + 16 * kh) * 1024u;
        auto oload = [&](auto I) -> v4u { constexpr int i = I; return ld16(rs, lane16 + (i & 3) * 1024, ob + (i >> 2) * 4096u); };
        v4u ofr[16];
        sfor<0, D2>([&](auto I) { constexpr int i = I; ofr[i] = oload(I); });
        if (layer == 0) BAR();
        sfor<0, 6>([&](auto I) { constexpr int u = I; ar[u] = wread(I); });
        SB();
        sfor<0, 32>([&](auto I) {
          constexpr int u = I;
          if constexpr (u & 1) { c1 = mfma16(ar[u % 6], ofr[u >> 1], c1); OPQV(c1); } else { c0 = mfma16(ar[u % 6], ofr[u >> 1], c0); OPQV(c0); }
          if constexpr (u + 6 < 32) ar[u % 6] = wread(IC(u + 6));
          if constexpr (!(u & 1) && (u >> 1) + D2 < 16) ofr[(u >> 1) + D2] = oload(IC((u >> 1) + D2));
          SB();
        });
        unsigned sown = R4S + cw * 4096 + lane16; OPQV(sown);
#pragma unroll
        for (int g = 0; g < 4; ++g) *(LAS f32x4*)(L + sown + g * 1024) = f32x4{c1[4 * g], c1[4 * g + 1], c1[4 * g + 2], c1[4 * g + 3]};
        OPQV(c0);
        STAMP(14);
        BAR();
        STAMP(15);
#pragma unroll
        for (int g = 0; g < 4; ++g) { const f32x4 t = *(LAS f32x4*)(L + (sown ^ 4096u) + g * 1024); c0[4 * g] += t[0]; c0[4 * g + 1] += t[1]; c0[4 * g + 2] += t[2]; c0[4 * g + 3] += t[3]; }
        v4u y0, y1;
        pack_tile(c0, y0, y1);
        const unsigned xo = OFF_X1 + layer * (2 * MB) + xfo;
        st16(y0, rs, lane16, xo); st16(y1, rs, lane16 + 1024, xo);
        st8(tile_stats(c0), rs, h ? OOB : r * 8, OFF_ST + ((layer * 128 + tb2) * 16 + 2 * nblk + kh) * 256u);
        VMWAIT(0);
        set_flag(rs, (FL_X + layer * 128 + tb2) * 128u + (2 * nblk + kh) * 8, tg, 1u, lane);
      }
      {
        ROLES(); LANE_CONSTS();
        STAMP(16);
        v2u fpre;
        v4u ar[6];
        unsigned lhi = lane16 + R2; OPQV(lhi);
        unsigned lp0 = lane16 + kh * (16384 + 1024), lp1 = lp0 ^ 1024u;
        OPQV(lp0); OPQV(lp1);
        auto aread = [&](auto U) -> v4u {
          constexpr int u = U;
          if constexpr (u < 64) return lds16(L, ((u & 1) ? R1 : R0) + (u >> 1) * 1024 + lane16);
          else if constexpr (u < 224) {
            constexpr int T = 2 + (u - 64) / 32, s = (u - 64) % 32;
            if constexpr (T == 4 || T == 5) return lds16(L, (T - 4) * 32768 + s * 1024 + lane16);
            else return lds16(L, (T == 3 ? 32768 : 0) + s * 1024 + lhi);
          } else return lds16(L, R3 + ((u - 224) >> 1) * 2048 + ((u & 1) ? lp1 : lp0));
        };
        const unsigned augb = OFF_AUG + mem * 7 * 512u;
        v4u pg0 = ld16(rs, r * 16, augb), pg1 = ld16(rs, r * 16 + 512, augb);
        v2u agn = make_aug(pg0), ag1 = make_aug(pg1);
        OPQV(agn); OPQV(ag1);
        wait_flags(rs, (FL_X + layer * 128 + tb3) * 128u, tg, 1u, lane, 16);
        STAMP(17);
        v2u sp[8];
        const unsigned xb = OFF_X1 + layer * (2 * MB) + tb3 * 32768u;
        auto xload = [&](auto I) -> v4u { constexpr int i = I; return ld16(rs, lane16 + (i & 3) * 1024, xb + (i >> 2) * 4096u); };
        v4u x[32];
        sfor<0, D3>([&](auto I) { constexpr int i = I; x[i] = xload(I); });
        STAMP(23);
        BAR();
        STAMP(24);
        sfor<0, 6>([&](auto I) { constexpr int u = I; ar[u] = aread(I); });
        SB();
        const unsigned hb = OFF_H + layer * (7 * MB) + (tb3 * 112 + mem * 7) * 1024u;
        const unsigned fh_mine = (FL_H + (layer * 64 + tb3) * 2 + (mem >> 3)) * 128u + (mem & 7) * 8;
        const unsigned fh_poll = (FL_H + (layer * 64 + tb2) * 2 + kh) * 128u;
        Stats st;
        float cg, cu;
        f32x16 acc[3] = {};
        float ee[4], uu[4]; v4u hf;
        auto epi = [&](auto P_, auto E_, auto G_) {
          constexpr int p = P_, e = E_, g = G_;
          if constexpr (g == 0) { ee[e & 3] = ex2(acc[p % 3][e] * cg); OPQV(ee[e & 3]); }
          if constexpr (g == 1) { ee[e & 3] = __builtin_amdgcn_rcpf(1.0f + ee[e & 3]); uu[e & 3] = acc[p % 3][8 + e] * cu; OPQV(ee[e & 3]); OPQV(uu[e & 3]); }
          if constexpr (g == 2) {
            uu[e & 3] *= ee[e & 3]; OPQV(uu[e & 3]);
            if constexpr (e & 1) hf[e >> 1] = cvtpk(uu[(e - 1) & 3], uu[e & 3]);
            if constexpr (e == 7) st16(hf, rs, lane16 + (p & 3) * 1024, hb + (p >> 2) * 4096);
          }
        };
        SB();
        sfor<0, 224>([&](auto I) {
          constexpr int u = I;
          constexpr int T = u < 64 ? (u & 1) : 2 + (u - 64) / 32, s = u < 64 ? (u >> 1) : (u - 64) % 32;
          constexpr int left = u < 64 ? 64 - u : 32 - s;
          if constexpr (left == 4) {
            BAR();
            ar[(u + 4) % 6] = aread(IC(u + 4));
            ar[(u + 5) % 6] = aread(IC(u + 5));
          }
          if constexpr (u == 64) STAMP(18);
          if constexpr (u == 96) STAMP(19);
          if constexpr (u == 128) STAMP(20);
          if constexpr (u == 192) STAMP(21);
          if constexpr (u >= 64 && s == 0) { const f32x16 z = {}; acc[T % 3] = mfma8(agn, st.aug, z); }
          acc[T % 3] = mfma16(ar[u % 6], x[s], acc[T % 3]); OPQV(acc[T % 3]);
          if constexpr (left > 6 || left <= 4) ar[u % 6] = aread(IC(u + 6));
          if constexpr (u < 64 && !(u & 1) && s + D3 < 32) x[s + D3] = xload(IC(s + D3));
          if constexpr (u < 16 && (u & 1)) sp[s] = ld8(rs, h * 2048 + r * 8 + s * 256, OFF_ST + (layer * 128 + tb3) * 4096u);
          if constexpr (u == 31) { st = make_stats(sp, h); cg = st.rstd * -LOG2E; cu = st.rstd; OPQV(cg); OPQV(st.aug); }
          if constexpr (u == 36) { acc[1] = mfma8(ag1, st.aug, acc[1]); OPQV(acc[1]); }
          if constexpr (u == 37) { acc[0] = mfma8(agn, st.aug, acc[0]); OPQV(acc[0]); pg0 = ld16(rs, r * 16 + 1024, augb); }
          if constexpr (left == 5 && u < 200) { agn = make_aug(pg0); OPQV(agn); }
          if constexpr (left == 3 && u < 168) { constexpr int n2 = (u < 64 ? 3 : T + 2); pg0 = ld16(rs, r * 16 + (n2 & 1) * 512, augb + (n2 >> 1) * 1024); }
          if constexpr (T == 2 && u >= 64) {
            sfor<0, 16>([&](auto Q) { constexpr int q = Q, g = s - 2 - (3 * q) / 2; if constexpr (g >= 0 && g < 3) epi(IC(q / 8), IC(q % 8), IC(g)); });
          } else if constexpr (T > 2) {
            sfor<0, 8>([&](auto Q) { constexpr int q = Q, g = s - 2 - 3 * q; if constexpr (g >= 0 && g < 3) epi(IC(T - 1), Q, IC(g)); });
            if constexpr (s == 27 && T == 5) { VMWAIT(1); set_flag(rs, fh_mine, tg, 4u, lane); }
            if constexpr (s == 14 && T == 6) fpre = poll_ld(rs, lane < 8 ? lane * 8 : OOB, fh_poll);
            if constexpr (s == 27 && T == 6) { VMWAIT(1); set_flag(rs, fh_mine, tg, 5u, lane); }
          }
          SB();
        });
        STAMP(22);
        const unsigned xfo = (tb2 * 32 + 4 * nblk + 2 * kh) * 1024u;
        if (!flags_ok(fpre, tg, 4u, lane, 8)) wait_flags(rs, fh_poll, tg, 4u, lane, 8);
        const unsigned hb4 = OFF_H + layer * (7 * MB) + (tb2 * 112 + 56 * kh) * 1024u;
        v4u hr[D4];
        unsigned lane16b = lane16 + 4096; OPQV(lane16b);
        auto hload = [&](auto Q) {
          constexpr int q = Q, c = q >> 3;
          hr[q % D4] = ld16(rs, (c < 4 ? lane16 : lane16b) + (c & 3) * 1024, hb4 + (q & 7) * 7168u);
        };
        const unsigned xr1 = OFF_X1 + layer * (2 * MB) + xfo;
        const v4u r0 = ld16(rs, lane16, xr1), r1 = ld16(rs, lane16 + 1024, xr1);
        sfor<0, 6>(hload);
        SB();
        sfor<0, 10>([&](auto S_) { sfor<0, 8>([&](auto Q) { constexpr int g = S_ - Q; if constexpr (g >= 0 && g < 3) epi(IC(6), Q, IC(g)); }); });
        sfor<6, D4>(hload);
        f32x16 c0 = unpack_tile(r0, r1), c1 = {};
        unsigned lp0h = lp0 + R2, lp1h = lp1 + R2; OPQV(lp0h); OPQV(lp1h);
        auto dread = [&](auto U) -> v4u {
          constexpr int u = U, c = u / 16, i = (u % 16) >> 1;
          if constexpr (c == 0 || c == 4) return lds16(L, 32768 + i * 2048 + ((u & 1) ? lp1h : lp0h));
          else if constexpr (c == 3) return lds16(L, i * 2048 + ((u & 1) ? lp1h : lp0h));
          else return lds16(L, ((c == 2 || c == 6) ? 32768 : 0) + i * 2048 + ((u & 1) ? lp1 : lp0));
        };
        SB();
        sfor<0, 112>([&](auto I) {
          constexpr int u = I, c = u / 16, v = u % 16, q = u >> 1;
          if constexpr (v == 12 && c < 6) {
            BAR();
            ar[(224 + u + 4) % 6] = dread(IC(u + 4));
            ar[(224 + u + 5) % 6] = dread(IC(u + 5));
          }
          constexpr int U5 = 2 * (32 - D4) - 1, U7 = 2 * (40 - D4) - 1;
          if constexpr (u == U5 - 15 || u == U7 - 15) fpre = poll_ld(rs, lane < 8 ? lane * 8 : OOB, fh_poll);
          if constexpr (u == 16) STAMP(25);
          if constexpr (u == 48) STAMP(26);
          if constexpr (u == 80) STAMP(27);
          if constexpr (u & 1) { c1 = mfma16(ar[(224 + u) % 6], hr[q % D4], c1); OPQV(c1); } else { c0 = mfma16(ar[(224 + u) % 6], hr[q % D4], c0); OPQV(c0); }
          if constexpr ((v < 10 || v >= 12) && u + 6 < 112) ar[(224 + u) % 6] = dread(IC(u + 6));
          if constexpr (u == 11) { asm volatile("s_waitcnt vmcnt(%0)" :: "i"(D4 - 6 + 5) : "memory"); set_flag(rs, fh_mine, tg, 7u, lane); }
          if constexpr (u == U5) { if (!flags_ok(fpre, tg, 5u, lane, 8)) wait_flags(rs, fh_poll, tg, 5u, lane, 8); }
          if constexpr (u == U7) { if (!flags_ok(fpre, tg, 7u, lane, 8)) wait_flags(rs, fh_poll, tg, 7u, lane, 8); }
          if constexpr ((u & 1) && q + D4 < 56) hload(IC(q + D4));
          SB();
        });
        unsigned sown = R4S + cw * 4096 + lane16; OPQV(sown);
#pragma unroll
        for (int g = 0; g < 4; ++g) *(LAS f32x4*)(L + sown + g * 1024) = f32x4{c1[4 * g], c1[4 * g + 1], c1[4 * g + 2], c1[4 * g + 3]};
        OPQV(c0);
        STAMP(28);
        BAR();
        STAMP(29);
#pragma unroll
        for (int g = 0; g < 4; ++g) { const f32x4 t = *(LAS f32x4*)(L + (sown ^ 4096u) + g * 1024); c0[4 * g] += t[0]; c0[4 * g + 1] += t[1]; c0[4 * g + 2] += t[2]; c0[4 * g + 3] += t[3]; }
        v4u y0, y1;
        pack_tile(c0, y0, y1);
        if (layer == 0) {
          st16(y0, rs, lane16, OFF_X2 + xfo); st16(y1, rs, lane16 + 1024, OFF_X2 + xfo);
          st8(tile_stats(c0), rs, h ? OOB : r * 8, OFF_ST + ((64 + tb2) * 16 + 2 * nblk + kh) * 256u);
          VMWAIT(0);
          set_flag(rs, (FL_X + 64 + tb2) * 128u + (2 * nblk + kh) * 8, tg, 1u, lane);
        } else {
          GAS v2u* op = (GAS v2u*)(kargs()->out + (size_t)(tb2 * 32 + r) * DM + 64 * nblk + 32 * kh + 4 * h);
          op[0] = v2u{y0.x, y0.y}; op[2] = v2u{y0.z, y0.w}; op[4] = v2u{y1.x, y1.y}; op[6] = v2u{y1.z, y1.w};
        }
        STAMP(30);
      }
    }
  }
  STAMP_OUT();
}

extern "C" void kernel_launch(void* const* d_in, const int* in_sizes, int n_in, void* d_out, int out_size, void* d_ws, size_t ws_size,
                              hipStream_t stream) {
  Args a;
  a.ws = (unsigned char*)d_ws; a.out = (u16*)d_out;
  a.x = (const u16*)d_in[0]; a.wqkv = (const u16*)d_in[1]; a.wo = (const u16*)d_in[2];
  a.wg = (const u16*)d_in[4]; a.wu = (const u16*)d_in[5]; a.wd = (const u16*)d_in[6];
  (void)hipFuncSetAttribute((const void*)main_k, hipFuncAttributeMaxDynamicSharedMemorySize, LDS_BYTES);
  main_k<<<256, 512, LDS_BYTES, stream>>>(a);
}
```

```cpp
#define OUTAUX 2
#include <hip/hip_runtime.h>
#include <stdint.h>
#include <type_traits>

typedef unsigned short u16;
typedef float f32x16 __attribute__((ext_vector_type(16)));
typedef float f32x4 __attribute__((ext_vector_type(4)));
typedef float f32x2 __attribute__((ext_vector_type(2)));
typedef __bf16 bf16x8 __attribute__((ext_vector_type(8)));
typedef __bf16 bf16x2 __attribute__((ext_vector_type(2)));
typedef short s16x4 __attribute__((ext_vector_type(4)));
typedef unsigned v4u __attribute__((ext_vector_type(4)));
typedef unsigned v2u __attribute__((ext_vector_type(2)));
#define GAS __attribute__((address_space(1)))
#define LAS __attribute__((address_space(3)))
#define DI __device__ __forceinline__
#define RLX_AGENT __ATOMIC_RELAXED, __HIP_MEMORY_SCOPE_AGENT
#define IC(n) std::integral_constant<int, (n)>{}
typedef __amdgpu_buffer_rsrc_t rsrc_t;

constexpr int SEQ = 2048, DM = 512, FF = 1792;
constexpr unsigned KB = 1024u, MB = 1u << 20;
constexpr unsigned FL_EPOCH = 0;
constexpr unsigned FL_SEEN = 2;
constexpr unsigned FL_P = 32;
constexpr unsigned FL_Q = FL_P + 44;
constexpr unsigned FL_X = FL_Q + 32;
constexpr unsigned FL_O = FL_X + 3 * 64;
constexpr unsigned FL_H = FL_O + 2 * 64 * 2;
constexpr unsigned FL_KV = FL_H + 2 * 64 * 2;
constexpr unsigned FL_LINES = FL_KV + 2 * 32 * 8;
static_assert(FL_LINES * 128 <= 256 * KB, "flags");
constexpr unsigned OFF_AUG = 256 * KB;
constexpr unsigned OFF_ST0 = 384 * KB;
constexpr unsigned OFF_CSQ = 512 * KB;
constexpr unsigned OFF_WQK = 1 * MB, OFF_WV = 2 * MB, OFF_WO = 2 * MB + 512 * KB, OFF_WGU = 3 * MB, OFF_WD = 6 * MB + 512 * KB;
constexpr unsigned OFF_ST = 9 * MB;
constexpr unsigned OFF_X1 = 11 * MB, OFF_X2 = 15 * MB, OFF_O = 17 * MB, OFF_KV = 21 * MB, OFF_H = 29 * MB;
constexpr unsigned WS_BYTES = 43 * MB;
constexpr unsigned OOB = 0x40000000u;
constexpr unsigned R0 = 0, R1 = 32 * KB, R2 = 64 * KB, R3 = 96 * KB, R4V = 128 * KB, R4S = 144 * KB, LDS_BYTES = 160 * KB;
constexpr float NEG_BIG = -1.0e30f;
constexpr float LOG2E = 1.4426950408889634f;
constexpr float SAFE2 = 2500.0f;
constexpr unsigned SPIN_CAP = 1u << 18;
#define D1V 6
#define D2V 8
#define D3V 8
#define D4V 10
constexpr int D1 = D1V, D2 = D2V, D3 = D3V, D4 = D4V;

template <int B, int E, class F> DI void sfor(F&& f) {
  if constexpr (B < E) { f(std::integral_constant<int, B>{}); sfor<B + 1, E>(f); }
}
DI bf16x8 as_bf8(v4u v) { return __builtin_bit_cast(bf16x8, v); }
DI bf16x2 as_bf2(unsigned v) { return __builtin_bit_cast(bf16x2, v); }
DI s16x4 as_s4(v2u v) { return __builtin_bit_cast(s16x4, v); }
DI f32x16 mfma16(v4u a, v4u b, f32x16 c) { return __builtin_amdgcn_mfma_f32_32x32x16_bf16(as_bf8(a), as_bf8(b), c, 0, 0, 0); }
DI f32x16 mfma8(v2u a, v2u b, f32x16 c) { return __builtin_amdgcn_mfma_f32_32x32x8bf16_1k(as_s4(a), as_s4(b), c, 0, 0, 0); }
DI f32x4 mfma16s(v4u a, v4u b, f32x4 c) { return __builtin_amdgcn_mfma_f32_16x16x32_bf16(as_bf8(a), as_bf8(b), c, 0, 0, 0); }
DI unsigned cvtpk(float lo, float hi) { f32x2 f = {lo, hi}; bf16x2 p = __builtin_convertvector(f, bf16x2); return __builtin_bit_cast(unsigned, p); }
DI float bflo(unsigned v) { return __uint_as_float(v << 16); }
DI float bfhi(unsigned v) { return __uint_as_float(v & 0xffff0000u); }
DI float dot2(unsigned a, unsigned b, float c) { return __builtin_amdgcn_fdot2_f32_bf16(as_bf2(a), as_bf2(b), c, false); }
DI float ex2(float v) { return __builtin_amdgcn_exp2f(v); }
DI float xhalf_add(float v) { auto r = __builtin_amdgcn_permlane32_swap(__float_as_uint(v), __float_as_uint(v), false, false); return __uint_as_float(r[0]) + __uint_as_float(r[1]); }
DI float xhalf_max(float v) { auto r = __builtin_amdgcn_permlane32_swap(__float_as_uint(v), __float_as_uint(v), false, false); return fmaxf(__uint_as_float(r[0]), __uint_as_float(r[1])); }
DI unsigned umax(unsigned a, unsigned b) { return a > b ? a : b; }
DI unsigned row_umax(unsigned v) {
  v = umax(v, (unsigned)__builtin_amdgcn_update_dpp(0, (int)v, 0x121, 0xf, 0xf, true));
  v = umax(v, (unsigned)__builtin_amdgcn_update_dpp(0, (int)v, 0x122, 0xf, 0xf, true));
  v = umax(v, (unsigned)__builtin_amdgcn_update_dpp(0, (int)v, 0x124, 0xf, 0xf, true));
  v = umax(v, (unsigned)__builtin_amdgcn_update_dpp(0, (int)v, 0x128, 0xf, 0xf, true));
  return v;
}
DI v4u ld16(rsrc_t rs, unsigned voff, unsigned soff) { return __builtin_amdgcn_raw_buffer_load_b128(rs, voff, soff, 16); }
DI v4u ld16p(rsrc_t rs, unsigned voff, unsigned soff) { return __builtin_amdgcn_raw_buffer_load_b128(rs, voff, soff, 0); }
DI v2u ld8p(rsrc_t rs, unsigned voff, unsigned soff) { return __builtin_bit_cast(v2u, __builtin_amdgcn_raw_buffer_load_b64(rs, voff, soff, 0)); }
DI v2u ld8(rsrc_t rs, unsigned voff, unsigned soff) { return __builtin_bit_cast(v2u, __builtin_amdgcn_raw_buffer_load_b64(rs, voff, soff, 16)); }
DI void st16(v4u v, rsrc_t rs, unsigned voff, unsigned soff) {
  __builtin_amdgcn_raw_buffer_store_b128(v, rs, voff, soff, 16);
  asm volatile("s_nop 1" :: "v"(v) : "memory");
}
DI void st8(v2u v, rsrc_t rs, unsigned voff, unsigned soff) { __builtin_amdgcn_raw_buffer_store_b64(v, rs, voff, soff, 16); }
DI v4u lds16(LAS unsigned char* L, unsigned off) { return *(LAS v4u*)(L + off); }
#define SB() __builtin_amdgcn_sched_barrier(0)
#define OPQV(x) asm volatile("" : "+v"(x))
#define OPQS(x) asm volatile("" : "+s"(x))
#define LANE_CONSTS() int lane = threadIdx.x & 63; OPQV(lane); const int r = lane & 31, h = lane >> 5; const unsigned lane16 = lane * 16; (void)r; (void)h
#define BAR() asm volatile("s_waitcnt lgkmcnt(0)\n\ts_barrier" ::: "memory")
#define VMWAIT(n) asm volatile("s_waitcnt vmcnt(" #n ")" ::: "memory")
#define DMA_HEAD "s_nop 4\n\ts_mov_b32 m0, %0\n\ts_nop 0\n\t"
#define DMA_LD(o) "buffer_load_dwordx4 %1, %2, %3 offen offset:" #o " sc1 lds\n\t"
#define DMA_ARGS :: "s"(lds), "v"(voff), "s"(rs), "s"(soff) : "memory"
DI void dma4(rsrc_t rs, unsigned lds, unsigned soff, unsigned voff) { asm volatile(DMA_HEAD DMA_LD(0) DMA_LD(1024) DMA_LD(2048) DMA_LD(3072) DMA_ARGS); }
DI void dma1p(rsrc_t rs, unsigned lds, unsigned soff, unsigned voff) { asm volatile(DMA_HEAD "buffer_load_dwordx4 %1, %2, %3 offen lds" DMA_ARGS); }
#define PACE 3
#define STR2(x) #x
#define STR(x) STR2(x)
#define DMA_W "s_waitcnt vmcnt(" STR(PACE) ")\n\t"
DI void dma4_paced(rsrc_t rs, unsigned lds, unsigned soff, unsigned voff) {
  asm volatile(DMA_HEAD DMA_LD(0) DMA_W DMA_LD(1024) DMA_W DMA_LD(2048) DMA_W DMA_LD(3072) DMA_W DMA_ARGS);
}
DI void dma_tile(rsrc_t rs, unsigned lds, unsigned soff, unsigned lw, unsigned lane16) {
  dma4(rs, lds + lw * 8192u, soff + lw * 8192u, lane16);
  dma4(rs, lds + lw * 8192u + 4096u, soff + lw * 8192u + 4096u, lane16);
}
extern "C" __device__ long long wsatomic_add64(long long, rsrc_t, int, int, int) __asm("llvm.amdgcn.raw.ptr.buffer.atomic.add.i64");
struct Tag { unsigned lo, hi; };
DI v2u poll_ld(rsrc_t rs, unsigned voff, unsigned soff) {
  long long z = 0; OPQV(z);
  return __builtin_bit_cast(v2u, wsatomic_add64(z, rs, voff, soff, 0));
}
DI bool word_ok(v2u f, Tag t, unsigned target) { const unsigned a = f.x, b = f.y; return b == t.hi && (a - t.lo - target) <= 0xffffu - target; }
DI bool flags_ok(v2u f, Tag t, unsigned target, int lane, int n) { return __all(word_ok(f, t, target) || lane >= n); }
DI v2u wait_words(rsrc_t rs, unsigned vo, unsigned soff, Tag t, unsigned target, bool act) {
  v2u f = {0u, 0u};
#pragma nounroll
  for (unsigned spins = 0; spins < SPIN_CAP; ++spins) {
    f = poll_ld(rs, vo, soff);
    if (__all(word_ok(f, t, target) || !act)) break;
  }
  asm volatile("" ::: "memory");
  return f;
}
DI v2u wait_flags(rsrc_t rs, unsigned soff, Tag t, unsigned target, int lane, int n) {
  return wait_words(rs, lane < n ? lane * 8 : OOB, soff, t, target, lane < n);
}
DI void swap64(rsrc_t rs, unsigned voff, unsigned soff, v2u v) {
  asm volatile("s_nop 4\n\tbuffer_atomic_swap_x2 %0, %1, %2, %3 offen\n\ts_nop 1" :: "v"(v), "v"(voff), "s"(rs), "s"(soff) : "memory");
}
DI void set_flag(rsrc_t rs, unsigned off, Tag t, unsigned payload, int lane) { swap64(rs, lane == 0 ? 0u : OOB, off, v2u{t.lo | payload, t.hi}); }

struct Stats { float rstd, mean; v2u aug; };
DI Stats finish_stats(float s1, float s2, int h) {
  const float mean = s1 * (1.0f / DM);
  const float var = fmaxf(s2 * (1.0f / DM) - mean * mean, 0.0f);
  Stats st; st.rstd = __builtin_amdgcn_rsqf(var + 1e-5f); st.mean = mean;
  const float nm = h ? 0.0f : -mean;
  const unsigned hi = cvtpk(nm, nm) & 0xffffu;
  const float lo = nm - __uint_as_float(hi << 16);
  const unsigned w = hi | (cvtpk(lo, lo) << 16);
  st.aug.x = w; st.aug.y = w;
  return st;
}
DI Stats make_stats(const v2u (&sp)[8], int h) {
  float s1 = 0.f, s2 = 0.f;
#pragma unroll
  for (int i = 0; i < 8; ++i) { const unsigned a = sp[i].x, b = sp[i].y; s1 += __uint_as_float(a); s2 += __uint_as_float(b); }
  return finish_stats(xhalf_add(s1), xhalf_add(s2), h);
}
DI float xrow_add(float v) { auto r = __builtin_amdgcn_permlane16_swap(__float_as_uint(v), __float_as_uint(v), false, false); return __uint_as_float(r[0]) + __uint_as_float(r[1]); }
DI float wave_add(float v) {
  v += __int_as_float(__builtin_amdgcn_update_dpp(0, __float_as_int(v), 0x121, 0xf, 0xf, true));
  v += __int_as_float(__builtin_amdgcn_update_dpp(0, __float_as_int(v), 0x122, 0xf, 0xf, true));
  v += __int_as_float(__builtin_amdgcn_update_dpp(0, __float_as_int(v), 0x124, 0xf, 0xf, true));
  v += __int_as_float(__builtin_amdgcn_update_dpp(0, __float_as_int(v), 0x128, 0xf, 0xf, true));
  return xhalf_add(xrow_add(v));
}
DI v2u trd(LAS unsigned char* L, unsigned off) { return __builtin_bit_cast(v2u, __builtin_amdgcn_ds_read_tr16_b64_v4i16((LAS s16x4*)(L + off))); }
DI v4u trfrag(LAS unsigned char* L, unsigned off, unsigned second) { const v2u a = trd(L, off), b = trd(L, off + second); return v4u{a.x, a.y, b.x, b.y}; }
DI float sum8(v4u f, float c) {
  const unsigned a = f.x, b = f.y, d = f.z, e = f.w;
  return dot2(a, 0x3f803f80u, dot2(b, 0x3f803f80u, dot2(d, 0x3f803f80u, dot2(e, 0x3f803f80u, c))));
}
DI v2u split_aug(float cs) {
  const unsigned hi = cvtpk(cs, cs);
  const float lo = cs - __uint_as_float(hi << 16);
  return v2u{hi, cvtpk(lo, lo)};
}
DI float sum4(v4u p) {
  const unsigned a = p.x, b = p.y, c = p.z, d = p.w;
  return (__uint_as_float(a) + __uint_as_float(b)) + (__uint_as_float(c) + __uint_as_float(d));
}
DI void swap16(v4u& a, v4u& b) {
#pragma unroll
  for (int i = 0; i < 4; ++i) { const unsigned x = a[i], y = b[i]; const auto t = __builtin_amdgcn_permlane16_swap(x, y, false, false); a[i] = t[0]; b[i] = t[1]; }
}
DI v2u make_aug(v4u p) { return split_aug(sum4(p)); }

DI float max16(const f32x16& s, float m) {
  m = fmaxf(fmaxf(m, s[0]), s[1]);   m = fmaxf(fmaxf(m, s[2]), s[3]);
  float n = fmaxf(s[4], s[5]);       n = fmaxf(fmaxf(n, s[6]), s[7]);
  m = fmaxf(fmaxf(m, s[8]), s[9]);   n = fmaxf(fmaxf(n, s[10]), s[11]);
  m = fmaxf(fmaxf(m, s[12]), s[13]); n = fmaxf(fmaxf(n, s[14]), s[15]);
  return fmaxf(m, n);
}
DI void exp_pv(const f32x16& s, v4u v0, v4u v1, f32x16& o) {
  v4u p0, p1;
  p0.x = cvtpk(ex2(s[0]), ex2(s[1])); p0.y = cvtpk(ex2(s[2]), ex2(s[3])); p0.z = cvtpk(ex2(s[4]), ex2(s[5])); p0.w = cvtpk(ex2(s[6]), ex2(s[7]));
  o = mfma16(v0, p0, o);
  p1.x = cvtpk(ex2(s[8]), ex2(s[9])); p1.y = cvtpk(ex2(s[10]), ex2(s[11])); p1.z = cvtpk(ex2(s[12]), ex2(s[13])); p1.w = cvtpk(ex2(s[14]), ex2(s[15]));
  o = mfma16(v1, p1, o);
}
DI void attn_general(LAS unsigned char* L, v4u q, const f32x16& cm, int bm, int bf0, int nf, unsigned kaddr, unsigned vbase, unsigned vstep,
                     float& m, f32x16& o) {
  const f32x16 zero = {};
  float mx = max16(mfma16(lds16(L, kaddr + bm * 2048), q, cm), m);
#pragma nounroll
  for (int b = 0; b < nf; ++b) mx = max16(mfma16(lds16(L, kaddr + (bf0 + b) * 2048), q, zero), mx);
  mx = xhalf_max(mx);
  const float f = ex2(m - mx);
#pragma unroll
  for (int i = 0; i < 16; ++i) o[i] *= f;
  m = mx;
  f32x16 c;
#pragma unroll
  for (int i = 0; i < 16; ++i) c[i] = cm[i] - mx;
  unsigned va = vbase + vstep * bm;
  exp_pv(mfma16(lds16(L, kaddr + bm * 2048), q, c), lds16(L, va), lds16(L, va + 512), o);
#pragma unroll
  for (int i = 0; i < 16; ++i) c[i] = -mx;
#pragma nounroll
  for (int b = 0; b < nf; ++b) {
    va = vbase + vstep * (bf0 + b);
    exp_pv(mfma16(lds16(L, kaddr + (bf0 + b) * 2048), q, c), lds16(L, va), lds16(L, va + 512), o);
  }
}
template <int K, int S0, int DIR, class H>
DI void attn_chain(LAS unsigned char* L, v4u q, int n, unsigned kaddr, unsigned va, unsigned vstep, const f32x16& s, v4u v0, v4u v1, v4u kn,
                   f32x16& o, H&& hook) {
  if constexpr (K == 7) { hook(IC(K)); exp_pv(s, v0, v1, o); }
  else {
    const f32x16 zero = {};
    f32x16 sn = mfma16(kn, q, zero);
    v4u w0 = lds16(L, va), w1 = lds16(L, va + 512);
    if constexpr (K < 6) kn = lds16(L, kaddr + (S0 + DIR * (K + 1)) * 2048);
    hook(IC(K));
    SB();
    exp_pv(s, v0, v1, o);
    asm volatile("" : "+v"(sn), "+v"(w0), "+v"(w1), "+v"(kn), "+v"(o));
    if (n > K) attn_chain<K + 1, S0, DIR>(L, q, n, kaddr, DIR > 0 ? va + vstep : va - vstep, vstep, sn, w0, w1, kn, o, hook);
  }
}
template <int S0, int DIR, class H>
DI void attn_bounded(LAS unsigned char* L, v4u q, const f32x16& cm, int bm, int n, unsigned kaddr, unsigned vbase, unsigned vstep, f32x16& o, H&& hook) {
  const unsigned vm = vbase + vstep * bm;
  const v4u km = lds16(L, kaddr + bm * 2048), kn = lds16(L, kaddr + S0 * 2048), v0 = lds16(L, vm), v1 = lds16(L, vm + 512);
  attn_chain<0, S0, DIR>(L, q, n, kaddr, vbase + vstep * S0, vstep, mfma16(km, q, cm), v0, v1, kn, o, hook);
}
template <int S0, class H>
DI void attn_full(LAS unsigned char* L, v4u q, int n, unsigned kaddr, unsigned vbase, unsigned vstep, f32x16& o, H&& hook) {
  const f32x16 zero = {};
  const unsigned va = vbase + vstep * S0;
  const v4u k0 = lds16(L, kaddr + S0 * 2048), kn = lds16(L, kaddr + (S0 + 1) * 2048), v0 = lds16(L, va), v1 = lds16(L, va + 512);
  attn_chain<1, S0, 1>(L, q, n, kaddr, va + vstep, vstep, mfma16(k0, q, zero), v0, v1, kn, o, hook);
}

DI f32x16 unpack_tile(v4u r0, v4u r1) {
  f32x16 k;
  k[0] = bflo(r0.x); k[1] = bfhi(r0.x); k[2] = bflo(r0.y); k[3] = bfhi(r0.y); k[4] = bflo(r0.z); k[5] = bfhi(r0.z); k[6] = bflo(r0.w); k[7] = bfhi(r0.w);
  k[8] = bflo(r1.x); k[9] = bfhi(r1.x); k[10] = bflo(r1.y); k[11] = bfhi(r1.y); k[12] = bflo(r1.z); k[13] = bfhi(r1.z); k[14] = bflo(r1.w); k[15] = bfhi(r1.w);
  return k;
}
DI void pack_tile(const f32x16& k, v4u& y0, v4u& y1) {
  y0.x = cvtpk(k[0], k[1]); y0.y = cvtpk(k[2], k[3]); y0.z = cvtpk(k[4], k[5]); y0.w = cvtpk(k[6], k[7]);
  y1.x = cvtpk(k[8], k[9]); y1.y = cvtpk(k[10], k[11]); y1.z = cvtpk(k[12], k[13]); y1.w = cvtpk(k[14], k[15]);
}
DI v2u tile_stats(const f32x16& k) {
  f32x2 a = {k[0], k[1]}, b = a * a;
#pragma unroll
  for (int i = 1; i < 8; ++i) { const f32x2 t = {k[2 * i], k[2 * i + 1]}; a += t; b += t * t; }
  return v2u{__float_as_uint(xhalf_add(a[0] + a[1])), __float_as_uint(xhalf_add(b[0] + b[1]))};
}

struct Args { unsigned char* ws; u16* out; const u16 *x, *wqkv, *wo, *wg, *wu, *wd; };
#define STAMP(id) do {} while (0)
#define STAMP_DECL() do {} while (0)
#define STAMP_OUT() do {} while (0)

typedef const __attribute__((address_space(4))) Args* kargs_t;
DI kargs_t kargs() { kargs_t p = (kargs_t)__builtin_amdgcn_kernarg_segment_ptr(); OPQS(p); return p; }
__global__ void __launch_bounds__(512, 2) main_k(Args a) {
  extern __shared__ __attribute__((aligned(16))) unsigned char smem[];
  LAS unsigned char* L = (LAS unsigned char*)smem;
  const unsigned lb = (unsigned)(size_t)L;
  const int w0 = __builtin_amdgcn_readfirstlane(threadIdx.x >> 6);
  STAMP_DECL();
  const int xcd = blockIdx.x & 7, idx = blockIdx.x >> 3;
  const int sg0 = (xcd & ~1) | (idx >> 4), i4 = idx & 15, j0 = ((i4 >> 2) << 3) | ((xcd & 1) << 2) | (i4 & 3);
  const rsrc_t rs = __builtin_amdgcn_make_buffer_rsrc(a.ws, 0, WS_BYTES, 0x00020000);
#define ROLES() int w = w0, sg = sg0, j = j0, layer = layer0; OPQS(w); OPQS(sg); OPQS(j); OPQS(layer); Tag tg = tg0; OPQS(tg.lo); OPQS(tg.hi); \
  const int head = j, tbl = w < 4 ? w : 11 - w, Tq = 8 * sg + tbl;              \
  const int grp = 2 * sg + (j >> 4), mem = j & 15, nblk = j & 7;                \
  const int cw = w & 3, th = cw >> 1, kh = cw & 1; \
  const int tb2 = 4 * grp + 2 * ((j >> 3) & 1) + th, tb3 = 4 * grp + cw;        \
  (void)head; (void)Tq; (void)mem; (void)nblk; (void)kh; (void)tb2; (void)tb3
#define XROUND(rho) do { _Pragma("unroll") for (int p_ = 0; p_ < 4; ++p_) \
    dma1p(rx, lb + R2 + w * 8192u + ((rho) & 1) * 4096u + p_ * 1024u, (Tq * 32 + 8 * p_) * 1024u + (((rho) + w + j) & 7) * 128u, (p_ & 1) ? (xve ^ 64u) : xve); } while (0)
  Tag tg0 = {0u, 0u};
  {
    const int layer0 = 0;
    STAMP(31);
    ROLES(); LANE_CONSTS();
    const rsrc_t rx = __builtin_amdgcn_make_buffer_rsrc((void*)kargs()->x, 0, SEQ * DM * 2, 0x00020000);
    const rsrc_t rw = __builtin_amdgcn_make_buffer_rsrc((void*)kargs()->wqkv, 0, DM * 3072, 0x00020000);
    const unsigned token = 256 * sg + 8 * j + w;
    v2u ep; v4u row;
    asm volatile("s_nop 4\n\tbuffer_load_dwordx2 %0, off, %2, 0 sc1\n\tbuffer_load_dwordx4 %1, %3, %4, %5 offen"
                 : "=&v"(ep), "=&v"(row) : "s"(rs), "v"(lane16), "s"(rx), "s"(token * 1024u) : "memory");
    const unsigned vq = (lane >> 2) * 3072 + (lane & 1) * 16 + ((lane >> 1) & 1) * 1024 + head * 32;
    const unsigned vv = (lane >> 1) * 3072 + (lane & 1) * 16 + 2048 + head * 32;
#pragma unroll
    for (int i = 0; i < 4; ++i) dma1p(rw, lb + R0 + (4 * w + i) * 1024u, (4 * w + i) * 16u * 3072u, vq);
#pragma unroll
    for (int i = 0; i < 2; ++i) dma1p(rw, lb + R4V + (2 * w + i) * 1024u, (2 * w + i) * 32u * 3072u, vv);
    const unsigned xve = (lane >> 3) * 1024 + (((lane & 7) ^ (lane >> 4)) * 16);
    XROUND(0); XROUND(1);
    asm volatile("s_waitcnt vmcnt(14)" : "+v"(ep), "+v"(row) :: "memory");
    const unsigned e0 = __builtin_amdgcn_readfirstlane(ep.x) & 0xffff0000u, e1 = __builtin_amdgcn_readfirstlane(ep.y);
    tg.lo = e0 + 0x10000u; tg.hi = e1 + (tg.lo == 0u ? 1u : 0u);
    tg0 = tg;
    const unsigned a0 = row.x, a1 = row.y, a2 = row.z, a3 = row.w;
    const float s1 = wave_add(sum8(row, 0.f)), s2 = wave_add(dot2(a0, a0, dot2(a1, a1, dot2(a2, a2, dot2(a3, a3, 0.f)))));
    swap64(rs, lane < 2 ? lane * 8 : OOB, OFF_ST0 + token * 16u, v2u{__float_as_uint(lane ? s2 : s1), (tg.lo >> 16) | (tg.hi << 16)});
    swap64(rs, (w | lane) == 0 ? 0u : OOB, FL_SEEN * 128u + blockIdx.x * 8u, v2u{tg.lo | 1u, tg.hi});
  }

  for (int layer0 = 0; layer0 < 2; ++layer0) {
    {
      ROLES(); LANE_CONSTS();
      STAMP(0);
      const unsigned own = R1 + (8 + tbl) * 2048;
      f32x16 cqk = {};
      f32x4 cva = {}, cvb = {};
      Stats st; float csv;
      f32x16 cm;
      f32x4 rra, rrb, mma, mmb;
      auto mask2 = [&](auto I) {
        constexpr int i = I;
        float a0 = ((i & 3) + 8 * (i >> 2) + 4 * h <= r) ? 0.0f : NEG_BIG, a1 = (((i + 1) & 3) + 8 * (i >> 2) + 4 * h <= r) ? 0.0f : NEG_BIG;
        OPQV(a0); OPQV(a1); cm[i] = a0; cm[i + 1] = a1;
      };
      auto bounce_rd = [&] {
        const unsigned ba = own + 1024 + (lane >> 4) * 16;
        rra = *(LAS f32x4*)(L + ba); rrb = *(LAS f32x4*)(L + ba + 64); mma = *(LAS f32x4*)(L + ba + 128); mmb = *(LAS f32x4*)(L + ba + 192);
      };
      if (layer == 0) {
        const rsrc_t rx = __builtin_amdgcn_make_buffer_rsrc((void*)kargs()->x, 0, SEQ * DM * 2, 0x00020000);
        const int g = lane >> 4, q = (lane & 15) >> 2, pp = lane & 3;
        VMWAIT(10);
        BAR();
        STAMP(2);
        { unsigned o2 = 0x3f803f80u; OPQV(o2); *(LAS v4u*)(L + R4S + (lane & 1) * 512) = v4u{o2, o2, o2, o2}; }
        {
          const unsigned tq = R0 + (64 * w + 4 * (g >> 1) + q) * 64 + (g & 1) * 32 + pp * 8;
          const unsigned tv = R4V + (64 * w + 16 * (g >> 1) + 4 * (g & 1) + q) * 32 + pp * 8;
          const unsigned so = w == sg ? lane16 : OOB;
          float cq = 0.f, cvp = 0.f;
#pragma unroll
          for (int i = 0; i < 4; ++i) { const v4u f = trfrag(L, tq + i * 1024, 512); cq = sum8(f, cq); st16(f, rs, so + i * 1024, OFF_WQK + (head * 32 + 4 * w) * 1024u); }
#pragma unroll
          for (int i = 0; i < 2; ++i) { const v4u f = trfrag(L, tv + i * 1024, 256); cvp = sum8(f, cvp); st16(f, rs, so + i * 1024, OFF_WV + head * 16384u + 4 * w * 512u); }
          *(LAS f32x2*)(L + R4S + 2048 + w * 512 + lane * 8) = f32x2{cq, cvp};
          *(LAS v2u*)(L + R4S + 1536) = v2u{0u, 0u};
        }
        BAR();
        f32x2 cs = {0.f, 0.f};
#pragma unroll
        for (int i = 0; i < 8; ++i) cs += *(LAS f32x2*)(L + R4S + 2048 + i * 512 + lane * 8);
        const v2u aqk = split_aug(xhalf_add(cs[0]));
        csv = xhalf_add(xrow_add(cs[1]));
        st16(v4u{aqk.x, aqk.y, __float_as_uint(csv), 0u}, rs, w == 0 ? lane16 : OOB, OFF_CSQ + (sg * 32 + j) * 1024u);
        sfor<0, 8>([&](auto I) { mask2(IC(2 * I)); });
        const unsigned xve = (lane >> 3) * 1024 + (((lane & 7) ^ (lane >> 4)) * 16);
        unsigned xa[4];
#pragma unroll
        for (int i = 0; i < 4; ++i) { xa[i] = R2 + w * 8192 + (r >> 3) * 1024 + (r & 7) * 128 + (((2 * i + h) ^ ((r >> 1) & 7)) << 4); OPQV(xa[i]); }
        unsigned tq = R0 + (8 * (g >> 1) + q) * 64 + (g & 1) * 32 + pp * 8, tv = R4V + (16 * (g & 1) + 8 * (g >> 1) + q) * 32 + pp * 8;
        OPQV(tq); OPQV(tv);
        v2u sgr = {0u, 0u};
        SB();
        sfor<0, 8>([&](auto RHO) {
          constexpr int rho = RHO;
          v4u wq[4], wv[2], xf[4];
          const unsigned kr = (rho + w + j) & 7;
#pragma unroll
          for (int i = 0; i < 4; ++i) wq[i] = trfrag(L, tq + kr * 4096 + i * 1024, 256);
#pragma unroll
          for (int i = 0; i < 2; ++i) wv[i] = trfrag(L, tv + kr * 2048 + i * 1024, 128);
          if constexpr (rho < 2) VMWAIT(13);
          else if constexpr (rho == 4) VMWAIT(5);
          else if constexpr (rho == 5) asm volatile("s_waitcnt vmcnt(4)" : "+v"(sgr) :: "memory");
          else if constexpr (rho == 7) VMWAIT(0);
          else VMWAIT(4);
#pragma unroll
          for (int i = 0; i < 4; ++i) xf[i] = lds16(L, xa[i] + (rho & 1) * 4096);
          asm volatile("s_waitcnt lgkmcnt(0)" : "+v"(xf[0]), "+v"(xf[1]), "+v"(xf[2]), "+v"(xf[3]) :: "memory");
          if constexpr (rho == 3)
            asm volatile("s_nop 4\n\tbuffer_atomic_add_x2 %0, %1, %2, %3 offen sc0" : "+v"(sgr) : "v"(r * 16 + h * 8), "s"(rs), "s"(OFF_ST0 + Tq * 512u) : "memory");
          if constexpr (rho + 2 < 8) XROUND(rho + 2);
#pragma unroll
          for (int i = 0; i < 4; ++i) { cqk = mfma16(wq[i], xf[i], cqk); OPQV(cqk); }
#pragma unroll
          for (int i = 0; i < 2; ++i) {
            swap16(xf[2 * i], xf[2 * i + 1]);
            cva = mfma16s(xf[2 * i], wv[i], cva); OPQV(cva);
            cvb = mfma16s(xf[2 * i + 1], wv[i], cvb); OPQV(cvb);
          }
          if constexpr (rho == 5) {
            const unsigned t32 = (tg.lo >> 16) | (tg.hi << 16);
            if (!__all(sgr.y == t32)) {
#pragma nounroll
              for (unsigned spins = 0; spins < SPIN_CAP; ++spins) { sgr = poll_ld(rs, r * 16 + h * 8, OFF_ST0 + Tq * 512u); if (__all(sgr.y == t32)) break; }
            }
            const unsigned sv = sgr.x;
            const auto t = __builtin_amdgcn_permlane32_swap(sv, sv, false, false);
            st = finish_stats(__uint_as_float(t[0]), __uint_as_float(t[1]), h); OPQV(st.aug);
            *(LAS float*)(L + own + 1024 + h * 128 + r * 4) = h ? st.mean : st.rstd;
          }
          if constexpr (rho == 6) { cqk = mfma8(aqk, st.aug, cqk); OPQV(cqk); bounce_rd(); }
          SB();
        });
      } else {
        const unsigned xb = OFF_X2 + Tq * 32768u;
        const v4u pcs = ld16(rs, lane16, OFF_CSQ + (sg * 32 + j) * 1024u);
        wait_flags(rs, (FL_X + 64 + Tq) * 128u, tg, 1u, lane, 16);
        STAMP(1);
        auto xload = [&](auto I) -> v4u { constexpr int i = I; return ld16(rs, lane16 + (i & 3) * 1024, xb + (i >> 2) * 4096u); };
        v4u xr[D1];
        sfor<0, D1>([&](auto I) { constexpr int i = I; xr[i] = xload(I); });
        v2u sp[8];
        asm volatile("s_waitcnt vmcnt(%0)" :: "i"(D1) : "memory");
        BAR();
        STAMP(2);
        { unsigned o2 = 0x3f803f80u; OPQV(o2); *(LAS v4u*)(L + R4S + (lane & 1) * 512) = v4u{o2, o2, o2, o2}; }
        unsigned vwa = R4V + ((lane >> 4) & 1) * 512 + (h * 16 + (lane & 15)) * 16; OPQV(vwa);
        v4u wq[3], wv[2];
        wq[0] = lds16(L, R0 + lane16); wq[1] = lds16(L, R0 + lane16 + 1024); wv[0] = lds16(L, vwa);
        csv = __uint_as_float(pcs.z);
        v4u xprev;
        SB();
        sfor<0, 32>([&](auto I) {
          constexpr int s = I;
          v4u xf = xr[s % D1];
          cqk = mfma16(wq[s % 3], xf, cqk); OPQV(cqk);
          if constexpr (s + 2 < 32) wq[(s + 2) % 3] = lds16(L, R0 + lane16 + (s + 2) * 1024);
          if constexpr (!(s & 1) && s + 2 < 32) wv[((s >> 1) + 1) & 1] = lds16(L, vwa + (s + 2) * 512);
          if constexpr (s & 1) {
            swap16(xprev, xf);
            cva = mfma16s(xprev, wv[(s >> 1) & 1], cva); OPQV(cva);
            cvb = mfma16s(xf, wv[(s >> 1) & 1], cvb); OPQV(cvb);
            if constexpr (s - 1 + D1 < 32) xr[(s - 1) % D1] = xload(IC(s - 1 + D1));
            if constexpr (s + D1 < 32) xr[s % D1] = xload(IC(s + D1));
          } else xprev = xf;
          if constexpr (s >= 4 && s < 12) sp[s - 4] = ld8(rs, h * 2048 + r * 8 + (s - 4) * 256, OFF_ST + (64 + Tq) * 4096u);
          if constexpr (s == 20) { st = make_stats(sp, h); OPQV(st.aug); *(LAS float*)(L + own + 1024 + h * 128 + r * 4) = h ? st.mean : st.rstd; }
          if constexpr (s == 22) { cqk = mfma8(v2u{pcs.x, pcs.y}, st.aug, cqk); OPQV(cqk); bounce_rd(); }
          if constexpr (s >= 23 && s < 31) mask2(IC(2 * (s - 23)));
          SB();
        });
      }
      STAMP(3);
      const bool up = (lane & 16) != 0;
      const unsigned kvb = OFF_KV + layer * (4 * MB) + (head * 64 + Tq) * 2048u;
      const unsigned kvo = sg < 7 ? lane16 : OOB;
      v4u qf, kf, vf;
      float q2 = 0.f, k2 = 0.f;
#pragma unroll
      for (int i = 0; i < 8; ++i) cqk[8 + i] *= st.rstd;
      kf.x = cvtpk(cqk[8], cqk[9]); kf.y = cvtpk(cqk[10], cqk[11]); kf.z = cvtpk(cqk[12], cqk[13]); kf.w = cvtpk(cqk[14], cqk[15]);
      st16(kf, rs, kvo, kvb);
#pragma unroll
      for (int i = 0; i < 4; ++i) { cva[i] = (cva[i] - mma[i] * csv) * rra[i]; cvb[i] = (cvb[i] - mmb[i] * csv) * rrb[i]; }
      {
        const auto t0 = __builtin_amdgcn_permlane32_swap(cvtpk(cva[0], cva[1]), cvtpk(cvb[0], cvb[1]), false, false);
        const auto t1 = __builtin_amdgcn_permlane32_swap(cvtpk(cva[2], cva[3]), cvtpk(cvb[2], cvb[3]), false, false);
        vf = v4u{t0[0], t1[0], t0[1], t1[1]};
      }
      st16(vf, rs, kvo + 1024, kvb);
      SB();
      *(LAS v4u*)(L + own + lane16) = kf;
      *(LAS v4u*)(L + own + 1024 + lane16) = vf;
      const unsigned vstep = up ? 0u : 2048u, vbase = up ? R4S : R1 + 1024 + (h * 16 + (lane & 15)) * 16;
      const v4u vd0 = lds16(L, vbase + vstep * (8 + tbl)), vd1 = lds16(L, vbase + vstep * (8 + tbl) + 512);
      const float qs = st.rstd * (0.25f * LOG2E);
#pragma unroll
      for (int i = 0; i < 8; ++i) { cqk[i] *= qs; q2 += cqk[i] * cqk[i]; k2 += cqk[8 + i] * cqk[8 + i]; }
      qf.x = cvtpk(cqk[0], cqk[1]); qf.y = cvtpk(cqk[2], cqk[3]); qf.z = cvtpk(cqk[4], cqk[5]); qf.w = cvtpk(cqk[6], cqk[7]);
      const f32x16 sd = mfma16(kf, qf, cm);
      q2 = xhalf_add(q2) * 1.02f;
      const unsigned kr = row_umax(__float_as_uint(xhalf_add(k2) * 1.02f));
      const unsigned km0 = (umax(__builtin_amdgcn_readlane(kr, 0), __builtin_amdgcn_readlane(kr, 16)) >> 16) + 1u;
      const unsigned kmax16 = km0 < 0x7fc0u ? km0 : 0x7fc0u;
      *(LAS unsigned*)(L + R4S + 1024 + (8 + tbl) * 4) = kmax16 << 16;
      const unsigned fkv = (FL_KV + (layer * 32 + head) * 8 + sg) * 128u;
      f32x16 o = {};
      bool ok = __all(q2 * __uint_as_float(kmax16 << 16) <= SAFE2);
      exp_pv(sd, vd0, vd1, o);
      int un = blockIdx.x; OPQS(un);
      const int uty = un < 112 ? 0 : (un < 224 ? 1 : 2);
      const int ucb = uty == 0 ? un >> 2 : (uty == 1 ? (un - 112) / 14 : (un - 224) >> 2);
      const int uks = 8 * (uty == 1 ? (un - 112) % 14 : un & 3) + w;
      auto prep_load = [&] {
        const kargs_t ka = kargs();
        const unsigned ld = uty == 0 ? FF * 2 : 1024;
        const rsrc_t ra = __builtin_amdgcn_make_buffer_rsrc((void*)(uty == 0 ? ka->wg : (uty == 1 ? ka->wd : ka->wo)), 0, uty == 2 ? DM * 1024 : FF * 1024, 0x00020000);
        const rsrc_t rb = __builtin_amdgcn_make_buffer_rsrc((void*)ka->wu, 0, uty == 0 ? FF * 1024 : 0, 0x00020000);
        const unsigned vo = (lane >> 3) * ld + (lane & 7) * 16, so = 16 * uks * ld + ucb * 128;
        dma1p(ra, lb + R0 + w * 4096u, so, vo); dma1p(ra, lb + R0 + w * 4096u + 1024, so + 8 * ld, vo);
        dma1p(rb, lb + R0 + w * 4096u + 2048, so, vo); dma1p(rb, lb + R0 + w * 4096u + 3072, so + 8 * ld, vo);
      };
      if (w < 4) {
        if (layer) {
#pragma unroll
          for (int i = 0; i < 4; ++i) dma4(rs, lb + R2 + w * 16384u + i * 4096u, OFF_WO + nblk * 65536u + w * 16384u + i * 4096u, lane16);
          VMWAIT(16);
        } else VMWAIT(0);
        if (sg < 7) set_flag(rs, fkv + tbl * 8, tg, kmax16, lane);
      }
      STAMP(4);
      BAR();
      STAMP(5);
      if (w < 4 && layer == 0) prep_load();
      unsigned kaddr = R1 + lane16; OPQV(kaddr);
      ok = ok && __all(q2 * __uint_as_float(row_umax(*(LAS unsigned*)(L + R4S + 1024 + (8 + (lane & 7)) * 4))) <= SAFE2);
      auto late_kv = [&] { VMWAIT(0); if (sg < 7) set_flag(rs, fkv + tbl * 8, tg, kmax16, lane); if (layer == 0) prep_load(); };
      if (ok && tbl > 0) attn_full<8>(L, qf, tbl, kaddr, vbase, vstep, o, [&](auto K) { if (K == 2 && w >= 4) late_kv(); });
      if (!ok && w >= 4) late_kv();
      STAMP(6);
      if (w < 4) {
        if (layer) dma_tile(rs, lb + R0, OFF_WGU + mem * 7 * 32768u, w, lane16);
        if (sg > 0) {
          const v2u f = wait_flags(rs, fkv - 128 + 16 * w, tg, 1u, lane, 2);
          dma4(rs, lb + R1 + w * 4096u, OFF_KV + layer * (4 * MB) + (head * 64 + 8 * (sg - 1) + 2 * w) * 2048u, lane16);
          if (lane < 2) *(LAS unsigned*)(L + R4S + 1024 + (2 * w + lane) * 4) = f.x << 16;
        }
        STAMP(7);
        VMWAIT(0);
      }
      if (layer == 0) {
        VMWAIT(0);
        const int g = lane >> 4, q = (lane & 15) >> 2, pp = lane & 3;
        const unsigned ta = R0 + w * 4096 + (4 * (g >> 1) + q) * 128 + pp * 8 + (g & 1) * (uty == 0 ? 2048 : 32), tstep = uty == 0 ? 32 : 64;
        const int mm = uks / 7, cc = uks % 7;
        const unsigned dst = uty == 0 ? OFF_WGU + (4 * ucb * 32 + uks) * 1024u
                           : (uty == 1 ? OFF_WD + (ucb * 224 + cc * 32 + (mm >> 3) * 16 + (mm & 7) * 2) * 1024u : OFF_WO + (ucb * 64 + uks) * 1024u);
        const unsigned dstep = uty == 1 ? 1024u : 32768u;
#pragma unroll
        for (int tl = 0; tl < 4; ++tl) {
          if (tl < 2 || uty == 0) {
            const v4u f = trfrag(L, ta + tl * tstep, 1024);
            st16(f, rs, lane16, dst + tl * dstep);
            *(LAS float*)(L + R4S + 2048 + ((w * 4 + tl) * 32 + r) * 4) = xhalf_add(sum8(f, 0.f));
          }
        }
      }
      STAMP(8);
      BAR();
      STAMP(9);
      bool pdone = layer != 0;
      if (layer == 0 && uty == 0 && w < 2) {
        float cs = 0.f;
#pragma unroll
        for (int i = 0; i < 8; ++i) cs += *(LAS float*)(L + R4S + 2048 + (i * 128 + w * 64 + lane) * 4);
        __builtin_amdgcn_raw_buffer_store_b32(__float_as_uint(cs), rs, lane16, OFF_AUG + (ucb * 128 + w * 64) * 16u + (un & 3) * 4u, 16);
      }
      auto prep_flag = [&] {
        unsigned old = 0;
        if (lane == 0) old = __hip_atomic_fetch_add((LAS unsigned*)(L + R4S + 1536), 1u, __ATOMIC_RELAXED, __HIP_MEMORY_SCOPE_WORKGROUP);
        if (__builtin_amdgcn_readfirstlane(old) == 7u)
          set_flag(rs, (FL_P + (uty == 0 ? 0 : (uty == 1 ? 28 : 36)) + ucb) * 128u + (uks >> 3) * 8u, tg, 1u, lane);
        pdone = true;
      };
      if (sg > 0) ok = ok && __all(q2 * __uint_as_float(row_umax(*(LAS unsigned*)(L + R4S + 1024 + (lane & 7) * 4))) <= SAFE2);
      if (ok) {
        if (sg > 0) {
#pragma unroll
          for (int i = 0; i < 16; ++i) cm[i] = NEG_BIG - cm[i];
          attn_bounded<7, -1>(L, qf, cm, tbl, 7 - tbl, kaddr, vbase, vstep, o, [&](auto K) { if (K == 2 && !pdone) { VMWAIT(0); prep_flag(); } });
        }
      } else {
        float m = NEG_BIG;
#pragma unroll
        for (int i = 0; i < 16; ++i) o[i] = 0.f;
        attn_general(L, qf, cm, 8 + tbl, 8, tbl, kaddr, vbase, vstep, m, o);
        if (sg > 0) {
#pragma unroll
          for (int i = 0; i < 16; ++i) cm[i] = NEG_BIG - cm[i];
          attn_general(L, qf, cm, tbl, tbl + 1, 7 - tbl, kaddr, vbase, vstep, m, o);
        }
      }
      STAMP(10);
      const float il = __builtin_amdgcn_rcpf(o[8]);
      v4u of;
      of.x = cvtpk(o[0] * il, o[1] * il); of.y = cvtpk(o[2] * il, o[3] * il); of.z = cvtpk(o[4] * il, o[5] * il); of.w = cvtpk(o[6] * il, o[7] * il);
      st16(of, rs, lane16, OFF_O + layer * (2 * MB) + (Tq * 32 + head) * 1024u);
      VMWAIT(0);
      set_flag(rs, (FL_O + (layer * 64 + Tq) * 2 + (head >> 4)) * 128u + (head & 15) * 8, tg, 1u, lane);
      if (!pdone) prep_flag();
      if (layer == 0 && w == sg) set_flag(rs, (FL_Q + head) * 128u + sg * 8, tg, 1u, lane);
      STAMP(11);
    }
    if (w0 >= 4) {
      ROLES(); LANE_CONSTS();
      const unsigned lw = w - 4;
      const unsigned gu = OFF_WGU + mem * 7 * 32768u, wd = OFF_WD + nblk * 7 * 32768u;
      unsigned wlo = lw * 8192u + lane16, whi = wlo + R2; OPQV(wlo); OPQV(whi);
      v4u sa[4][8];
      auto stage = [&](auto B, unsigned src, auto PACED) {
        constexpr int b = B;
        sfor<0, 8>([&](auto I) {
          constexpr int i = I;
          sa[b][i] = ld16(rs, lane16 + (i & 3) * 1024, src + lw * 8192u + (i >> 2) * 4096u);
          if constexpr (PACED) asm volatile(DMA_W ::: "memory"); else SB();
        });
      };
      auto put = [&](auto B, unsigned base) { constexpr int b = B; sfor<0, 8>([&](auto I) { constexpr int i = I; *(LAS v4u*)(L + base + i * 1024) = sa[b][i]; }); };
      if (layer) {
        stage(IC(0), gu + 1 * 32768u, IC(0));
        BAR();
        stage(IC(1), gu + 2 * 32768u, IC(1));
        put(IC(0), wlo + R1);
        stage(IC(2), gu + 3 * 32768u, IC(1));
        STAMP(14); BAR();
      } else {
        BAR();
        volatile LAS unsigned* relay = (volatile LAS unsigned*)(L + R4S + 1540);
        if (lw == 0) { wait_flags(rs, (FL_P + 36 + nblk) * 128u, tg, 1u, lane, 4); *relay = 1u; }
        else {
#pragma nounroll
          for (unsigned spins = 0; spins < 64 * SPIN_CAP && *relay == 0u; ++spins) __builtin_amdgcn_s_sleep(1);
          asm volatile("" ::: "memory");
        }
        dma_tile(rs, lb + R2, OFF_WO + nblk * 65536u, lw, lane16);
        dma_tile(rs, lb + R3, OFF_WO + nblk * 65536u + 32768u, lw, lane16);
        if (lw == 0) {
          const unsigned c0 = (7 * mem) >> 2, nw = 4 * (((7 * mem + 6) >> 2) - c0 + 1);
          wait_words(rs, (unsigned)lane < nw ? (lane >> 2) * 128 + (lane & 3) * 8 : OOB, (FL_P + c0) * 128u, tg, 1u, (unsigned)lane < nw);
        }
        VMWAIT(0);
        BAR();
        dma_tile(rs, lb + R0, gu, lw, lane16);
        stage(IC(0), gu + 1 * 32768u, IC(1));
        STAMP(14); BAR();
        stage(IC(1), gu + 2 * 32768u, IC(1));
        put(IC(0), wlo + R1);
        stage(IC(2), gu + 3 * 32768u, IC(1));
      }
      put(IC(1), whi); put(IC(2), whi + 32768u);
      STAMP(17); BAR();
      if (layer == 0) {
        if (lw == 0) wait_flags(rs, (FL_P + 28 + nblk) * 128u, tg, 1u, lane, 14);
        if (lw == 1) wait_flags(rs, (FL_Q + head) * 128u, tg, 1u, lane, 8);
      }
      STAMP(18); BAR();
      dma_tile(rs, lb + R0, gu + 4 * 32768u, lw, lane16);
      dma_tile(rs, lb + R1, gu + 5 * 32768u, lw, lane16);
      STAMP(19); BAR();
      dma_tile(rs, lb + R2, gu + 6 * 32768u, lw, lane16);
      stage(IC(0), wd + 3 * 32768u, IC(0));
      VMWAIT(24); STAMP(20); BAR();
      dma_tile(rs, lb + R3, wd + 0 * 32768u, lw, lane16);
      stage(IC(1), wd + 4 * 32768u, IC(0));
      VMWAIT(32); BAR();
      dma_tile(rs, lb + R0, wd + 1 * 32768u, lw, lane16);
      stage(IC(2), wd + 5 * 32768u, IC(0));
      VMWAIT(40); STAMP(21); BAR();
      dma_tile(rs, lb + R1, wd + 2 * 32768u, lw, lane16);
      stage(IC(3), wd + 6 * 32768u, IC(0));
      VMWAIT(40); STAMP(22); BAR();
      put(IC(0), whi);
      VMWAIT(24); STAMP(25); BAR();
      put(IC(1), whi + 32768u);
      VMWAIT(8); BAR();
      put(IC(2), wlo);
      STAMP(26); BAR();
      put(IC(3), wlo + R1);
      BAR();
      STAMP(27); BAR();
      BAR();
      STAMP(28); BAR();
      if (layer == 0) {
        dma4_paced(rs, lb + R0 + lw * 8192u, OFF_WQK + head * 32768u + lw * 8192u, lane16);
        dma4_paced(rs, lb + R0 + lw * 8192u + 4096u, OFF_WQK + head * 32768u + lw * 8192u + 4096u, lane16);
        dma4(rs, lb + R4V + lw * 4096u, OFF_WV + head * 16384u + lw * 4096u, lane16);
      } else if (blockIdx.x == 0 && w == 4) {
#pragma unroll
        for (int i = 0; i < 4; ++i) wait_flags(rs, FL_SEEN * 128u + i * 512u, tg, 1u, lane, 64);
        set_flag(rs, FL_EPOCH * 128u, tg, 0u, lane);
      }
    } else {
      BAR();
      STAMP(12);
      {
        ROLES(); LANE_CONSTS();
        const unsigned xfo = (tb2 * 32 + 4 * nblk + 2 * kh) * 1024u;
        v4u r0, r1;
        if (layer) { r0 = ld16(rs, lane16, OFF_X2 + xfo); r1 = ld16(rs, lane16 + 1024, OFF_X2 + xfo); }
        else {
          const rsrc_t rx = __builtin_amdgcn_make_buffer_rsrc((void*)kargs()->x, 0, SEQ * DM * 2, 0x00020000);
          const unsigned vo = r * 1024 + h * 8, so = tb2 * 32768u + nblk * 128u + kh * 64u;
          const v2u t0 = ld8p(rx, vo, so), t1 = ld8p(rx, vo + 16, so), t2 = ld8p(rx, vo + 32, so), t3 = ld8p(rx, vo + 48, so);
          r0 = v4u{t0.x, t0.y, t1.x, t1.y}; r1 = v4u{t2.x, t2.y, t3.x, t3.y};
        }
        unsigned wa0 = R2 + kh * (16384 + 32768) + lane16, wa1 = wa0 ^ 32768u; OPQV(wa0); OPQV(wa1);
        auto wread = [&](auto U) -> v4u { constexpr int u = U; return lds16(L, ((u & 1) ? wa1 : wa0) + (u >> 1) * 1024); };
        v4u ar[6];
        f32x16 c0 = unpack_tile(r0, r1), c1 = {};
        OPQV(c0);
        wait_flags(rs, (FL_O + (layer * 64 + tb2) * 2 + kh) * 128u, tg, 1u, lane, 16);
        STAMP(13);
        const unsigned ob = OFF_O + layer * (2 * MB) + (tb2 * 32 + 16 * kh) * 1024u;
        auto oload = [&](auto I) -> v4u { constexpr int i = I; return ld16(rs, lane16 + (i & 3) * 1024, ob + (i >> 2) * 4096u); };
        v4u ofr[16];
        sfor<0, D2>([&](auto I) { constexpr int i = I; ofr[i] = oload(I); });
        if (layer == 0) BAR();
        sfor<0, 6>([&](auto I) { constexpr int u = I; ar[u] = wread(I); });
        SB();
        sfor<0, 32>([&](auto I) {
          constexpr int u = I;
          if constexpr (u & 1) { c1 = mfma16(ar[u % 6], ofr[u >> 1], c1); OPQV(c1); } else { c0 = mfma16(ar[u % 6], ofr[u >> 1], c0); OPQV(c0); }
          if constexpr (u + 6 < 32) ar[u % 6] = wread(IC(u + 6));
          if constexpr (!(u & 1) && (u >> 1) + D2 < 16) ofr[(u >> 1) + D2] = oload(IC((u >> 1) + D2));
          SB();
        });
        unsigned sown = R4S + cw * 4096 + lane16; OPQV(sown);
#pragma unroll
        for (int g = 0; g < 4; ++g) *(LAS f32x4*)(L + sown + g * 1024) = f32x4{c1[4 * g], c1[4 * g + 1], c1[4 * g + 2], c1[4 * g + 3]};
        OPQV(c0);
        STAMP(14);
        BAR();
        STAMP(15);
#pragma unroll
        for (int g = 0; g < 4; ++g) { const f32x4 t = *(LAS f32x4*)(L + (sown ^ 4096u) + g * 1024); c0[4 * g] += t[0]; c0[4 * g + 1] += t[1]; c0[4 * g + 2] += t[2]; c0[4 * g + 3] += t[3]; }
        v4u y0, y1;
        pack_tile(c0, y0, y1);
        const unsigned xo = OFF_X1 + layer * (2 * MB) + xfo;
        st16(y0, rs, lane16, xo); st16(y1, rs, lane16 + 1024, xo);
        st8(tile_stats(c0), rs, h ? OOB : r * 8, OFF_ST + ((layer * 128 + tb2) * 16 + 2 * nblk + kh) * 256u);
        VMWAIT(0);
        set_flag(rs, (FL_X + layer * 128 + tb2) * 128u + (2 * nblk + kh) * 8, tg, 1u, lane);
      }
      {
        ROLES(); LANE_CONSTS();
        STAMP(16);
        v2u fpre;
        v4u ar[6];
        unsigned lhi = lane16 + R2; OPQV(lhi);
        unsigned lp0 = lane16 + kh * (16384 + 1024), lp1 = lp0 ^ 1024u;
        OPQV(lp0); OPQV(lp1);
        auto aread = [&](auto U) -> v4u {
          constexpr int u = U;
          if constexpr (u < 64) return lds16(L, ((u & 1) ? R1 : R0) + (u >> 1) * 1024 + lane16);
          else if constexpr (u < 224) {
            constexpr int T = 2 + (u - 64) / 32, s = (u - 64) % 32;
            if constexpr (T == 4 || T == 5) return lds16(L, (T - 4) * 32768 + s * 1024 + lane16);
            else return lds16(L, (T == 3 ? 32768 : 0) + s * 1024 + lhi);
          } else return lds16(L, R3 + ((u - 224) >> 1) * 2048 + ((u & 1) ? lp1 : lp0));
        };
        const unsigned augb = OFF_AUG + mem * 7 * 512u;
        v4u pg0 = ld16(rs, r * 16, augb), pg1 = ld16(rs, r * 16 + 512, augb);
        v2u agn = make_aug(pg0), ag1 = make_aug(pg1);
        OPQV(agn); OPQV(ag1);
        wait_flags(rs, (FL_X + layer * 128 + tb3) * 128u, tg, 1u, lane, 16);
        STAMP(17);
        v2u sp[8];
        const unsigned xb = OFF_X1 + layer * (2 * MB) + tb3 * 32768u;
        auto xload = [&](auto I) -> v4u { constexpr int i = I; return ld16(rs, lane16 + (i & 3) * 1024, xb + (i >> 2) * 4096u); };
        v4u x[32];
        sfor<0, D3>([&](auto I) { constexpr int i = I; x[i] = xload(I); });
        STAMP(23);
        BAR();
        STAMP(24);
        sfor<0, 6>([&](auto I) { constexpr int u = I; ar[u] = aread(I); });
        SB();
        const unsigned hb = OFF_H + layer * (7 * MB) + (tb3 * 112 + mem * 7) * 1024u;
        const unsigned fh_mine = (FL_H + (layer * 64 + tb3) * 2 + (mem >> 3)) * 128u + (mem & 7) * 8;
        const unsigned fh_poll = (FL_H + (layer * 64 + tb2) * 2 + kh) * 128u;
        Stats st;
        float cg, cu;
        f32x16 acc[3] = {};
        float ee[4], uu[4]; v4u hf;
        auto epi = [&](auto P_, auto E_, auto G_) {
          constexpr int p = P_, e = E_, g = G_;
          if constexpr (g == 0) { ee[e & 3] = ex2(acc[p % 3][e] * cg); OPQV(ee[e & 3]); }
          if constexpr (g == 1) { ee[e & 3] = __builtin_amdgcn_rcpf(1.0f + ee[e & 3]); uu[e & 3] = acc[p % 3][8 + e] * cu; OPQV(ee[e & 3]); OPQV(uu[e & 3]); }
          if constexpr (g == 2) {
            uu[e & 3] *= ee[e & 3]; OPQV(uu[e & 3]);
            if constexpr (e & 1) hf[e >> 1] = cvtpk(uu[(e - 1) & 3], uu[e & 3]);
            if constexpr (e == 7) st16(hf, rs, lane16 + (p & 3) * 1024, hb + (p >> 2) * 4096);
          }
        };
        SB();
        sfor<0, 224>([&](auto I) {
          constexpr int u = I;
          constexpr int T = u < 64 ? (u & 1) : 2 + (u - 64) / 32, s = u < 64 ? (u >> 1) : (u - 64) % 32;
          constexpr int left = u < 64 ? 64 - u : 32 - s;
          if constexpr (left == 4) {
            BAR();
            ar[(u + 4) % 6] = aread(IC(u + 4));
            ar[(u + 5) % 6] = aread(IC(u + 5));
          }
          if constexpr (u == 64) STAMP(18);
          if constexpr (u == 96) STAMP(19);
          if constexpr (u == 128) STAMP(20);
          if constexpr (u == 192) STAMP(21);
          if constexpr (u >= 64 && s == 0) { const f32x16 z = {}; acc[T % 3] = mfma8(agn, st.aug, z); }
          acc[T % 3] = mfma16(ar[u % 6], x[s], acc[T % 3]); OPQV(acc[T % 3]);
          if constexpr (left > 6 || left <= 4) ar[u % 6] = aread(IC(u + 6));
          if constexpr (u < 64 && !(u & 1) && s + D3 < 32) x[s + D3] = xload(IC(s + D3));
          if constexpr (u < 16 && (u & 1)) sp[s] = ld8(rs, h * 2048 + r * 8 + s * 256, OFF_ST + (layer * 128 + tb3) * 4096u);
          if constexpr (u == 31) { st = make_stats(sp, h); cg = st.rstd * -LOG2E; cu = st.rstd; OPQV(cg); OPQV(st.aug); }
          if constexpr (u == 36) { acc[1] = mfma8(ag1, st.aug, acc[1]); OPQV(acc[1]); }
          if constexpr (u == 37) { acc[0] = mfma8(agn, st.aug, acc[0]); OPQV(acc[0]); pg0 = ld16(rs, r * 16 + 1024, augb); }
          if constexpr (left == 5 && u < 200) { agn = make_aug(pg0); OPQV(agn); }
          if constexpr (left == 3 && u < 168) { constexpr int n2 = (u < 64 ? 3 : T + 2); pg0 = ld16(rs, r * 16 + (n2 & 1) * 512, augb + (n2 >> 1) * 1024); }
          if constexpr (T == 2 && u >= 64) {
            sfor<0, 16>([&](auto Q) { constexpr int q = Q, g = s - 2 - (3 * q) / 2; if constexpr (g >= 0 && g < 3) epi(IC(q / 8), IC(q % 8), IC(g)); });
          } else if constexpr (T > 2) {
            sfor<0, 8>([&](auto Q) { constexpr int q = Q, g = s - 2 - 3 * q; if constexpr (g >= 0 && g < 3) epi(IC(T - 1), Q, IC(g)); });
            if constexpr (s == 27 && T == 5) { VMWAIT(1); set_flag(rs, fh_mine, tg, 4u, lane); }
            if constexpr (s == 14 && T == 6) fpre = poll_ld(rs, lane < 8 ? lane * 8 : OOB, fh_poll);
            if constexpr (s == 27 && T == 6) { VMWAIT(1); set_flag(rs, fh_mine, tg, 5u, lane); }
          }
          SB();
        });
        STAMP(22);
        const unsigned xfo = (tb2 * 32 + 4 * nblk + 2 * kh) * 1024u;
        if (!flags_ok(fpre, tg, 4u, lane, 8)) wait_flags(rs, fh_poll, tg, 4u, lane, 8);
        const unsigned hb4 = OFF_H + layer * (7 * MB) + (tb2 * 112 + 56 * kh) * 1024u;
        v4u hr[D4];
        unsigned lane16b = lane16 + 4096; OPQV(lane16b);
        auto hload = [&](auto Q) {
          constexpr int q = Q, c = q >> 3;
          hr[q % D4] = ld16(rs, (c < 4 ? lane16 : lane16b) + (c & 3) * 1024, hb4 + (q & 7) * 7168u);
        };
        const unsigned xr1 = OFF_X1 + layer * (2 * MB) + xfo;
        const v4u r0 = ld16(rs, lane16, xr1), r1 = ld16(rs, lane16 + 1024, xr1);
        sfor<0, 6>(hload);
        SB();
        sfor<0, 10>([&](auto S_) { sfor<0, 8>([&](auto Q) { constexpr int g = S_ - Q; if constexpr (g >= 0 && g < 3) epi(IC(6), Q, IC(g)); }); });
        sfor<6, D4>(hload);
        f32x16 c0 = unpack_tile(r0, r1), c1 = {};
        unsigned lp0h = lp0 + R2, lp1h = lp1 + R2; OPQV(lp0h); OPQV(lp1h);
        auto dread = [&](auto U) -> v4u {
          constexpr int u = U, c = u / 16, i = (u % 16) >> 1;
          if constexpr (c == 0 || c == 4) return lds16(L, 32768 + i * 2048 + ((u & 1) ? lp1h : lp0h));
          else if constexpr (c == 3) return lds16(L, i * 2048 + ((u & 1) ? lp1h : lp0h));
          else return lds16(L, ((c == 2 || c == 6) ? 32768 : 0) + i * 2048 + ((u & 1) ? lp1 : lp0));
        };
        SB();
        sfor<0, 112>([&](auto I) {
          constexpr int u = I, c = u / 16, v = u % 16, q = u >> 1;
          if constexpr (v == 12 && c < 6) {
            BAR();
            ar[(224 + u + 4) % 6] = dread(IC(u + 4));
            ar[(224 + u + 5) % 6] = dread(IC(u + 5));
          }
          constexpr int U5 = 2 * (32 - D4) - 1, U7 = 2 * (40 - D4) - 1;
          if constexpr (u == U5 - 15 || u == U7 - 15) fpre = poll_ld(rs, lane < 8 ? lane * 8 : OOB, fh_poll);
          if constexpr (u == 16) STAMP(25);
          if constexpr (u == 48) STAMP(26);
          if constexpr (u == 80) STAMP(27);
          if constexpr (u & 1) { c1 = mfma16(ar[(224 + u) % 6], hr[q % D4], c1); OPQV(c1); } else { c0 = mfma16(ar[(224 + u) % 6], hr[q % D4], c0); OPQV(c0); }
          if constexpr ((v < 10 || v >= 12) && u + 6 < 112) ar[(224 + u) % 6] = dread(IC(u + 6));
          if constexpr (u == 11) { asm volatile("s_waitcnt vmcnt(%0)" :: "i"(D4 - 6 + 5) : "memory"); set_flag(rs, fh_mine, tg, 7u, lane); }
          if constexpr (u == U5) { if (!flags_ok(fpre, tg, 5u, lane, 8)) wait_flags(rs, fh_poll, tg, 5u, lane, 8); }
          if constexpr (u == U7) { if (!flags_ok(fpre, tg, 7u, lane, 8)) wait_flags(rs, fh_poll, tg, 7u, lane, 8); }
          if constexpr ((u & 1) && q + D4 < 56) hload(IC(q + D4));
          SB();
        });
        unsigned sown = R4S + cw * 4096 + lane16; OPQV(sown);
#pragma unroll
        for (int g = 0; g < 4; ++g) *(LAS f32x4*)(L + sown + g * 1024) = f32x4{c1[4 * g], c1[4 * g + 1], c1[4 * g + 2], c1[4 * g + 3]};
        OPQV(c0);
        STAMP(28);
        BAR();
        STAMP(29);
#pragma unroll
        for (int g = 0; g < 4; ++g) { const f32x4 t = *(LAS f32x4*)(L + (sown ^ 4096u) + g * 1024); c0[4 * g] += t[0]; c0[4 * g + 1] += t[1]; c0[4 * g + 2] += t[2]; c0[4 * g + 3] += t[3]; }
        v4u y0, y1;
        pack_tile(c0, y0, y1);
        if (layer == 0) {
          st16(y0, rs, lane16, OFF_X2 + xfo); st16(y1, rs, lane16 + 1024, OFF_X2 + xfo);
          st8(tile_stats(c0), rs, h ? OOB : r * 8, OFF_ST + ((64 + tb2) * 16 + 2 * nblk + kh) * 256u);
          VMWAIT(0);
          set_flag(rs, (FL_X + 64 + tb2) * 128u + (2 * nblk + kh) * 8, tg, 1u, lane);
        } else {
          const rsrc_t ro = __builtin_amdgcn_make_buffer_rsrc((void*)kargs()->out, 0, SEQ * DM * 2, 0x00020000);
          const unsigned a0 = y0.x, a1 = y0.y, a2 = y0.z, a3 = y0.w, b0 = y1.x, b1 = y1.y, b2 = y1.z, b3 = y1.w;
          const auto p0 = __builtin_amdgcn_permlane32_swap(a0, a2, false, false), p1 = __builtin_amdgcn_permlane32_swap(a1, a3, false, false);
          const auto p2 = __builtin_amdgcn_permlane32_swap(b0, b2, false, false), p3 = __builtin_amdgcn_permlane32_swap(b1, b3, false, false);
          const unsigned vo = r * 1024 + h * 16, so = tb2 * 32768u + nblk * 128u + kh * 64u;
          const v4u z0 = {p0[0], p1[0], p0[1], p1[1]}, z1 = {p2[0], p3[0], p2[1], p3[1]};
          __builtin_amdgcn_raw_buffer_store_b128(z0, ro, vo, so, OUTAUX); __builtin_amdgcn_raw_buffer_store_b128(z1, ro, vo + 32, so, OUTAUX);
          asm volatile("s_nop 1" :: "v"(z0), "v"(z1) : "memory");
        }
        STAMP(30);
      }
    }
  }
  STAMP_OUT();
}

extern "C" void kernel_launch(void* const* d_in, const int* in_sizes, int n_in, void* d_out, int out_size, void* d_ws, size_t ws_size,
                              hipStream_t stream) {
  Args a;
  a.ws = (unsigned char*)d_ws; a.out = (u16*)d_out;
  a.x = (const u16*)d_in[0]; a.wqkv = (const u16*)d_in[1]; a.wo = (const u16*)d_in[2];
  a.wg = (const u16*)d_in[4]; a.wu = (const u16*)d_in[5]; a.wd = (const u16*)d_in[6];
  (void)hipFuncSetAttribute((const void*)main_k, hipFuncAttributeMaxDynamicSharedMemorySize, LDS_BYTES);
  main_k<<<256, 512, LDS_BYTES, stream>>>(a);
}
```

```cpp
#include <hip/hip_runtime.h>
#include <stdint.h>
#include <type_traits>

typedef unsigned short u16;
typedef float f32x16 __attribute__((ext_vector_type(16)));
typedef float f32x4 __attribute__((ext_vector_type(4)));
typedef float f32x2 __attribute__((ext_vector_type(2)));
typedef __bf16 bf16x8 __attribute__((ext_vector_type(8)));
typedef __bf16 bf16x2 __attribute__((ext_vector_type(2)));
typedef short s16x4 __attribute__((ext_vector_type(4)));
typedef unsigned v4u __attribute__((ext_vector_type(4)));
typedef unsigned v2u __attribute__((ext_vector_type(2)));
#define GAS __attribute__((address_space(1)))
#define LAS __attribute__((address_space(3)))
#define DI __device__ __forceinline__
#define RLX_AGENT __ATOMIC_RELAXED, __HIP_MEMORY_SCOPE_AGENT
#define IC(n) std::integral_constant<int, (n)>{}
typedef __amdgpu_buffer_rsrc_t rsrc_t;

constexpr int SEQ = 2048, DM = 512, FF = 1792;
constexpr unsigned KB = 1024u, MB = 1u << 20;
constexpr unsigned FL_EPOCH = 0;
constexpr unsigned FL_SEEN = 2;
constexpr unsigned FL_P = 32;
constexpr unsigned FL_Q = FL_P + 44;
constexpr unsigned FL_X = FL_Q + 32;
constexpr unsigned FL_O = FL_X + 3 * 64;
constexpr unsigned FL_H = FL_O + 2 * 64 * 2;
constexpr unsigned FL_KV = FL_H + 2 * 64 * 2;
constexpr unsigned FL_LINES = FL_KV + 2 * 32 * 8;
static_assert(FL_LINES * 128 <= 256 * KB, "flags");
constexpr unsigned OFF_AUG = 256 * KB;
constexpr unsigned OFF_ST0 = 384 * KB;
constexpr unsigned OFF_CSQ = 512 * KB;
constexpr unsigned OFF_WQK = 1 * MB, OFF_WV = 2 * MB, OFF_WO = 2 * MB + 512 * KB, OFF_WGU = 3 * MB, OFF_WD = 6 * MB + 512 * KB;
constexpr unsigned OFF_ST = 9 * MB;
constexpr unsigned OFF_X1 = 11 * MB, OFF_X2 = 15 * MB, OFF_O = 17 * MB, OFF_KV = 21 * MB, OFF_H = 29 * MB;
constexpr unsigned WS_BYTES = 43 * MB;
constexpr unsigned OOB = 0x40000000u;
constexpr unsigned R0 = 0, R1 = 32 * KB, R2 = 64 * KB, R3 = 96 * KB, R4V = 128 * KB, R4S = 144 * KB, LDS_BYTES = 160 * KB;
constexpr float NEG_BIG = -1.0e30f;
constexpr float LOG2E = 1.4426950408889634f;
constexpr float SAFE2 = 2500.0f;
constexpr unsigned SPIN_CAP = 1u << 18;
#define D1V 6
#define D2V 8
#define D3V 8
#define D4V 10
#define OUTAUX 0
#define TLBN 0
#define TLBW 1
#define TLBSTEP 0
constexpr int D1 = D1V, D2 = D2V, D3 = D3V, D4 = D4V;

template <int B, int E, class F> DI void sfor(F&& f) {
  if constexpr (B < E) { f(std::integral_constant<int, B>{}); sfor<B + 1, E>(f); }
}
DI bf16x8 as_bf8(v4u v) { return __builtin_bit_cast(bf16x8, v); }
DI bf16x2 as_bf2(unsigned v) { return __builtin_bit_cast(bf16x2, v); }
DI s16x4 as_s4(v2u v) { return __builtin_bit_cast(s16x4, v); }
DI f32x16 mfma16(v4u a, v4u b, f32x16 c) { return __builtin_amdgcn_mfma_f32_32x32x16_bf16(as_bf8(a), as_bf8(b), c, 0, 0, 0); }
DI f32x16 mfma8(v2u a, v2u b, f32x16 c) { return __builtin_amdgcn_mfma_f32_32x32x8bf16_1k(as_s4(a), as_s4(b), c, 0, 0, 0); }
DI f32x4 mfma16s(v4u a, v4u b, f32x4 c) { return __builtin_amdgcn_mfma_f32_16x16x32_bf16(as_bf8(a), as_bf8(b), c, 0, 0, 0); }
DI unsigned cvtpk(float lo, float hi) { f32x2 f = {lo, hi}; bf16x2 p = __builtin_convertvector(f, bf16x2); return __builtin_bit_cast(unsigned, p); }
DI float bflo(unsigned v) { return __uint_as_float(v << 16); }
DI float bfhi(unsigned v) { return __uint_as_float(v & 0xffff0000u); }
DI float dot2(unsigned a, unsigned b, float c) { return __builtin_amdgcn_fdot2_f32_bf16(as_bf2(a), as_bf2(b), c, false); }
DI float ex2(float v) { return __builtin_amdgcn_exp2f(v); }
DI float xhalf_add(float v) { auto r = __builtin_amdgcn_permlane32_swap(__float_as_uint(v), __float_as_uint(v), false, false); return __uint_as_float(r[0]) + __uint_as_float(r[1]); }
DI float xhalf_max(float v) { auto r = __builtin_amdgcn_permlane32_swap(__float_as_uint(v), __float_as_uint(v), false, false); return fmaxf(__uint_as_float(r[0]), __uint_as_float(r[1])); }
DI unsigned umax(unsigned a, unsigned b) { return a > b ? a : b; }
DI unsigned row_umax(unsigned v) {
  v = umax(v, (unsigned)__builtin_amdgcn_update_dpp(0, (int)v, 0x121, 0xf, 0xf, true));
  v = umax(v, (unsigned)__builtin_amdgcn_update_dpp(0, (int)v, 0x122, 0xf, 0xf, true));
  v = umax(v, (unsigned)__builtin_amdgcn_update_dpp(0, (int)v, 0x124, 0xf, 0xf, true));
  v = umax(v, (unsigned)__builtin_amdgcn_update_dpp(0, (int)v, 0x128, 0xf, 0xf, true));
  return v;
}
DI v4u ld16(rsrc_t rs, unsigned voff, unsigned soff) { return __builtin_amdgcn_raw_buffer_load_b128(rs, voff, soff, 16); }
DI v4u ld16p(rsrc_t rs, unsigned voff, unsigned soff) { return __builtin_amdgcn_raw_buffer_load_b128(rs, voff, soff, 0); }
DI v2u ld8p(rsrc_t rs, unsigned voff, unsigned soff) { return __builtin_bit_cast(v2u, __builtin_amdgcn_raw_buffer_load_b64(rs, voff, soff, 0)); }
DI v2u ld8(rsrc_t rs, unsigned voff, unsigned soff) { return __builtin_bit_cast(v2u, __builtin_amdgcn_raw_buffer_load_b64(rs, voff, soff, 16)); }
DI void st16(v4u v, rsrc_t rs, unsigned voff, unsigned soff) {
  __builtin_amdgcn_raw_buffer_store_b128(v, rs, voff, soff, 16);
  asm volatile("s_nop 1" :: "v"(v) : "memory");
}
DI void st8(v2u v, rsrc_t rs, unsigned voff, unsigned soff) { __builtin_amdgcn_raw_buffer_store_b64(v, rs, voff, soff, 16); }
DI v4u lds16(LAS unsigned char* L, unsigned off) { return *(LAS v4u*)(L + off); }
#define SB() __builtin_amdgcn_sched_barrier(0)
#define OPQV(x) asm volatile("" : "+v"(x))
#define OPQS(x) asm volatile("" : "+s"(x))
#define LANE_CONSTS() int lane = threadIdx.x & 63; OPQV(lane); const int r = lane & 31, h = lane >> 5; const unsigned lane16 = lane * 16; (void)r; (void)h
#define BAR() asm volatile("s_waitcnt lgkmcnt(0)\n\ts_barrier" ::: "memory")
#define VMWAIT(n) asm volatile("s_waitcnt vmcnt(" #n ")" ::: "memory")
#define DMA_HEAD "s_nop 4\n\ts_mov_b32 m0, %0\n\ts_nop 0\n\t"
#define DMA_LD(o) "buffer_load_dwordx4 %1, %2, %3 offen offset:" #o " sc1 lds\n\t"
#define DMA_ARGS :: "s"(lds), "v"(voff), "s"(rs), "s"(soff) : "memory"
DI void dma4(rsrc_t rs, unsigned lds, unsigned soff, unsigned voff) { asm volatile(DMA_HEAD DMA_LD(0) DMA_LD(1024) DMA_LD(2048) DMA_LD(3072) DMA_ARGS); }
DI void dma1p(rsrc_t rs, unsigned lds, unsigned soff, unsigned voff) { asm volatile(DMA_HEAD "buffer_load_dwordx4 %1, %2, %3 offen lds" DMA_ARGS); }
#define PACE 3
#define STR2(x) #x
#define STR(x) STR2(x)
#define DMA_W "s_waitcnt vmcnt(" STR(PACE) ")\n\t"
DI void dma4_paced(rsrc_t rs, unsigned lds, unsigned soff, unsigned voff) {
  asm volatile(DMA_HEAD DMA_LD(0) DMA_W DMA_LD(1024) DMA_W DMA_LD(2048) DMA_W DMA_LD(3072) DMA_W DMA_ARGS);
}
DI void dma_tile(rsrc_t rs, unsigned lds, unsigned soff, unsigned lw, unsigned lane16) {
  dma4(rs, lds + lw * 8192u, soff + lw * 8192u, lane16);
  dma4(rs, lds + lw * 8192u + 4096u, soff + lw * 8192u + 4096u, lane16);
}
extern "C" __device__ long long wsatomic_add64(long long, rsrc_t, int, int, int) __asm("llvm.amdgcn.raw.ptr.buffer.atomic.add.i64");
struct Tag { unsigned lo, hi; };
DI v2u poll_ld(rsrc_t rs, unsigned voff, unsigned soff) {
  long long z = 0; OPQV(z);
  return __builtin_bit_cast(v2u, wsatomic_add64(z, rs, voff, soff, 0));
}
DI bool word_ok(v2u f, Tag t, unsigned target) { const unsigned a = f.x, b = f.y; return b == t.hi && (a - t.lo - target) <= 0xffffu - target; }
DI bool flags_ok(v2u f, Tag t, unsigned target, int lane, int n) { return __all(word_ok(f, t, target) || lane >= n); }
DI v2u wait_words(rsrc_t rs, unsigned vo, unsigned soff, Tag t, unsigned target, bool act) {
  v2u f = {0u, 0u};
#pragma nounroll
  for (unsigned spins = 0; spins < SPIN_CAP; ++spins) {
    f = poll_ld(rs, vo, soff);
    if (__all(word_ok(f, t, target) || !act)) break;
  }
  asm volatile("" ::: "memory");
  return f;
}
DI v2u wait_flags(rsrc_t rs, unsigned soff, Tag t, unsigned target, int lane, int n) {
  return wait_words(rs, lane < n ? lane * 8 : OOB, soff, t, target, lane < n);
}
DI void swap64(rsrc_t rs, unsigned voff, unsigned soff, v2u v) {
  asm volatile("s_nop 4\n\tbuffer_atomic_swap_x2 %0, %1, %2, %3 offen\n\ts_nop 1" :: "v"(v), "v"(voff), "s"(rs), "s"(soff) : "memory");
}
DI void set_flag(rsrc_t rs, unsigned off, Tag t, unsigned payload, int lane) { swap64(rs, lane == 0 ? 0u : OOB, off, v2u{t.lo | payload, t.hi}); }

struct Stats { float rstd, mean; v2u aug; };
DI Stats finish_stats(float s1, float s2, int h) {
  const float mean = s1 * (1.0f / DM);
  const float var = fmaxf(s2 * (1.0f / DM) - mean * mean, 0.0f);
  Stats st; st.rstd = __builtin_amdgcn_rsqf(var + 1e-5f); st.mean = mean;
  const float nm = h ? 0.0f : -mean;
  const unsigned hi = cvtpk(nm, nm) & 0xffffu;
  const float lo = nm - __uint_as_float(hi << 16);
  const unsigned w = hi | (cvtpk(lo, lo) << 16);
  st.aug.x = w; st.aug.y = w;
  return st;
}
DI Stats make_stats(const v2u (&sp)[8], int h) {
  float s1 = 0.f, s2 = 0.f;
#pragma unroll
  for (int i = 0; i < 8; ++i) { const unsigned a = sp[i].x, b = sp[i].y; s1 += __uint_as_float(a); s2 += __uint_as_float(b); }
  return finish_stats(xhalf_add(s1), xhalf_add(s2), h);
}
DI float xrow_add(float v) { auto r = __builtin_amdgcn_permlane16_swap(__float_as_uint(v), __float_as_uint(v), false, false); return __uint_as_float(r[0]) + __uint_as_float(r[1]); }
DI float wave_add(float v) {
  v += __int_as_float(__builtin_amdgcn_update_dpp(0, __float_as_int(v), 0x121, 0xf, 0xf, true));
  v += __int_as_float(__builtin_amdgcn_update_dpp(0, __float_as_int(v), 0x122, 0xf, 0xf, true));
  v += __int_as_float(__builtin_amdgcn_update_dpp(0, __float_as_int(v), 0x124, 0xf, 0xf, true));
  v += __int_as_float(__builtin_amdgcn_update_dpp(0, __float_as_int(v), 0x128, 0xf, 0xf, true));
  return xhalf_add(xrow_add(v));
}
DI v2u trd(LAS unsigned char* L, unsigned off) { return __builtin_bit_cast(v2u, __builtin_amdgcn_ds_read_tr16_b64_v4i16((LAS s16x4*)(L + off))); }
DI v4u trfrag(LAS unsigned char* L, unsigned off, unsigned second) { const v2u a = trd(L, off), b = trd(L, off + second); return v4u{a.x, a.y, b.x, b.y}; }
DI float sum8(v4u f, float c) {
  const unsigned a = f.x, b = f.y, d = f.z, e = f.w;
  return dot2(a, 0x3f803f80u, dot2(b, 0x3f803f80u, dot2(d, 0x3f803f80u, dot2(e, 0x3f803f80u, c))));
}
DI v2u split_aug(float cs) {
  const unsigned hi = cvtpk(cs, cs);
  const float lo = cs - __uint_as_float(hi << 16);
  return v2u{hi, cvtpk(lo, lo)};
}
DI float sum4(v4u p) {
  const unsigned a = p.x, b = p.y, c = p.z, d = p.w;
  return (__uint_as_float(a) + __uint_as_float(b)) + (__uint_as_float(c) + __uint_as_float(d));
}
DI void swap16(v4u& a, v4u& b) {
#pragma unroll
  for (int i = 0; i < 4; ++i) { const unsigned x = a[i], y = b[i]; const auto t = __builtin_amdgcn_permlane16_swap(x, y, false, false); a[i] = t[0]; b[i] = t[1]; }
}
DI v2u make_aug(v4u p) { return split_aug(sum4(p)); }

DI float max16(const f32x16& s, float m) {
  m = fmaxf(fmaxf(m, s[0]), s[1]);   m = fmaxf(fmaxf(m, s[2]), s[3]);
  float n = fmaxf(s[4], s[5]);       n = fmaxf(fmaxf(n, s[6]), s[7]);
  m = fmaxf(fmaxf(m, s[8]), s[9]);   n = fmaxf(fmaxf(n, s[10]), s[11]);
  m = fmaxf(fmaxf(m, s[12]), s[13]); n = fmaxf(fmaxf(n, s[14]), s[15]);
  return fmaxf(m, n);
}
DI void exp_pv(const f32x16& s, v4u v0, v4u v1, f32x16& o) {
  v4u p0, p1;
  p0.x = cvtpk(ex2(s[0]), ex2(s[1])); p0.y = cvtpk(ex2(s[2]), ex2(s[3])); p0.z = cvtpk(ex2(s[4]), ex2(s[5])); p0.w = cvtpk(ex2(s[6]), ex2(s[7]));
  o = mfma16(v0, p0, o);
  p1.x = cvtpk(ex2(s[8]), ex2(s[9])); p1.y = cvtpk(ex2(s[10]), ex2(s[11])); p1.z = cvtpk(ex2(s[12]), ex2(s[13])); p1.w = cvtpk(ex2(s[14]), ex2(s[15]));
  o = mfma16(v1, p1, o);
}
DI void attn_general(LAS unsigned char* L, v4u q, const f32x16& cm, int bm, int bf0, int nf, unsigned kaddr, unsigned vbase, unsigned vstep,
                     float& m, f32x16& o) {
  const f32x16 zero = {};
  float mx = max16(mfma16(lds16(L, kaddr + bm * 2048), q, cm), m);
#pragma nounroll
  for (int b = 0; b < nf; ++b) mx = max16(mfma16(lds16(L, kaddr + (bf0 + b) * 2048), q, zero), mx);
  mx = xhalf_max(mx);
  const float f = ex2(m - mx);
#pragma unroll
  for (int i = 0; i < 16; ++i) o[i] *= f;
  m = mx;
  f32x16 c;
#pragma unroll
  for (int i = 0; i < 16; ++i) c[i] = cm[i] - mx;
  unsigned va = vbase + vstep * bm;
  exp_pv(mfma16(lds16(L, kaddr + bm * 2048), q, c), lds16(L, va), lds16(L, va + 512), o);
#pragma unroll
  for (int i = 0; i < 16; ++i) c[i] = -mx;
#pragma nounroll
  for (int b = 0; b < nf; ++b) {
    va = vbase + vstep * (bf0 + b);
    exp_pv(mfma16(lds16(L, kaddr + (bf0 + b) * 2048), q, c), lds16(L, va), lds16(L, va + 512), o);
  }
}
template <int K, int S0, int DIR, class H>
DI void attn_chain(LAS unsigned char* L, v4u q, int n, unsigned kaddr, unsigned va, unsigned vstep, const f32x16& s, v4u v0, v4u v1, v4u kn,
                   f32x16& o, H&& hook) {
  if constexpr (K == 7) { hook(IC(K)); exp_pv(s, v0, v1, o); }
  else {
    const f32x16 zero = {};
    f32x16 sn = mfma16(kn, q, zero);
    v4u w0 = lds16(L, va), w1 = lds16(L, va + 512);
    if constexpr (K < 6) kn = lds16(L, kaddr + (S0 + DIR * (K + 1)) * 2048);
    hook(IC(K));
    SB();
    exp_pv(s, v0, v1, o);
    asm volatile("" : "+v"(sn), "+v"(w0), "+v"(w1), "+v"(kn), "+v"(o));
    if (n > K) attn_chain<K + 1, S0, DIR>(L, q, n, kaddr, DIR > 0 ? va + vstep : va - vstep, vstep, sn, w0, w1, kn, o, hook);
  }
}
template <int S0, int DIR, class H>
DI void attn_bounded(LAS unsigned char* L, v4u q, const f32x16& cm, int bm, int n, unsigned kaddr, unsigned vbase, unsigned vstep, f32x16& o, H&& hook) {
  const unsigned vm = vbase + vstep * bm;
  const v4u km = lds16(L, kaddr + bm * 2048), kn = lds16(L, kaddr + S0 * 2048), v0 = lds16(L, vm), v1 = lds16(L, vm + 512);
  attn_chain<0, S0, DIR>(L, q, n, kaddr, vbase + vstep * S0, vstep, mfma16(km, q, cm), v0, v1, kn, o, hook);
}
template <int S0, class H>
DI void attn_full(LAS unsigned char* L, v4u q, int n, unsigned kaddr, unsigned vbase, unsigned vstep, f32x16& o, H&& hook) {
  const f32x16 zero = {};
  const unsigned va = vbase + vstep * S0;
  const v4u k0 = lds16(L, kaddr + S0 * 2048), kn = lds16(L, kaddr + (S0 + 1) * 2048), v0 = lds16(L, va), v1 = lds16(L, va + 512);
  attn_chain<1, S0, 1>(L, q, n, kaddr, va + vstep, vstep, mfma16(k0, q, zero), v0, v1, kn, o, hook);
}

DI f32x16 unpack_tile(v4u r0, v4u r1) {
  f32x16 k;
  k[0] = bflo(r0.x); k[1] = bfhi(r0.x); k[2] = bflo(r0.y); k[3] = bfhi(r0.y); k[4] = bflo(r0.z); k[5] = bfhi(r0.z); k[6] = bflo(r0.w); k[7] = bfhi(r0.w);
  k[8] = bflo(r1.x); k[9] = bfhi(r1.x); k[10] = bflo(r1.y); k[11] = bfhi(r1.y); k[12] = bflo(r1.z); k[13] = bfhi(r1.z); k[14] = bflo(r1.w); k[15] = bfhi(r1.w);
  return k;
}
DI void pack_tile(const f32x16& k, v4u& y0, v4u& y1) {
  y0.x = cvtpk(k[0], k[1]); y0.y = cvtpk(k[2], k[3]); y0.z = cvtpk(k[4], k[5]); y0.w = cvtpk(k[6], k[7]);
  y1.x = cvtpk(k[8], k[9]); y1.y = cvtpk(k[10], k[11]); y1.z = cvtpk(k[12], k[13]); y1.w = cvtpk(k[14], k[15]);
}
DI v2u tile_stats(const f32x16& k) {
  f32x2 a = {k[0], k[1]}, b = a * a;
#pragma unroll
  for (int i = 1; i < 8; ++i) { const f32x2 t = {k[2 * i], k[2 * i + 1]}; a += t; b += t * t; }
  return v2u{__float_as_uint(xhalf_add(a[0] + a[1])), __float_as_uint(xhalf_add(b[0] + b[1]))};
}

struct Args { unsigned char* ws; u16* out; const u16 *x, *wqkv, *wo, *wg, *wu, *wd; };
#define STAMP(id) do {} while (0)
#define STAMP_DECL() do {} while (0)
#define STAMP_OUT() do {} while (0)

typedef const __attribute__((address_space(4))) Args* kargs_t;
DI kargs_t kargs() { kargs_t p = (kargs_t)__builtin_amdgcn_kernarg_segment_ptr(); OPQS(p); return p; }
__global__ void __launch_bounds__(512, 2) main_k(Args a) {
  extern __shared__ __attribute__((aligned(16))) unsigned char smem[];
  LAS unsigned char* L = (LAS unsigned char*)smem;
  const unsigned lb = (unsigned)(size_t)L;
  const int w0 = __builtin_amdgcn_readfirstlane(threadIdx.x >> 6);
  STAMP_DECL();
  const int xcd = blockIdx.x & 7, idx = blockIdx.x >> 3;
  const int sg0 = (xcd & ~1) | (idx >> 4), i4 = idx & 15, j0 = ((i4 >> 2) << 3) | ((xcd & 1) << 2) | (i4 & 3);
  const rsrc_t rs = __builtin_amdgcn_make_buffer_rsrc(a.ws, 0, WS_BYTES, 0x00020000);
#define ROLES() int w = w0, sg = sg0, j = j0, layer = layer0; OPQS(w); OPQS(sg); OPQS(j); OPQS(layer); Tag tg = tg0; OPQS(tg.lo); OPQS(tg.hi); \
  const int head = j, tbl = w < 4 ? w : 11 - w, Tq = 8 * sg + tbl;              \
  const int grp = 2 * sg + (j >> 4), mem = j & 15, nblk = j & 7;                \
  const int cw = w & 3, th = cw >> 1, kh = cw & 1; \
  const int tb2 = 4 * grp + 2 * ((j >> 3) & 1) + th, tb3 = 4 * grp + cw;        \
  (void)head; (void)Tq; (void)mem; (void)nblk; (void)kh; (void)tb2; (void)tb3
#define XROUND(rho) do { _Pragma("unroll") for (int p_ = 0; p_ < 4; ++p_) \
    dma1p(rx, lb + R2 + w * 8192u + ((rho) & 1) * 4096u + p_ * 1024u, (Tq * 32 + 8 * p_) * 1024u + (((rho) + w + j) & 7) * 128u, (p_ & 1) ? (xve ^ 64u) : xve); } while (0)
  Tag tg0 = {0u, 0u};
  {
    const int layer0 = 0;
    STAMP(31);
    ROLES(); LANE_CONSTS();
    const rsrc_t rx = __builtin_amdgcn_make_buffer_rsrc((void*)kargs()->x, 0, SEQ * DM * 2, 0x00020000);
    const rsrc_t rw = __builtin_amdgcn_make_buffer_rsrc((void*)kargs()->wqkv, 0, DM * 3072, 0x00020000);
    const unsigned token = 256 * sg + 8 * j + w;
    v2u ep; v4u row;
    asm volatile("s_nop 4\n\tbuffer_load_dwordx2 %0, off, %2, 0 sc1\n\tbuffer_load_dwordx4 %1, %3, %4, %5 offen"
                 : "=&v"(ep), "=&v"(row) : "s"(rs), "v"(lane16), "s"(rx), "s"(token * 1024u) : "memory");
    const unsigned vq = (lane >> 2) * 3072 + (lane & 1) * 16 + ((lane >> 1) & 1) * 1024 + head * 32;
    const unsigned vv = (lane >> 1) * 3072 + (lane & 1) * 16 + 2048 + head * 32;
#pragma unroll
    for (int i = 0; i < 4; ++i) dma1p(rw, lb + R0 + (4 * w + i) * 1024u, (4 * w + i) * 16u * 3072u, vq);
#pragma unroll
    for (int i = 0; i < 2; ++i) dma1p(rw, lb + R4V + (2 * w + i) * 1024u, (2 * w + i) * 32u * 3072u, vv);
    const unsigned xve = (lane >> 3) * 1024 + (((lane & 7) ^ (lane >> 4)) * 16);
    XROUND(0); XROUND(1);
    asm volatile("s_waitcnt vmcnt(14)" : "+v"(ep), "+v"(row) :: "memory");
    const unsigned e0 = __builtin_amdgcn_readfirstlane(ep.x) & 0xffff0000u, e1 = __builtin_amdgcn_readfirstlane(ep.y);
    tg.lo = e0 + 0x10000u; tg.hi = e1 + (tg.lo == 0u ? 1u : 0u);
    tg0 = tg;
    const unsigned a0 = row.x, a1 = row.y, a2 = row.z, a3 = row.w;
    const float s1 = wave_add(sum8(row, 0.f)), s2 = wave_add(dot2(a0, a0, dot2(a1, a1, dot2(a2, a2, dot2(a3, a3, 0.f)))));
    swap64(rs, lane < 2 ? lane * 8 : OOB, OFF_ST0 + token * 16u, v2u{__float_as_uint(lane ? s2 : s1), (tg.lo >> 16) | (tg.hi << 16)});
    swap64(rs, (w | lane) == 0 ? 0u : OOB, FL_SEEN * 128u + blockIdx.x * 8u, v2u{tg.lo | 1u, tg.hi});
  }

  for (int layer0 = 0; layer0 < 2; ++layer0) {
    {
      ROLES(); LANE_CONSTS();
      STAMP(0);
      const unsigned own = R1 + (8 + tbl) * 2048;
      f32x16 cqk = {};
      f32x4 cva = {}, cvb = {};
      Stats st; float csv;
      f32x16 cm;
      f32x4 rra, rrb, mma, mmb;
      auto mask2 = [&](auto I) {
        constexpr int i = I;
        float a0 = ((i & 3) + 8 * (i >> 2) + 4 * h <= r) ? 0.0f : NEG_BIG, a1 = (((i + 1) & 3) + 8 * (i >> 2) + 4 * h <= r) ? 0.0f : NEG_BIG;
        OPQV(a0); OPQV(a1); cm[i] = a0; cm[i + 1] = a1;
      };
      auto bounce_rd = [&] {
        const unsigned ba = own + 1024 + (lane >> 4) * 16;
        rra = *(LAS f32x4*)(L + ba); rrb = *(LAS f32x4*)(L + ba + 64); mma = *(LAS f32x4*)(L + ba + 128); mmb = *(LAS f32x4*)(L + ba + 192);
      };
      if (layer == 0) {
        const rsrc_t rx = __builtin_amdgcn_make_buffer_rsrc((void*)kargs()->x, 0, SEQ * DM * 2, 0x00020000);
        const int g = lane >> 4, q = (lane & 15) >> 2, pp = lane & 3;
        VMWAIT(10);
        BAR();
        STAMP(2);
        { unsigned o2 = 0x3f803f80u; OPQV(o2); *(LAS v4u*)(L + R4S + (lane & 1) * 512) = v4u{o2, o2, o2, o2}; }
        {
          const unsigned tq = R0 + (64 * w + 4 * (g >> 1) + q) * 64 + (g & 1) * 32 + pp * 8;
          const unsigned tv = R4V + (64 * w + 16 * (g >> 1) + 4 * (g & 1) + q) * 32 + pp * 8;
          const unsigned so = w == sg ? lane16 : OOB;
          float cq = 0.f, cvp = 0.f;
#pragma unroll
          for (int i = 0; i < 4; ++i) { const v4u f = trfrag(L, tq + i * 1024, 512); cq = sum8(f, cq); st16(f, rs, so + i * 1024, OFF_WQK + (head * 32 + 4 * w) * 1024u); }
#pragma unroll
          for (int i = 0; i < 2; ++i) { const v4u f = trfrag(L, tv + i * 1024, 256); cvp = sum8(f, cvp); st16(f, rs, so + i * 1024, OFF_WV + head * 16384u + 4 * w * 512u); }
          *(LAS f32x2*)(L + R4S + 2048 + w * 512 + lane * 8) = f32x2{cq, cvp};
          *(LAS v2u*)(L + R4S + 1536) = v2u{0u, 0u};
        }
        BAR();
        f32x2 cs = {0.f, 0.f};
#pragma unroll
        for (int i = 0; i < 8; ++i) cs += *(LAS f32x2*)(L + R4S + 2048 + i * 512 + lane * 8);
        const v2u aqk = split_aug(xhalf_add(cs[0]));
        csv = xhalf_add(xrow_add(cs[1]));
        st16(v4u{aqk.x, aqk.y, __float_as_uint(csv), 0u}, rs, w == 0 ? lane16 : OOB, OFF_CSQ + (sg * 32 + j) * 1024u);
        sfor<0, 8>([&](auto I) { mask2(IC(2 * I)); });
        const unsigned xve = (lane >> 3) * 1024 + (((lane & 7) ^ (lane >> 4)) * 16);
        unsigned xa[4];
#pragma unroll
        for (int i = 0; i < 4; ++i) { xa[i] = R2 + w * 8192 + (r >> 3) * 1024 + (r & 7) * 128 + (((2 * i + h) ^ ((r >> 1) & 7)) << 4); OPQV(xa[i]); }
        unsigned tq = R0 + (8 * (g >> 1) + q) * 64 + (g & 1) * 32 + pp * 8, tv = R4V + (16 * (g & 1) + 8 * (g >> 1) + q) * 32 + pp * 8;
        OPQV(tq); OPQV(tv);
        v2u sgr = {0u, 0u};
        SB();
        sfor<0, 8>([&](auto RHO) {
          constexpr int rho = RHO;
          v4u wq[4], wv[2], xf[4];
          const unsigned kr = (rho + w + j) & 7;
#pragma unroll
          for (int i = 0; i < 4; ++i) wq[i] = trfrag(L, tq + kr * 4096 + i * 1024, 256);
#pragma unroll
          for (int i = 0; i < 2; ++i) wv[i] = trfrag(L, tv + kr * 2048 + i * 1024, 128);
          if constexpr (rho < 2) VMWAIT(13);
          else if constexpr (rho == 4) VMWAIT(5);
          else if constexpr (rho == 5) asm volatile("s_waitcnt vmcnt(4)" : "+v"(sgr) :: "memory");
          else if constexpr (rho == 6) asm volatile("s_waitcnt vmcnt(%0)" :: "i"(4 + TLBN) : "memory");
          else if constexpr (rho == 7) asm volatile("s_waitcnt vmcnt(%0)" :: "i"(TLBN) : "memory");
          else VMWAIT(4);
#pragma unroll
          for (int i = 0; i < 4; ++i) xf[i] = lds16(L, xa[i] + (rho & 1) * 4096);
          asm volatile("s_waitcnt lgkmcnt(0)" : "+v"(xf[0]), "+v"(xf[1]), "+v"(xf[2]), "+v"(xf[3]) :: "memory");
          if constexpr (rho == 3)
            asm volatile("s_nop 4\n\tbuffer_atomic_add_x2 %0, %1, %2, %3 offen sc0" : "+v"(sgr) : "v"(r * 16 + h * 8), "s"(rs), "s"(OFF_ST0 + Tq * 512u) : "memory");
          if constexpr (rho + 2 < 8) XROUND(rho + 2);
          if constexpr (rho == 5 && TLBN > 0) {
            int z = 0; OPQV(z);
            asm volatile("buffer_atomic_add %0, %1, %2, 0 offen" :: "v"(z), "v"((unsigned)((lane * TLBW + w) * (TLBSTEP) + (j + 32 * sg) * 128 + (w < TLBW ? 0 : OOB))), "s"(rs) : "memory");
          }
#pragma unroll
          for (int i = 0; i < 4; ++i) { cqk = mfma16(wq[i], xf[i], cqk); OPQV(cqk); }
#pragma unroll
          for (int i = 0; i < 2; ++i) {
            swap16(xf[2 * i], xf[2 * i + 1]);
            cva = mfma16s(xf[2 * i], wv[i], cva); OPQV(cva);
            cvb = mfma16s(xf[2 * i + 1], wv[i], cvb); OPQV(cvb);
          }
          if constexpr (rho == 5) {
            const unsigned t32 = (tg.lo >> 16) | (tg.hi << 16);
            if (!__all(sgr.y == t32)) {
#pragma nounroll
              for (unsigned spins = 0; spins < SPIN_CAP; ++spins) { sgr = poll_ld(rs, r * 16 + h * 8, OFF_ST0 + Tq * 512u); if (__all(sgr.y == t32)) break; }
            }
            const unsigned sv = sgr.x;
            const auto t = __builtin_amdgcn_permlane32_swap(sv, sv, false, false);
            st = finish_stats(__uint_as_float(t[0]), __uint_as_float(t[1]), h); OPQV(st.aug);
            *(LAS float*)(L + own + 1024 + h * 128 + r * 4) = h ? st.mean : st.rstd;
          }
          if constexpr (rho == 6) { cqk = mfma8(aqk, st.aug, cqk); OPQV(cqk); bounce_rd(); }
          SB();
        });
      } else {
        const unsigned xb = OFF_X2 + Tq * 32768u;
        const v4u pcs = ld16(rs, lane16, OFF_CSQ + (sg * 32 + j) * 1024u);
        wait_flags(rs, (FL_X + 64 + Tq) * 128u, tg, 1u, lane, 16);
        STAMP(1);
        auto xload = [&](auto I) -> v4u { constexpr int i = I; return ld16(rs, lane16 + (i & 3) * 1024, xb + (i >> 2) * 4096u); };
        v4u xr[D1];
        sfor<0, D1>([&](auto I) { constexpr int i = I; xr[i] = xload(I); });
        v2u sp[8];
        asm volatile("s_waitcnt vmcnt(%0)" :: "i"(D1) : "memory");
        BAR();
        STAMP(2);
        { unsigned o2 = 0x3f803f80u; OPQV(o2); *(LAS v4u*)(L + R4S + (lane & 1) * 512) = v4u{o2, o2, o2, o2}; }
        unsigned vwa = R4V + ((lane >> 4) & 1) * 512 + (h * 16 + (lane & 15)) * 16; OPQV(vwa);
        v4u wq[3], wv[2];
        wq[0] = lds16(L, R0 + lane16); wq[1] = lds16(L, R0 + lane16 + 1024); wv[0] = lds16(L, vwa);
        csv = __uint_as_float(pcs.z);
        v4u xprev;
        SB();
        sfor<0, 32>([&](auto I) {
          constexpr int s = I;
          v4u xf = xr[s % D1];
          cqk = mfma16(wq[s % 3], xf, cqk); OPQV(cqk);
          if constexpr (s + 2 < 32) wq[(s + 2) % 3] = lds16(L, R0 + lane16 + (s + 2) * 1024);
          if constexpr (!(s & 1) && s + 2 < 32) wv[((s >> 1) + 1) & 1] = lds16(L, vwa + (s + 2) * 512);
          if constexpr (s & 1) {
            swap16(xprev, xf);
            cva = mfma16s(xprev, wv[(s >> 1) & 1], cva); OPQV(cva);
            cvb = mfma16s(xf, wv[(s >> 1) & 1], cvb); OPQV(cvb);
            if constexpr (s - 1 + D1 < 32) xr[(s - 1) % D1] = xload(IC(s - 1 + D1));
            if constexpr (s + D1 < 32) xr[s % D1] = xload(IC(s + D1));
          } else xprev = xf;
          if constexpr (s >= 4 && s < 12) sp[s - 4] = ld8(rs, h * 2048 + r * 8 + (s - 4) * 256, OFF_ST + (64 + Tq) * 4096u);
          if constexpr (s == 20) { st = make_stats(sp, h); OPQV(st.aug); *(LAS float*)(L + own + 1024 + h * 128 + r * 4) = h ? st.mean : st.rstd; }
          if constexpr (s == 22) { cqk = mfma8(v2u{pcs.x, pcs.y}, st.aug, cqk); OPQV(cqk); bounce_rd(); }
          if constexpr (s >= 23 && s < 31) mask2(IC(2 * (s - 23)));
          SB();
        });
      }
      STAMP(3);
      const bool up = (lane & 16) != 0;
      const unsigned kvb = OFF_KV + layer * (4 * MB) + (head * 64 + Tq) * 2048u;
      const unsigned kvo = sg < 7 ? lane16 : OOB;
      v4u qf, kf, vf;
      float q2 = 0.f, k2 = 0.f;
#pragma unroll
      for (int i = 0; i < 8; ++i) cqk[8 + i] *= st.rstd;
      kf.x = cvtpk(cqk[8], cqk[9]); kf.y = cvtpk(cqk[10], cqk[11]); kf.z = cvtpk(cqk[12], cqk[13]); kf.w = cvtpk(cqk[14], cqk[15]);
      st16(kf, rs, kvo, kvb);
#pragma unroll
      for (int i = 0; i < 4; ++i) { cva[i] = (cva[i] - mma[i] * csv) * rra[i]; cvb[i] = (cvb[i] - mmb[i] * csv) * rrb[i]; }
      {
        const auto t0 = __builtin_amdgcn_permlane32_swap(cvtpk(cva[0], cva[1]), cvtpk(cvb[0], cvb[1]), false, false);
        const auto t1 = __builtin_amdgcn_permlane32_swap(cvtpk(cva[2], cva[3]), cvtpk(cvb[2], cvb[3]), false, false);
        vf = v4u{t0[0], t1[0], t0[1], t1[1]};
      }
      st16(vf, rs, kvo + 1024, kvb);
      SB();
      *(LAS v4u*)(L + own + lane16) = kf;
      *(LAS v4u*)(L + own + 1024 + lane16) = vf;
      const unsigned vstep = up ? 0u : 2048u, vbase = up ? R4S : R1 + 1024 + (h * 16 + (lane & 15)) * 16;
      const v4u vd0 = lds16(L, vbase + vstep * (8 + tbl)), vd1 = lds16(L, vbase + vstep * (8 + tbl) + 512);
      const float qs = st.rstd * (0.25f * LOG2E);
#pragma unroll
      for (int i = 0; i < 8; ++i) { cqk[i] *= qs; q2 += cqk[i] * cqk[i]; k2 += cqk[8 + i] * cqk[8 + i]; }
      qf.x = cvtpk(cqk[0], cqk[1]); qf.y = cvtpk(cqk[2], cqk[3]); qf.z = cvtpk(cqk[4], cqk[5]); qf.w = cvtpk(cqk[6], cqk[7]);
      const f32x16 sd = mfma16(kf, qf, cm);
      q2 = xhalf_add(q2) * 1.02f;
      const unsigned kr = row_umax(__float_as_uint(xhalf_add(k2) * 1.02f));
      const unsigned km0 = (umax(__builtin_amdgcn_readlane(kr, 0), __builtin_amdgcn_readlane(kr, 16)) >> 16) + 1u;
      const unsigned kmax16 = km0 < 0x7fc0u ? km0 : 0x7fc0u;
      *(LAS unsigned*)(L + R4S + 1024 + (8 + tbl) * 4) = kmax16 << 16;
      const unsigned fkv = (FL_KV + (layer * 32 + head) * 8 + sg) * 128u;
      f32x16 o = {};
      bool ok = __all(q2 * __uint_as_float(kmax16 << 16) <= SAFE2);
      exp_pv(sd, vd0, vd1, o);
      int un = blockIdx.x; OPQS(un);
      const int uty = un < 112 ? 0 : (un < 224 ? 1 : 2);
      const int ucb = uty == 0 ? un >> 2 : (uty == 1 ? (un - 112) / 14 : (un - 224) >> 2);
      const int uks = 8 * (uty == 1 ? (un - 112) % 14 : un & 3) + w;
      auto prep_load = [&] {
        const kargs_t ka = kargs();
        const unsigned ld = uty == 0 ? FF * 2 : 1024;
        const rsrc_t ra = __builtin_amdgcn_make_buffer_rsrc((void*)(uty == 0 ? ka->wg : (uty == 1 ? ka->wd : ka->wo)), 0, uty == 2 ? DM * 1024 : FF * 1024, 0x00020000);
        const rsrc_t rb = __builtin_amdgcn_make_buffer_rsrc((void*)ka->wu, 0, uty == 0 ? FF * 1024 : 0, 0x00020000);
        const unsigned vo = (lane >> 3) * ld + (lane & 7) * 16, so = 16 * uks * ld + ucb * 128;
        dma1p(ra, lb + R0 + w * 4096u, so, vo); dma1p(ra, lb + R0 + w * 4096u + 1024, so + 8 * ld, vo);
        dma1p(rb, lb + R0 + w * 4096u + 2048, so, vo); dma1p(rb, lb + R0 + w * 4096u + 3072, so + 8 * ld, vo);
      };
      if (w < 4) {
        if (layer) {
#pragma unroll
          for (int i = 0; i < 4; ++i) dma4(rs, lb + R2 + w * 16384u + i * 4096u, OFF_WO + nblk * 65536u + w * 16384u + i * 4096u, lane16);
          VMWAIT(16);
        } else VMWAIT(0);
        if (sg < 7) set_flag(rs, fkv + tbl * 8, tg, kmax16, lane);
      }
      STAMP(4);
      BAR();
      STAMP(5);
      if (w < 4 && layer == 0) prep_load();
      unsigned kaddr = R1 + lane16; OPQV(kaddr);
      ok = ok && __all(q2 * __uint_as_float(row_umax(*(LAS unsigned*)(L + R4S + 1024 + (8 + (lane & 7)) * 4))) <= SAFE2);
      auto late_kv = [&] { VMWAIT(0); if (sg < 7) set_flag(rs, fkv + tbl * 8, tg, kmax16, lane); if (layer == 0) prep_load(); };
      if (ok && tbl > 0) attn_full<8>(L, qf, tbl, kaddr, vbase, vstep, o, [&](auto K) { if (K == 2 && w >= 4) late_kv(); });
      if (!ok && w >= 4) late_kv();
      STAMP(6);
      if (w < 4) {
        if (layer) dma_tile(rs, lb + R0, OFF_WGU + mem * 7 * 32768u, w, lane16);
        if (sg > 0) {
          const v2u f = wait_flags(rs, fkv - 128 + 16 * w, tg, 1u, lane, 2);
          dma4(rs, lb + R1 + w * 4096u, OFF_KV + layer * (4 * MB) + (head * 64 + 8 * (sg - 1) + 2 * w) * 2048u, lane16);
          if (lane < 2) *(LAS unsigned*)(L + R4S + 1024 + (2 * w + lane) * 4) = f.x << 16;
        }
        STAMP(7);
        VMWAIT(0);
      }
      if (layer == 0) {
        VMWAIT(0);
        const int g = lane >> 4, q = (lane & 15) >> 2, pp = lane & 3;
        const unsigned ta = R0 + w * 4096 + (4 * (g >> 1) + q) * 128 + pp * 8 + (g & 1) * (uty == 0 ? 2048 : 32), tstep = uty == 0 ? 32 : 64;
        const int mm = uks / 7, cc = uks % 7;
        const unsigned dst = uty == 0 ? OFF_WGU + (4 * ucb * 32 + uks) * 1024u
                           : (uty == 1 ? OFF_WD + (ucb * 224 + cc * 32 + (mm >> 3) * 16 + (mm & 7) * 2) * 1024u : OFF_WO + (ucb * 64 + uks) * 1024u);
        const unsigned dstep = uty == 1 ? 1024u : 32768u;
#pragma unroll
        for (int tl = 0; tl < 4; ++tl) {
          if (tl < 2 || uty == 0) {
            const v4u f = trfrag(L, ta + tl * tstep, 1024);
            st16(f, rs, lane16, dst + tl * dstep);
            *(LAS float*)(L + R4S + 2048 + ((w * 4 + tl) * 32 + r) * 4) = xhalf_add(sum8(f, 0.f));
          }
        }
      }
      STAMP(8);
      BAR();
      STAMP(9);
      bool pdone = layer != 0;
      if (layer == 0 && uty == 0 && w < 2) {
        float cs = 0.f;
#pragma unroll
        for (int i = 0; i < 8; ++i) cs += *(LAS float*)(L + R4S + 2048 + (i * 128 + w * 64 + lane) * 4);
        __builtin_amdgcn_raw_buffer_store_b32(__float_as_uint(cs), rs, lane16, OFF_AUG + (ucb * 128 + w * 64) * 16u + (un & 3) * 4u, 16);
      }
      auto prep_flag = [&] {
        unsigned old = 0;
        if (lane == 0) old = __hip_atomic_fetch_add((LAS unsigned*)(L + R4S + 1536), 1u, __ATOMIC_RELAXED, __HIP_MEMORY_SCOPE_WORKGROUP);
        if (__builtin_amdgcn_readfirstlane(old) == 7u)
          set_flag(rs, (FL_P + (uty == 0 ? 0 : (uty == 1 ? 28 : 36)) + ucb) * 128u + (uks >> 3) * 8u, tg, 1u, lane);
        pdone = true;
      };
      if (sg > 0) ok = ok && __all(q2 * __uint_as_float(row_umax(*(LAS unsigned*)(L + R4S + 1024 + (lane & 7) * 4))) <= SAFE2);
      if (ok) {
        if (sg > 0) {
#pragma unroll
          for (int i = 0; i < 16; ++i) cm[i] = NEG_BIG - cm[i];
          attn_bounded<7, -1>(L, qf, cm, tbl, 7 - tbl, kaddr, vbase, vstep, o, [&](auto K) { if (K == 2 && !pdone) { VMWAIT(0); prep_flag(); } });
        }
      } else {
        float m = NEG_BIG;
#pragma unroll
        for (int i = 0; i < 16; ++i) o[i] = 0.f;
        attn_general(L, qf, cm, 8 + tbl, 8, tbl, kaddr, vbase, vstep, m, o);
        if (sg > 0) {
#pragma unroll
          for (int i = 0; i < 16; ++i) cm[i] = NEG_BIG - cm[i];
          attn_general(L, qf, cm, tbl, tbl + 1, 7 - tbl, kaddr, vbase, vstep, m, o);
        }
      }
      STAMP(10);
      const float il = __builtin_amdgcn_rcpf(o[8]);
      v4u of;
      of.x = cvtpk(o[0] * il, o[1] * il); of.y = cvtpk(o[2] * il, o[3] * il); of.z = cvtpk(o[4] * il, o[5] * il); of.w = cvtpk(o[6] * il, o[7] * il);
      st16(of, rs, lane16, OFF_O + layer * (2 * MB) + (Tq * 32 + head) * 1024u);
      VMWAIT(0);
      set_flag(rs, (FL_O + (layer * 64 + Tq) * 2 + (head >> 4)) * 128u + (head & 15) * 8, tg, 1u, lane);
      if (!pdone) prep_flag();
      if (layer == 0 && w == sg) set_flag(rs, (FL_Q + head) * 128u + sg * 8, tg, 1u, lane);
      STAMP(11);
    }
    if (w0 >= 4) {
      ROLES(); LANE_CONSTS();
      const unsigned lw = w - 4;
      const unsigned gu = OFF_WGU + mem * 7 * 32768u, wd = OFF_WD + nblk * 7 * 32768u;
      unsigned wlo = lw * 8192u + lane16, whi = wlo + R2; OPQV(wlo); OPQV(whi);
      v4u sa[4][8];
      auto stage = [&](auto B, unsigned src, auto PACED) {
        constexpr int b = B;
        sfor<0, 8>([&](auto I) {
          constexpr int i = I;
          sa[b][i] = ld16(rs, lane16 + (i & 3) * 1024, src + lw * 8192u + (i >> 2) * 4096u);
          if constexpr (PACED) asm volatile(DMA_W ::: "memory"); else SB();
        });
      };
      auto put = [&](auto B, unsigned base) { constexpr int b = B; sfor<0, 8>([&](auto I) { constexpr int i = I; *(LAS v4u*)(L + base + i * 1024) = sa[b][i]; }); };
      if (layer) {
        stage(IC(0), gu + 1 * 32768u, IC(0));
        BAR();
        stage(IC(1), gu + 2 * 32768u, IC(1));
        put(IC(0), wlo + R1);
        stage(IC(2), gu + 3 * 32768u, IC(1));
        STAMP(14); BAR();
      } else {
        BAR();
        volatile LAS unsigned* relay = (volatile LAS unsigned*)(L + R4S + 1540);
        if (lw == 0) { wait_flags(rs, (FL_P + 36 + nblk) * 128u, tg, 1u, lane, 4); *relay = 1u; }
        else {
#pragma nounroll
          for (unsigned spins = 0; spins < 64 * SPIN_CAP && *relay == 0u; ++spins) __builtin_amdgcn_s_sleep(1);
          asm volatile("" ::: "memory");
        }
        dma_tile(rs, lb + R2, OFF_WO + nblk * 65536u, lw, lane16);
        dma_tile(rs, lb + R3, OFF_WO + nblk * 65536u + 32768u, lw, lane16);
        if (lw == 0) {
          const unsigned c0 = (7 * mem) >> 2, nw = 4 * (((7 * mem + 6) >> 2) - c0 + 1);
          wait_words(rs, (unsigned)lane < nw ? (lane >> 2) * 128 + (lane & 3) * 8 : OOB, (FL_P + c0) * 128u, tg, 1u, (unsigned)lane < nw);
        }
        VMWAIT(0);
        BAR();
        dma_tile(rs, lb + R0, gu, lw, lane16);
        stage(IC(0), gu + 1 * 32768u, IC(1));
        STAMP(14); BAR();
        stage(IC(1), gu + 2 * 32768u, IC(1));
        put(IC(0), wlo + R1);
        stage(IC(2), gu + 3 * 32768u, IC(1));
      }
      put(IC(1), whi); put(IC(2), whi + 32768u);
      STAMP(17); BAR();
      if (layer == 0) {
        if (lw == 0) wait_flags(rs, (FL_P + 28 + nblk) * 128u, tg, 1u, lane, 14);
        if (lw == 1) wait_flags(rs, (FL_Q + head) * 128u, tg, 1u, lane, 8);
      }
      STAMP(18); BAR();
      dma_tile(rs, lb + R0, gu + 4 * 32768u, lw, lane16);
      dma_tile(rs, lb + R1, gu + 5 * 32768u, lw, lane16);
      STAMP(19); BAR();
      dma_tile(rs, lb + R2, gu + 6 * 32768u, lw, lane16);
      stage(IC(0), wd + 3 * 32768u, IC(0));
      VMWAIT(24); STAMP(20); BAR();
      dma_tile(rs, lb + R3, wd + 0 * 32768u, lw, lane16);
      stage(IC(1), wd + 4 * 32768u, IC(0));
      VMWAIT(32); BAR();
      dma_tile(rs, lb + R0, wd + 1 * 32768u, lw, lane16);
      stage(IC(2), wd + 5 * 32768u, IC(0));
      VMWAIT(40); STAMP(21); BAR();
      dma_tile(rs, lb + R1, wd + 2 * 32768u, lw, lane16);
      stage(IC(3), wd + 6 * 32768u, IC(0));
      VMWAIT(40); STAMP(22); BAR();
      put(IC(0), whi);
      VMWAIT(24); STAMP(25); BAR();
      put(IC(1), whi + 32768u);
      VMWAIT(8); BAR();
      put(IC(2), wlo);
      STAMP(26); BAR();
      put(IC(3), wlo + R1);
      BAR();
      STAMP(27); BAR();
      BAR();
      STAMP(28); BAR();
      if (layer == 0) {
        dma4_paced(rs, lb + R0 + lw * 8192u, OFF_WQK + head * 32768u + lw * 8192u, lane16);
        dma4_paced(rs, lb + R0 + lw * 8192u + 4096u, OFF_WQK + head * 32768u + lw * 8192u + 4096u, lane16);
        dma4(rs, lb + R4V + lw * 4096u, OFF_WV + head * 16384u + lw * 4096u, lane16);
      } else if (blockIdx.x == 0 && w == 4) {
#pragma unroll
        for (int i = 0; i < 4; ++i) wait_flags(rs, FL_SEEN * 128u + i * 512u, tg, 1u, lane, 64);
        set_flag(rs, FL_EPOCH * 128u, tg, 0u, lane);
      }
    } else {
      BAR();
      STAMP(12);
      {
        ROLES(); LANE_CONSTS();
        const unsigned xfo = (tb2 * 32 + 4 * nblk + 2 * kh) * 1024u;
        v4u r0, r1;
        if (layer) { r0 = ld16(rs, lane16, OFF_X2 + xfo); r1 = ld16(rs, lane16 + 1024, OFF_X2 + xfo); }
        else {
          const rsrc_t rx = __builtin_amdgcn_make_buffer_rsrc((void*)kargs()->x, 0, SEQ * DM * 2, 0x00020000);
          const unsigned vo = r * 1024 + h * 8, so = tb2 * 32768u + nblk * 128u + kh * 64u;
          const v2u t0 = ld8p(rx, vo, so), t1 = ld8p(rx, vo + 16, so), t2 = ld8p(rx, vo + 32, so), t3 = ld8p(rx, vo + 48, so);
          r0 = v4u{t0.x, t0.y, t1.x, t1.y}; r1 = v4u{t2.x, t2.y, t3.x, t3.y};
        }
        unsigned wa0 = R2 + kh * (16384 + 32768) + lane16, wa1 = wa0 ^ 32768u; OPQV(wa0); OPQV(wa1);
        auto wread = [&](auto U) -> v4u { constexpr int u = U; return lds16(L, ((u & 1) ? wa1 : wa0) + (u >> 1) * 1024); };
        v4u ar[6];
        f32x16 c0 = unpack_tile(r0, r1), c1 = {};
        OPQV(c0);
        wait_flags(rs, (FL_O + (layer * 64 + tb2) * 2 + kh) * 128u, tg, 1u, lane, 16);
        STAMP(13);
        const unsigned ob = OFF_O + layer * (2 * MB) + (tb2 * 32 + 16 * kh) * 1024u;
        auto oload = [&](auto I) -> v4u { constexpr int i = I; return ld16(rs, lane16 + (i & 3) * 1024, ob + (i >> 2) * 4096u); };
        v4u ofr[16];
        sfor<0, D2>([&](auto I) { constexpr int i = I; ofr[i] = oload(I); });
        if (layer == 0) BAR();
        sfor<0, 6>([&](auto I) { constexpr int u = I; ar[u] = wread(I); });
        SB();
        sfor<0, 32>([&](auto I) {
          constexpr int u = I;
          if constexpr (u & 1) { c1 = mfma16(ar[u % 6], ofr[u >> 1], c1); OPQV(c1); } else { c0 = mfma16(ar[u % 6], ofr[u >> 1], c0); OPQV(c0); }
          if constexpr (u + 6 < 32) ar[u % 6] = wread(IC(u + 6));
          if constexpr (!(u & 1) && (u >> 1) + D2 < 16) ofr[(u >> 1) + D2] = oload(IC((u >> 1) + D2));
          SB();
        });
        unsigned sown = R4S + cw * 4096 + lane16; OPQV(sown);
#pragma unroll
        for (int g = 0; g < 4; ++g) *(LAS f32x4*)(L + sown + g * 1024) = f32x4{c1[4 * g], c1[4 * g + 1], c1[4 * g + 2], c1[4 * g + 3]};
        OPQV(c0);
        STAMP(14);
        BAR();
        STAMP(15);
#pragma unroll
        for (int g = 0; g < 4; ++g) { const f32x4 t = *(LAS f32x4*)(L + (sown ^ 4096u) + g * 1024); c0[4 * g] += t[0]; c0[4 * g + 1] += t[1]; c0[4 * g + 2] += t[2]; c0[4 * g + 3] += t[3]; }
        v4u y0, y1;
        pack_tile(c0, y0, y1);
        const unsigned xo = OFF_X1 + layer * (2 * MB) + xfo;
        st16(y0, rs, lane16, xo); st16(y1, rs, lane16 + 1024, xo);
        st8(tile_stats(c0), rs, h ? OOB : r * 8, OFF_ST + ((layer * 128 + tb2) * 16 + 2 * nblk + kh) * 256u);
        VMWAIT(0);
        set_flag(rs, (FL_X + layer * 128 + tb2) * 128u + (2 * nblk + kh) * 8, tg, 1u, lane);
      }
      {
        ROLES(); LANE_CONSTS();
        STAMP(16);
        v2u fpre;
        v4u ar[6];
        unsigned lhi = lane16 + R2; OPQV(lhi);
        unsigned lp0 = lane16 + kh * (16384 + 1024), lp1 = lp0 ^ 1024u;
        OPQV(lp0); OPQV(lp1);
        auto aread = [&](auto U) -> v4u {
          constexpr int u = U;
          if constexpr (u < 64) return lds16(L, ((u & 1) ? R1 : R0) + (u >> 1) * 1024 + lane16);
          else if constexpr (u < 224) {
            constexpr int T = 2 + (u - 64) / 32, s = (u - 64) % 32;
            if constexpr (T == 4 || T == 5) return lds16(L, (T - 4) * 32768 + s * 1024 + lane16);
            else return lds16(L, (T == 3 ? 32768 : 0) + s * 1024 + lhi);
          } else return lds16(L, R3 + ((u - 224) >> 1) * 2048 + ((u & 1) ? lp1 : lp0));
        };
        const unsigned augb = OFF_AUG + mem * 7 * 512u;
        v4u pg0 = ld16(rs, r * 16, augb), pg1 = ld16(rs, r * 16 + 512, augb);
        v2u agn = make_aug(pg0), ag1 = make_aug(pg1);
        OPQV(agn); OPQV(ag1);
        wait_flags(rs, (FL_X + layer * 128 + tb3) * 128u, tg, 1u, lane, 16);
        STAMP(17);
        v2u sp[8];
        const unsigned xb = OFF_X1 + layer * (2 * MB) + tb3 * 32768u;
        auto xload = [&](auto I) -> v4u { constexpr int i = I; return ld16(rs, lane16 + (i & 3) * 1024, xb + (i >> 2) * 4096u); };
        v4u x[32];
        sfor<0, D3>([&](auto I) { constexpr int i = I; x[i] = xload(I); });
        STAMP(23);
        BAR();
        STAMP(24);
        sfor<0, 6>([&](auto I) { constexpr int u = I; ar[u] = aread(I); });
        SB();
        const unsigned hb = OFF_H + layer * (7 * MB) + (tb3 * 112 + mem * 7) * 1024u;
        const unsigned fh_mine = (FL_H + (layer * 64 + tb3) * 2 + (mem >> 3)) * 128u + (mem & 7) * 8;
        const unsigned fh_poll = (FL_H + (layer * 64 + tb2) * 2 + kh) * 128u;
        Stats st;
        float cg, cu;
        f32x16 acc[3] = {};
        float ee[4], uu[4]; v4u hf;
        auto epi = [&](auto P_, auto E_, auto G_) {
          constexpr int p = P_, e = E_, g = G_;
          if constexpr (g == 0) { ee[e & 3] = ex2(acc[p % 3][e] * cg); OPQV(ee[e & 3]); }
          if constexpr (g == 1) { ee[e & 3] = __builtin_amdgcn_rcpf(1.0f + ee[e & 3]); uu[e & 3] = acc[p % 3][8 + e] * cu; OPQV(ee[e & 3]); OPQV(uu[e & 3]); }
          if constexpr (g == 2) {
            uu[e & 3] *= ee[e & 3]; OPQV(uu[e & 3]);
            if constexpr (e & 1) hf[e >> 1] = cvtpk(uu[(e - 1) & 3], uu[e & 3]);
            if constexpr (e == 7) st16(hf, rs, lane16 + (p & 3) * 1024, hb + (p >> 2) * 4096);
          }
        };
        SB();
        sfor<0, 224>([&](auto I) {
          constexpr int u = I;
          constexpr int T = u < 64 ? (u & 1) : 2 + (u - 64) / 32, s = u < 64 ? (u >> 1) : (u - 64) % 32;
          constexpr int left = u < 64 ? 64 - u : 32 - s;
          if constexpr (left == 4) {
            BAR();
            ar[(u + 4) % 6] = aread(IC(u + 4));
            ar[(u + 5) % 6] = aread(IC(u + 5));
          }
          if constexpr (u == 64) STAMP(18);
          if constexpr (u == 96) STAMP(19);
          if constexpr (u == 128) STAMP(20);
          if constexpr (u == 192) STAMP(21);
          if constexpr (u >= 64 && s == 0) { const f32x16 z = {}; acc[T % 3] = mfma8(agn, st.aug, z); }
          acc[T % 3] = mfma16(ar[u % 6], x[s], acc[T % 3]); OPQV(acc[T % 3]);
          if constexpr (left > 6 || left <= 4) ar[u % 6] = aread(IC(u + 6));
          if constexpr (u < 64 && !(u & 1) && s + D3 < 32) x[s + D3] = xload(IC(s + D3));
          if constexpr (u < 16 && (u & 1)) sp[s] = ld8(rs, h * 2048 + r * 8 + s * 256, OFF_ST + (layer * 128 + tb3) * 4096u);
          if constexpr (u == 31) { st = make_stats(sp, h); cg = st.rstd * -LOG2E; cu = st.rstd; OPQV(cg); OPQV(st.aug); }
          if constexpr (u == 36) { acc[1] = mfma8(ag1, st.aug, acc[1]); OPQV(acc[1]); }
          if constexpr (u == 37) { acc[0] = mfma8(agn, st.aug, acc[0]); OPQV(acc[0]); pg0 = ld16(rs, r * 16 + 1024, augb); }
          if constexpr (left == 5 && u < 200) { agn = make_aug(pg0); OPQV(agn); }
          if constexpr (left == 3 && u < 168) { constexpr int n2 = (u < 64 ? 3 : T + 2); pg0 = ld16(rs, r * 16 + (n2 & 1) * 512, augb + (n2 >> 1) * 1024); }
          if constexpr (T == 2 && u >= 64) {
            sfor<0, 16>([&](auto Q) { constexpr int q = Q, g = s - 2 - (3 * q) / 2; if constexpr (g >= 0 && g < 3) epi(IC(q / 8), IC(q % 8), IC(g)); });
          } else if constexpr (T > 2) {
            sfor<0, 8>([&](auto Q) { constexpr int q = Q, g = s - 2 - 3 * q; if constexpr (g >= 0 && g < 3) epi(IC(T - 1), Q, IC(g)); });
            if constexpr (s == 27 && T == 5) { VMWAIT(1); set_flag(rs, fh_mine, tg, 4u, lane); }
            if constexpr (s == 14 && T == 6) fpre = poll_ld(rs, lane < 8 ? lane * 8 : OOB, fh_poll);
            if constexpr (s == 27 && T == 6) { VMWAIT(1); set_flag(rs, fh_mine, tg, 5u, lane); }
          }
          SB();
        });
        STAMP(22);
        const unsigned xfo = (tb2 * 32 + 4 * nblk + 2 * kh) * 1024u;
        if (!flags_ok(fpre, tg, 4u, lane, 8)) wait_flags(rs, fh_poll, tg, 4u, lane, 8);
        const unsigned hb4 = OFF_H + layer * (7 * MB) + (tb2 * 112 + 56 * kh) * 1024u;
        v4u hr[D4];
        unsigned lane16b = lane16 + 4096; OPQV(lane16b);
        auto hload = [&](auto Q) {
          constexpr int q = Q, c = q >> 3;
          hr[q % D4] = ld16(rs, (c < 4 ? lane16 : lane16b) + (c & 3) * 1024, hb4 + (q & 7) * 7168u);
        };
        const unsigned xr1 = OFF_X1 + layer * (2 * MB) + xfo;
        const v4u r0 = ld16(rs, lane16, xr1), r1 = ld16(rs, lane16 + 1024, xr1);
        sfor<0, 6>(hload);
        SB();
        sfor<0, 10>([&](auto S_) { sfor<0, 8>([&](auto Q) { constexpr int g = S_ - Q; if constexpr (g >= 0 && g < 3) epi(IC(6), Q, IC(g)); }); });
        sfor<6, D4>(hload);
        f32x16 c0 = unpack_tile(r0, r1), c1 = {};
        unsigned lp0h = lp0 + R2, lp1h = lp1 + R2; OPQV(lp0h); OPQV(lp1h);
        auto dread = [&](auto U) -> v4u {
          constexpr int u = U, c = u / 16, i = (u % 16) >> 1;
          if constexpr (c == 0 || c == 4) return lds16(L, 32768 + i * 2048 + ((u & 1) ? lp1h : lp0h));
          else if constexpr (c == 3) return lds16(L, i * 2048 + ((u & 1) ? lp1h : lp0h));
          else return lds16(L, ((c == 2 || c == 6) ? 32768 : 0) + i * 2048 + ((u & 1) ? lp1 : lp0));
        };
        SB();
        sfor<0, 112>([&](auto I) {
          constexpr int u = I, c = u / 16, v = u % 16, q = u >> 1;
          if constexpr (v == 12 && c < 6) {
            BAR();
            ar[(224 + u + 4) % 6] = dread(IC(u + 4));
            ar[(224 + u + 5) % 6] = dread(IC(u + 5));
          }
          constexpr int U5 = 2 * (32 - D4) - 1, U7 = 2 * (40 - D4) - 1;
          if constexpr (u == U5 - 15 || u == U7 - 15) fpre = poll_ld(rs, lane < 8 ? lane * 8 : OOB, fh_poll);
          if constexpr (u == 16) STAMP(25);
          if constexpr (u == 48) STAMP(26);
          if constexpr (u == 80) STAMP(27);
          if constexpr (u & 1) { c1 = mfma16(ar[(224 + u) % 6], hr[q % D4], c1); OPQV(c1); } else { c0 = mfma16(ar[(224 + u) % 6], hr[q % D4], c0); OPQV(c0); }
          if constexpr ((v < 10 || v >= 12) && u + 6 < 112) ar[(224 + u) % 6] = dread(IC(u + 6));
          if constexpr (u == 11) { asm volatile("s_waitcnt vmcnt(%0)" :: "i"(D4 - 6 + 5) : "memory"); set_flag(rs, fh_mine, tg, 7u, lane); }
          if constexpr (u == U5) { if (!flags_ok(fpre, tg, 5u, lane, 8)) wait_flags(rs, fh_poll, tg, 5u, lane, 8); }
          if constexpr (u == U7) { if (!flags_ok(fpre, tg, 7u, lane, 8)) wait_flags(rs, fh_poll, tg, 7u, lane, 8); }
          if constexpr ((u & 1) && q + D4 < 56) hload(IC(q + D4));
          SB();
        });
        unsigned sown = R4S + cw * 4096 + lane16; OPQV(sown);
#pragma unroll
        for (int g = 0; g < 4; ++g) *(LAS f32x4*)(L + sown + g * 1024) = f32x4{c1[4 * g], c1[4 * g + 1], c1[4 * g + 2], c1[4 * g + 3]};
        OPQV(c0);
        STAMP(28);
        BAR();
        STAMP(29);
#pragma unroll
        for (int g = 0; g < 4; ++g) { const f32x4 t = *(LAS f32x4*)(L + (sown ^ 4096u) + g * 1024); c0[4 * g] += t[0]; c0[4 * g + 1] += t[1]; c0[4 * g + 2] += t[2]; c0[4 * g + 3] += t[3]; }
        v4u y0, y1;
        pack_tile(c0, y0, y1);
        if (layer == 0) {
          st16(y0, rs, lane16, OFF_X2 + xfo); st16(y1, rs, lane16 + 1024, OFF_X2 + xfo);
          st8(tile_stats(c0), rs, h ? OOB : r * 8, OFF_ST + ((64 + tb2) * 16 + 2 * nblk + kh) * 256u);
          VMWAIT(0);
          set_flag(rs, (FL_X + 64 + tb2) * 128u + (2 * nblk + kh) * 8, tg, 1u, lane);
        } else {
          const rsrc_t ro = __builtin_amdgcn_make_buffer_rsrc((void*)kargs()->out, 0, SEQ * DM * 2, 0x00020000);
          const unsigned a0 = y0.x, a1 = y0.y, a2 = y0.z, a3 = y0.w, b0 = y1.x, b1 = y1.y, b2 = y1.z, b3 = y1.w;
          const auto p0 = __builtin_amdgcn_permlane32_swap(a0, a2, false, false), p1 = __builtin_amdgcn_permlane32_swap(a1, a3, false, false);
          const auto p2 = __builtin_amdgcn_permlane32_swap(b0, b2, false, false), p3 = __builtin_amdgcn_permlane32_swap(b1, b3, false, false);
          const unsigned vo = r * 1024 + h * 16, so = tb2 * 32768u + nblk * 128u + kh * 64u;
          const v4u z0 = {p0[0], p1[0], p0[1], p1[1]}, z1 = {p2[0], p3[0], p2[1], p3[1]};
          __builtin_amdgcn_raw_buffer_store_b128(z0, ro, vo, so, OUTAUX); __builtin_amdgcn_raw_buffer_store_b128(z1, ro, vo + 32, so, OUTAUX);
          asm volatile("s_nop 1" :: "v"(z0), "v"(z1) : "memory");
        }
        STAMP(30);
      }
    }
  }
  STAMP_OUT();
}

extern "C" void kernel_launch(void* const* d_in, const int* in_sizes, int n_in, void* d_out, int out_size, void* d_ws, size_t ws_size,
                              hipStream_t stream) {
  Args a;
  a.ws = (unsigned char*)d_ws; a.out = (u16*)d_out;
  a.x = (const u16*)d_in[0]; a.wqkv = (const u16*)d_in[1]; a.wo = (const u16*)d_in[2];
  a.wg = (const u16*)d_in[4]; a.wu = (const u16*)d_in[5]; a.wd = (const u16*)d_in[6];
  (void)hipFuncSetAttribute((const void*)main_k, hipFuncAttributeMaxDynamicSharedMemorySize, LDS_BYTES);
  main_k<<<256, 512, LDS_BYTES, stream>>>(a);
}
```

```cpp
#include <hip/hip_runtime.h>
#include <stdint.h>
#include <type_traits>

typedef unsigned short u16;
typedef float f32x16 __attribute__((ext_vector_type(16)));
typedef float f32x4 __attribute__((ext_vector_type(4)));
typedef float f32x2 __attribute__((ext_vector_type(2)));
typedef __bf16 bf16x8 __attribute__((ext_vector_type(8)));
typedef __bf16 bf16x2 __attribute__((ext_vector_type(2)));
typedef short s16x4 __attribute__((ext_vector_type(4)));
typedef unsigned v4u __attribute__((ext_vector_type(4)));
typedef unsigned v2u __attribute__((ext_vector_type(2)));
#define GAS __attribute__((address_space(1)))
#define LAS __attribute__((address_space(3)))
#define DI __device__ __forceinline__
#define RLX_AGENT __ATOMIC_RELAXED, __HIP_MEMORY_SCOPE_AGENT
#define IC(n) std::integral_constant<int, (n)>{}
typedef __amdgpu_buffer_rsrc_t rsrc_t;

constexpr int SEQ = 2048, DM = 512, FF = 1792;
constexpr unsigned KB = 1024u, MB = 1u << 20;
constexpr unsigned FL_EPOCH = 0;
constexpr unsigned FL_SEEN = 2;
constexpr unsigned FL_P = 32;
constexpr unsigned FL_Q = FL_P + 44;
constexpr unsigned FL_X = FL_Q + 32;
constexpr unsigned FL_O = FL_X + 3 * 64;
constexpr unsigned FL_H = FL_O + 2 * 64 * 2;
constexpr unsigned FL_KV = FL_H + 2 * 64 * 2;
constexpr unsigned FL_LINES = FL_KV + 2 * 32 * 8;
static_assert(FL_LINES * 128 <= 256 * KB, "flags");
constexpr unsigned OFF_AUG = 256 * KB;
constexpr unsigned OFF_ST0 = 384 * KB;
constexpr unsigned OFF_CSQ = 512 * KB;
constexpr unsigned OFF_WQK = 1 * MB, OFF_WV = 2 * MB, OFF_WO = 2 * MB + 512 * KB, OFF_WGU = 3 * MB, OFF_WD = 6 * MB + 512 * KB;
constexpr unsigned OFF_ST = 9 * MB;
constexpr unsigned OFF_X1 = 11 * MB, OFF_X2 = 15 * MB, OFF_O = 17 * MB, OFF_KV = 21 * MB, OFF_H = 29 * MB;
constexpr unsigned WS_BYTES = 43 * MB;
constexpr unsigned OOB = 0x40000000u;
constexpr unsigned R0 = 0, R1 = 32 * KB, R2 = 64 * KB, R3 = 96 * KB, R4V = 128 * KB, R4S = 144 * KB, LDS_BYTES = 160 * KB;
constexpr float NEG_BIG = -1.0e30f;
constexpr float LOG2E = 1.4426950408889634f;
constexpr float SAFE2 = 2500.0f;
constexpr unsigned SPIN_CAP = 1u << 18;
#define D1V 8
#define D2V 8
#define D3V 8
#define D4V 10
#define OUTAUX 0
#define TLBN 0
#define TLBW 1
#define TLBSTEP 0
constexpr int D1 = D1V, D2 = D2V, D3 = D3V, D4 = D4V;

template <int B, int E, class F> DI void sfor(F&& f) {
  if constexpr (B < E) { f(std::integral_constant<int, B>{}); sfor<B + 1, E>(f); }
}
DI bf16x8 as_bf8(v4u v) { return __builtin_bit_cast(bf16x8, v); }
DI bf16x2 as_bf2(unsigned v) { return __builtin_bit_cast(bf16x2, v); }
DI s16x4 as_s4(v2u v) { return __builtin_bit_cast(s16x4, v); }
DI f32x16 mfma16(v4u a, v4u b, f32x16 c) { return __builtin_amdgcn_mfma_f32_32x32x16_bf16(as_bf8(a), as_bf8(b), c, 0, 0, 0); }
DI f32x16 mfma8(v2u a, v2u b, f32x16 c) { return __builtin_amdgcn_mfma_f32_32x32x8bf16_1k(as_s4(a), as_s4(b), c, 0, 0, 0); }
DI f32x4 mfma16s(v4u a, v4u b, f32x4 c) { return __builtin_amdgcn_mfma_f32_16x16x32_bf16(as_bf8(a), as_bf8(b), c, 0, 0, 0); }
DI unsigned cvtpk(float lo, float hi) { f32x2 f = {lo, hi}; bf16x2 p = __builtin_convertvector(f, bf16x2); return __builtin_bit_cast(unsigned, p); }
DI float bflo(unsigned v) { return __uint_as_float(v << 16); }
DI float bfhi(unsigned v) { return __uint_as_float(v & 0xffff0000u); }
DI float dot2(unsigned a, unsigned b, float c) { return __builtin_amdgcn_fdot2_f32_bf16(as_bf2(a), as_bf2(b), c, false); }
DI float ex2(float v) { return __builtin_amdgcn_exp2f(v); }
DI float xhalf_add(float v) { auto r = __builtin_amdgcn_permlane32_swap(__float_as_uint(v), __float_as_uint(v), false, false); return __uint_as_float(r[0]) + __uint_as_float(r[1]); }
DI float xhalf_max(float v) { auto r = __builtin_amdgcn_permlane32_swap(__float_as_uint(v), __float_as_uint(v), false, false); return fmaxf(__uint_as_float(r[0]), __uint_as_float(r[1])); }
DI unsigned umax(unsigned a, unsigned b) { return a > b ? a : b; }
DI unsigned row_umax(unsigned v) {
  v = umax(v, (unsigned)__builtin_amdgcn_update_dpp(0, (int)v, 0x121, 0xf, 0xf, true));
  v = umax(v, (unsigned)__builtin_amdgcn_update_dpp(0, (int)v, 0x122, 0xf, 0xf, true));
  v = umax(v, (unsigned)__builtin_amdgcn_update_dpp(0, (int)v, 0x124, 0xf, 0xf, true));
  v = umax(v, (unsigned)__builtin_amdgcn_update_dpp(0, (int)v, 0x128, 0xf, 0xf, true));
  return v;
}
DI v4u ld16(rsrc_t rs, unsigned voff, unsigned soff) { return __builtin_amdgcn_raw_buffer_load_b128(rs, voff, soff, 16); }
DI v4u ld16p(rsrc_t rs, unsigned voff, unsigned soff) { return __builtin_amdgcn_raw_buffer_load_b128(rs, voff, soff, 0); }
DI v2u ld8p(rsrc_t rs, unsigned voff, unsigned soff) { return __builtin_bit_cast(v2u, __builtin_amdgcn_raw_buffer_load_b64(rs, voff, soff, 0)); }
DI v2u ld8(rsrc_t rs, unsigned voff, unsigned soff) { return __builtin_bit_cast(v2u, __builtin_amdgcn_raw_buffer_load_b64(rs, voff, soff, 16)); }
DI void st16(v4u v, rsrc_t rs, unsigned voff, unsigned soff) {
  __builtin_amdgcn_raw_buffer_store_b128(v, rs, voff, soff, 16);
  asm volatile("s_nop 1" :: "v"(v) : "memory");
}
DI void st8(v2u v, rsrc_t rs, unsigned voff, unsigned soff) { __builtin_amdgcn_raw_buffer_store_b64(v, rs, voff, soff, 16); }
DI v4u lds16(LAS unsigned char* L, unsigned off) { return *(LAS v4u*)(L + off); }
#define SB() __builtin_amdgcn_sched_barrier(0)
#define OPQV(x) asm volatile("" : "+v"(x))
#define OPQS(x) asm volatile("" : "+s"(x))
#define LANE_CONSTS() int lane = threadIdx.x & 63; OPQV(lane); const int r = lane & 31, h = lane >> 5; const unsigned lane16 = lane * 16; (void)r; (void)h
#define BAR() asm volatile("s_waitcnt lgkmcnt(0)\n\ts_barrier" ::: "memory")
#define VMWAIT(n) asm volatile("s_waitcnt vmcnt(" #n ")" ::: "memory")
#define DMA_HEAD "s_nop 4\n\ts_mov_b32 m0, %0\n\ts_nop 0\n\t"
#define DMA_LD(o) "buffer_load_dwordx4 %1, %2, %3 offen offset:" #o " sc1 lds\n\t"
#define DMA_ARGS :: "s"(lds), "v"(voff), "s"(rs), "s"(soff) : "memory"
DI void dma4(rsrc_t rs, unsigned lds, unsigned soff, unsigned voff) { asm volatile(DMA_HEAD DMA_LD(0) DMA_LD(1024) DMA_LD(2048) DMA_LD(3072) DMA_ARGS); }
DI void dma2(rsrc_t rs, unsigned lds, unsigned soff, unsigned voff) { asm volatile(DMA_HEAD DMA_LD(0) DMA_LD(1024) DMA_ARGS); }
DI void dma1p(rsrc_t rs, unsigned lds, unsigned soff, unsigned voff) { asm volatile(DMA_HEAD "buffer_load_dwordx4 %1, %2, %3 offen lds" DMA_ARGS); }
#define PACE 3
#define STR2(x) #x
#define STR(x) STR2(x)
#define DMA_W "s_waitcnt vmcnt(" STR(PACE) ")\n\t"
DI void dma4_paced(rsrc_t rs, unsigned lds, unsigned soff, unsigned voff) {
  asm volatile(DMA_HEAD DMA_LD(0) DMA_W DMA_LD(1024) DMA_W DMA_LD(2048) DMA_W DMA_LD(3072) DMA_W DMA_ARGS);
}
DI void dma_tile(rsrc_t rs, unsigned lds, unsigned soff, unsigned lw, unsigned lane16) {
  dma4(rs, lds + lw * 8192u, soff + lw * 8192u, lane16);
  dma4(rs, lds + lw * 8192u + 4096u, soff + lw * 8192u + 4096u, lane16);
}
extern "C" __device__ long long wsatomic_add64(long long, rsrc_t, int, int, int) __asm("llvm.amdgcn.raw.ptr.buffer.atomic.add.i64");
struct Tag { unsigned lo, hi; };
DI v2u poll_ld(rsrc_t rs, unsigned voff, unsigned soff) {
  long long z = 0; OPQV(z);
  return __builtin_bit_cast(v2u, wsatomic_add64(z, rs, voff, soff, 0));
}
DI bool word_ok(v2u f, Tag t, unsigned target) { const unsigned a = f.x, b = f.y; return b == t.hi && (a - t.lo - target) <= 0xffffu - target; }
DI bool flags_ok(v2u f, Tag t, unsigned target, int lane, int n) { return __all(word_ok(f, t, target) || lane >= n); }
DI v2u wait_words(rsrc_t rs, unsigned vo, unsigned soff, Tag t, unsigned target, bool act) {
  v2u f = {0u, 0u};
#pragma nounroll
  for (unsigned spins = 0; spins < SPIN_CAP; ++spins) {
    f = poll_ld(rs, vo, soff);
    if (__all(word_ok(f, t, target) || !act)) break;
  }
  asm volatile("" ::: "memory");
  return f;
}
DI v2u wait_flags(rsrc_t rs, unsigned soff, Tag t, unsigned target, int lane, int n) {
  return wait_words(rs, lane < n ? lane * 8 : OOB, soff, t, target, lane < n);
}
DI void relay_set(LAS unsigned char* L, Tag t, unsigned id) { *(volatile LAS v2u*)(L + R4S + 1536) = v2u{t.lo | id, t.hi}; }
DI bool relay_get(LAS unsigned char* L, Tag t, unsigned id) {
  const v2u v = *(volatile LAS v2u*)(L + R4S + 1536);
  const unsigned a = v.x, b = v.y;
  return __builtin_amdgcn_readfirstlane(a) == (t.lo | id) && __builtin_amdgcn_readfirstlane(b) == t.hi;
}
DI void relay_wait(LAS unsigned char* L, Tag t, unsigned id) {
#pragma nounroll
  for (unsigned spins = 0; spins < 64 * SPIN_CAP && !relay_get(L, t, id); ++spins) __builtin_amdgcn_s_sleep(1);
  asm volatile("" ::: "memory");
}
DI void swap64(rsrc_t rs, unsigned voff, unsigned soff, v2u v) {
  asm volatile("s_nop 4\n\tbuffer_atomic_swap_x2 %0, %1, %2, %3 offen\n\ts_nop 1" :: "v"(v), "v"(voff), "s"(rs), "s"(soff) : "memory");
}
DI void set_flag(rsrc_t rs, unsigned off, Tag t, unsigned payload, int lane) { swap64(rs, lane == 0 ? 0u : OOB, off, v2u{t.lo | payload, t.hi}); }

struct Stats { float rstd, mean; v2u aug; };
DI Stats finish_stats(float s1, float s2, int h) {
  const float mean = s1 * (1.0f / DM);
  const float var = fmaxf(s2 * (1.0f / DM) - mean * mean, 0.0f);
  Stats st; st.rstd = __builtin_amdgcn_rsqf(var + 1e-5f); st.mean = mean;
  const float nm = h ? 0.0f : -mean;
  const unsigned hi = cvtpk(nm, nm) & 0xffffu;
  const float lo = nm - __uint_as_float(hi << 16);
  const unsigned w = hi | (cvtpk(lo, lo) << 16);
  st.aug.x = w; st.aug.y = w;
  return st;
}
DI Stats make_stats(const v2u (&sp)[8], int h) {
  float s1 = 0.f, s2 = 0.f;
#pragma unroll
  for (int i = 0; i < 8; ++i) { const unsigned a = sp[i].x, b = sp[i].y; s1 += __uint_as_float(a); s2 += __uint_as_float(b); }
  return finish_stats(xhalf_add(s1), xhalf_add(s2), h);
}
DI float xrow_add(float v) { auto r = __builtin_amdgcn_permlane16_swap(__float_as_uint(v), __float_as_uint(v), false, false); return __uint_as_float(r[0]) + __uint_as_float(r[1]); }
DI float wave_add(float v) {
  v += __int_as_float(__builtin_amdgcn_update_dpp(0, __float_as_int(v), 0x121, 0xf, 0xf, true));
  v += __int_as_float(__builtin_amdgcn_update_dpp(0, __float_as_int(v), 0x122, 0xf, 0xf, true));
  v += __int_as_float(__builtin_amdgcn_update_dpp(0, __float_as_int(v), 0x124, 0xf, 0xf, true));
  v += __int_as_float(__builtin_amdgcn_update_dpp(0, __float_as_int(v), 0x128, 0xf, 0xf, true));
  return xhalf_add(xrow_add(v));
}
DI v2u trd(LAS unsigned char* L, unsigned off) { return __builtin_bit_cast(v2u, __builtin_amdgcn_ds_read_tr16_b64_v4i16((LAS s16x4*)(L + off))); }
DI v4u trfrag(LAS unsigned char* L, unsigned off, unsigned second) { const v2u a = trd(L, off), b = trd(L, off + second); return v4u{a.x, a.y, b.x, b.y}; }
DI float sum8(v4u f, float c) {
  const unsigned a = f.x, b = f.y, d = f.z, e = f.w;
  return dot2(a, 0x3f803f80u, dot2(b, 0x3f803f80u, dot2(d, 0x3f803f80u, dot2(e, 0x3f803f80u, c))));
}
DI v2u split_aug(float cs) {
  const unsigned hi = cvtpk(cs, cs);
  const float lo = cs - __uint_as_float(hi << 16);
  return v2u{hi, cvtpk(lo, lo)};
}
DI float sum4(v4u p) {
  const unsigned a = p.x, b = p.y, c = p.z, d = p.w;
  return (__uint_as_float(a) + __uint_as_float(b)) + (__uint_as_float(c) + __uint_as_float(d));
}
DI void swap16(v4u& a, v4u& b) {
#pragma unroll
  for (int i = 0; i < 4; ++i) { const unsigned x = a[i], y = b[i]; const auto t = __builtin_amdgcn_permlane16_swap(x, y, false, false); a[i] = t[0]; b[i] = t[1]; }
}
DI v2u make_aug(v4u p) { return split_aug(sum4(p)); }

DI float max16(const f32x16& s, float m) {
  m = fmaxf(fmaxf(m, s[0]), s[1]);   m = fmaxf(fmaxf(m, s[2]), s[3]);
  float n = fmaxf(s[4], s[5]);       n = fmaxf(fmaxf(n, s[6]), s[7]);
  m = fmaxf(fmaxf(m, s[8]), s[9]);   n = fmaxf(fmaxf(n, s[10]), s[11]);
  m = fmaxf(fmaxf(m, s[12]), s[13]); n = fmaxf(fmaxf(n, s[14]), s[15]);
  return fmaxf(m, n);
}
DI void exp_pv(const f32x16& s, v4u v0, v4u v1, f32x16& o) {
  v4u p0, p1;
  p0.x = cvtpk(ex2(s[0]), ex2(s[1])); p0.y = cvtpk(ex2(s[2]), ex2(s[3])); p0.z = cvtpk(ex2(s[4]), ex2(s[5])); p0.w = cvtpk(ex2(s[6]), ex2(s[7]));
  o = mfma16(v0, p0, o);
  p1.x = cvtpk(ex2(s[8]), ex2(s[9])); p1.y = cvtpk(ex2(s[10]), ex2(s[11])); p1.z = cvtpk(ex2(s[12]), ex2(s[13])); p1.w = cvtpk(ex2(s[14]), ex2(s[15]));
  o = mfma16(v1, p1, o);
}
DI void attn_general(LAS unsigned char* L, v4u q, const f32x16& cm, int bm, int bf0, int nf, unsigned kaddr, unsigned vbase, unsigned vstep,
                     float& m, f32x16& o) {
  const f32x16 zero = {};
  float mx = max16(mfma16(lds16(L, kaddr + bm * 2048), q, cm), m);
#pragma nounroll
  for (int b = 0; b < nf; ++b) mx = max16(mfma16(lds16(L, kaddr + (bf0 + b) * 2048), q, zero), mx);
  mx = xhalf_max(mx);
  const float f = ex2(m - mx);
#pragma unroll
  for (int i = 0; i < 16; ++i) o[i] *= f;
  m = mx;
  f32x16 c;
#pragma unroll
  for (int i = 0; i < 16; ++i) c[i] = cm[i] - mx;
  unsigned va = vbase + vstep * bm;
  exp_pv(mfma16(lds16(L, kaddr + bm * 2048), q, c), lds16(L, va), lds16(L, va + 512), o);
#pragma unroll
  for (int i = 0; i < 16; ++i) c[i] = -mx;
#pragma nounroll
  for (int b = 0; b < nf; ++b) {
    va = vbase + vstep * (bf0 + b);
    exp_pv(mfma16(lds16(L, kaddr + (bf0 + b) * 2048), q, c), lds16(L, va), lds16(L, va + 512), o);
  }
}
template <int K, int S0, int DIR, class H>
DI void attn_chain(LAS unsigned char* L, v4u q, int n, unsigned kaddr, unsigned va, unsigned vstep, const f32x16& s, v4u v0, v4u v1, v4u kn,
                   f32x16& o, H&& hook) {
  if constexpr (K == 7) { hook(IC(K)); exp_pv(s, v0, v1, o); }
  else {
    const f32x16 zero = {};
    f32x16 sn = mfma16(kn, q, zero);
    v4u w0 = lds16(L, va), w1 = lds16(L, va + 512);
    if constexpr (K < 6) kn = lds16(L, kaddr + (S0 + DIR * (K + 1)) * 2048);
    hook(IC(K));
    SB();
    exp_pv(s, v0, v1, o);
    asm volatile("" : "+v"(sn), "+v"(w0), "+v"(w1), "+v"(kn), "+v"(o));
    if (n > K) attn_chain<K + 1, S0, DIR>(L, q, n, kaddr, DIR > 0 ? va + vstep : va - vstep, vstep, sn, w0, w1, kn, o, hook);
  }
}
template <int S0, int DIR, class H>
DI void attn_bounded(LAS unsigned char* L, v4u q, const f32x16& cm, int bm, int n, unsigned kaddr, unsigned vbase, unsigned vstep, f32x16& o, H&& hook) {
  const unsigned vm = vbase + vstep * bm;
  const v4u km = lds16(L, kaddr + bm * 2048), kn = lds16(L, kaddr + S0 * 2048), v0 = lds16(L, vm), v1 = lds16(L, vm + 512);
  attn_chain<0, S0, DIR>(L, q, n, kaddr, vbase + vstep * S0, vstep, mfma16(km, q, cm), v0, v1, kn, o, hook);
}
template <int S0, class H>
DI void attn_full(LAS unsigned char* L, v4u q, int n, unsigned kaddr, unsigned vbase, unsigned vstep, f32x16& o, H&& hook) {
  const f32x16 zero = {};
  const unsigned va = vbase + vstep * S0;
  const v4u k0 = lds16(L, kaddr + S0 * 2048), kn = lds16(L, kaddr + (S0 + 1) * 2048), v0 = lds16(L, va), v1 = lds16(L, va + 512);
  attn_chain<1, S0, 1>(L, q, n, kaddr, va + vstep, vstep, mfma16(k0, q, zero), v0, v1, kn, o, hook);
}

DI f32x16 unpack_tile(v4u r0, v4u r1) {
  f32x16 k;
  k[0] = bflo(r0.x); k[1] = bfhi(r0.x); k[2] = bflo(r0.y); k[3] = bfhi(r0.y); k[4] = bflo(r0.z); k[5] = bfhi(r0.z); k[6] = bflo(r0.w); k[7] = bfhi(r0.w);
  k[8] = bflo(r1.x); k[9] = bfhi(r1.x); k[10] = bflo(r1.y); k[11] = bfhi(r1.y); k[12] = bflo(r1.z); k[13] = bfhi(r1.z); k[14] = bflo(r1.w); k[15] = bfhi(r1.w);
  return k;
}
DI void pack_tile(const f32x16& k, v4u& y0, v4u& y1) {
  y0.x = cvtpk(k[0], k[1]); y0.y = cvtpk(k[2], k[3]); y0.z = cvtpk(k[4], k[5]); y0.w = cvtpk(k[6], k[7]);
  y1.x = cvtpk(k[8], k[9]); y1.y = cvtpk(k[10], k[11]); y1.z = cvtpk(k[12], k[13]); y1.w = cvtpk(k[14], k[15]);
}
DI v2u tile_stats(const f32x16& k) {
  f32x2 a = {k[0], k[1]}, b = a * a;
#pragma unroll
  for (int i = 1; i < 8; ++i) { const f32x2 t = {k[2 * i], k[2 * i + 1]}; a += t; b += t * t; }
  return v2u{__float_as_uint(xhalf_add(a[0] + a[1])), __float_as_uint(xhalf_add(b[0] + b[1]))};
}

struct Args { unsigned char* ws; u16* out; const u16 *x, *wqkv, *wo, *wg, *wu, *wd; };
#define STAMP(id) do {} while (0)
#define STAMP_DECL() do {} while (0)
#define STAMP_OUT() do {} while (0)

typedef const __attribute__((address_space(4))) Args* kargs_t;
DI kargs_t kargs() { kargs_t p = (kargs_t)__builtin_amdgcn_kernarg_segment_ptr(); OPQS(p); return p; }
__global__ void __launch_bounds__(512, 2) main_k(Args a) {
  extern __shared__ __attribute__((aligned(16))) unsigned char smem[];
  LAS unsigned char* L = (LAS unsigned char*)smem;
  const unsigned lb = (unsigned)(size_t)L;
  const int w0 = __builtin_amdgcn_readfirstlane(threadIdx.x >> 6);
  STAMP_DECL();
  const int xcd = blockIdx.x & 7, idx = blockIdx.x >> 3;
  const int sg0 = (xcd & ~1) | (idx >> 4), i4 = idx & 15, j0 = ((i4 >> 2) << 3) | ((xcd & 1) << 2) | (i4 & 3);
  const rsrc_t rs = __builtin_amdgcn_make_buffer_rsrc(a.ws, 0, WS_BYTES, 0x00020000);
#define ROLES() int w = w0, sg = sg0, j = j0, layer = layer0; OPQS(w); OPQS(sg); OPQS(j); OPQS(layer); Tag tg = tg0; OPQS(tg.lo); OPQS(tg.hi); \
  const int head = j, tbl = w < 4 ? w : 11 - w, Tq = 8 * sg + tbl;              \
  const int grp = 2 * sg + (j >> 4), mem = j & 15, nblk = j & 7;                \
  const int cw = w & 3, th = cw >> 1, kh = cw & 1; \
  const int tb2 = 4 * grp + 2 * ((j >> 3) & 1) + th, tb3 = 4 * grp + cw;        \
  (void)head; (void)Tq; (void)mem; (void)nblk; (void)kh; (void)tb2; (void)tb3
#define PREP_ROLES() const int pun = sg * 32 + j, uty = pun < 112 ? 0 : (pun < 224 ? 1 : 2); \
  const int pcb = uty == 0 ? pun >> 2 : (uty == 1 ? (pun - 112) / 14 : (pun - 224) >> 2), pkq = uty == 1 ? (pun - 112) % 14 : pun & 3, pks = 8 * pkq + 2 * w
  Tag tg0 = {0u, 0u};
  f32x16 cqk, cm;
  f32x4 cva, cvb, rra, rrb, mma, mmb;
  float rstd1, csv;
#define CAUSAL2(i) do { float a0 = (((i) & 3) + 8 * ((i) >> 2) + 4 * h <= r) ? 0.0f : NEG_BIG, a1 = ((((i) + 1) & 3) + 8 * ((i) >> 2) + 4 * h <= r) ? 0.0f : NEG_BIG; \
    OPQV(a0); OPQV(a1); cm[i] = a0; cm[(i) + 1] = a1; } while (0)
#define BOUNCE_WR(st) *(LAS float*)(L + R4S + 8192 + w * 256 + h * 128 + r * 4) = h ? (st).mean : (st).rstd
#define BOUNCE_RD() do { const unsigned ba = R4S + 8192 + w * 256 + (lane >> 4) * 16; \
    rra = *(LAS f32x4*)(L + ba); rrb = *(LAS f32x4*)(L + ba + 64); mma = *(LAS f32x4*)(L + ba + 128); mmb = *(LAS f32x4*)(L + ba + 192); } while (0)
#define ONES_WR() do { unsigned o2 = 0x3f803f80u; OPQV(o2); *(LAS v4u*)(L + R4S + (lane & 1) * 512) = v4u{o2, o2, o2, o2}; } while (0)
  {
    const int layer0 = 0;
    STAMP(31);
    ROLES(); LANE_CONSTS();
    const rsrc_t rx = __builtin_amdgcn_make_buffer_rsrc((void*)a.x, 0, SEQ * DM * 2, 0x00020000);
    const rsrc_t rw = __builtin_amdgcn_make_buffer_rsrc((void*)a.wqkv, 0, DM * 3072, 0x00020000);
    const int un = sg * 32 + j;
    const bool uq = w == 0, uv = w == 1 && un < 128;
    const int ucb = uq ? un >> 5 : un >> 4, uk = uq ? un & 31 : 2 * (un & 15);
    const unsigned uvo = (uq || uv) ? (lane >> 3) * 3072 + (lane & 7) * 16 : OOB, uso = 16 * uk * 3072 + ucb * 128 + (uq ? 0 : 2048);
    const unsigned ulds = R1 + w * 12288 + 8192, u2 = uq ? 1024 : 16 * 3072;
    dma1p(rw, lb + ulds, uso, uvo); dma1p(rw, lb + ulds + 1024, uso + 8 * 3072, uvo);
    dma1p(rw, lb + ulds + 2048, uso + u2, uvo); dma1p(rw, lb + ulds + 3072, uso + u2 + 8 * 3072, uvo);
    const unsigned token = 8 * un + w;
    v2u ep; v4u row;
    asm volatile("s_nop 4\n\tbuffer_load_dwordx2 %0, off, %2, 0 sc1\n\tbuffer_load_dwordx4 %1, %3, %4, %5 offen"
                 : "=&v"(ep), "=&v"(row) : "s"(rs), "v"(lane16), "s"(rx), "s"(token * 1024u) : "memory");
    const unsigned xv0 = (lane >> 3) * 1024 + (((lane & 7) ^ (lane >> 4)) * 16), xv1 = (xv0 ^ 64u) + 7168, xv2 = xv0 + 14336, xv3 = xv1 + 14336;
    const unsigned rot = (w + j) & 7;
    auto xround = [&](int rho) {
      asm volatile("s_nop 4\n\ts_mov_b32 m0, %0\n\ts_nop 0\n\tbuffer_load_dwordx4 %1, %5, %6 offen lds\n\tbuffer_load_dwordx4 %2, %5, %6 offen offset:1024 lds\n\t"
                   "buffer_load_dwordx4 %3, %5, %6 offen offset:2048 lds\n\tbuffer_load_dwordx4 %4, %5, %6 offen offset:3072 lds"
                   :: "s"(lb + R1 + w * 12288u + (rho % 3) * 4096u), "v"(xv0), "v"(xv1), "v"(xv2), "v"(xv3), "s"(rx), "s"(Tq * 32768u + ((rho + rot) & 7) * 128u) : "memory");
    };
    xround(0); xround(1);
    asm volatile("s_waitcnt vmcnt(8)" : "+v"(ep), "+v"(row) :: "memory");
    const unsigned e0 = __builtin_amdgcn_readfirstlane(ep.x) & 0xffff0000u, e1 = __builtin_amdgcn_readfirstlane(ep.y);
    tg.lo = e0 + 0x10000u; tg.hi = e1 + (tg.lo == 0u ? 1u : 0u);
    tg0 = tg;
    {
      const int g = lane >> 4, q = (lane & 15) >> 2, pp = lane & 3;
      const unsigned ta = ulds + pp * 8 + q * 128 + (uq ? (g & 1) * 2048 + (g >> 1) * 512 : (g >> 1) * 2048 + (g & 1) * 512);
      const unsigned dst = uq ? OFF_WQK + (4 * ucb * 32 + uk) * 1024u : OFF_WV + 4 * ucb * 16384u + uk * 512u, dstep = uq ? 32768u : 16384u;
      const unsigned so = (uq || uv) ? lane16 : OOB;
#pragma unroll
      for (int tl = 0; tl < 4; ++tl) st16(trfrag(L, ta + tl * 32, 1024), rs, so, dst + tl * dstep);
    }
    swap64(rs, (w | lane) == 0 ? 0u : OOB, FL_SEEN * 128u + un * 8u, v2u{tg.lo | 1u, tg.hi});
    xround(2);
    const unsigned t32 = (tg.lo >> 16) | (tg.hi << 16);
    {
      const unsigned a0 = row.x, a1 = row.y, a2 = row.z, a3 = row.w;
      const float s1 = wave_add(sum8(row, 0.f)), s2 = wave_add(dot2(a0, a0, dot2(a1, a1, dot2(a2, a2, dot2(a3, a3, 0.f)))));
      swap64(rs, lane < 2 ? lane * 8 : OOB, OFF_ST0 + token * 16u, v2u{__float_as_uint(lane ? s2 : s1), t32});
    }
    unsigned xa[4];
#pragma unroll
    for (int i = 0; i < 4; ++i) { xa[i] = R1 + w * 12288 + (r >> 3) * 1024 + (r & 7) * 128 + (((2 * i) ^ ((r >> 1) & 7)) << 4) + h * 8; OPQV(xa[i]); }
    v4u x0[32];
    v2u wf = {0u, 0u}, sgr = {0u, 0u};
    const unsigned wfo = w == 0 && lane < 48 ? lane * 8 : OOB, wfs = (FL_Q + (head >> 2) * 3) * 128u;
    auto wdma = [&] {
      dma4(rs, lb + R0 + w * 4096u, OFF_WQK + head * 32768u + w * 4096u, lane16);
      dma2(rs, lb + R4V + w * 2048u, OFF_WV + head * 16384u + w * 2048u, lane16);
    };
    bool early = false;
    auto wpoll = [&] { wf = v2u{0u, 0u}; asm volatile("s_nop 4\n\tbuffer_atomic_add_x2 %0, %1, %2, %3 offen sc0" : "+v"(wf) : "v"(wfo), "s"(rs), "s"(wfs) : "memory"); };
    SB();
    sfor<0, 8>([&](auto RHO) {
      constexpr int rho = RHO;
      if constexpr (rho == 0) VMWAIT(5);
      else if constexpr (rho == 1) {}
      else if constexpr (rho == 2) VMWAIT(10);
      else if constexpr (rho == 3) VMWAIT(9);
      else if constexpr (rho == 4) asm volatile("s_waitcnt vmcnt(8)" : "+v"(wf) :: "memory");
      else if constexpr (rho == 5) { if (early) VMWAIT(15); else VMWAIT(9); }
      else if constexpr (rho == 6) { if (early) VMWAIT(11); else VMWAIT(6); }
      else { if (early) VMWAIT(6); else VMWAIT(0); asm volatile("" : "+v"(wf), "+v"(sgr) :: "memory"); }
      v2u t[8];
#pragma unroll
      for (int i = 0; i < 4; ++i) { t[2 * i] = *(LAS v2u*)(L + xa[i] + (rho % 3) * 4096); t[2 * i + 1] = *(LAS v2u*)(L + (xa[i] ^ 16u) + (rho % 3) * 4096); }
      if constexpr (rho == 4) { if (w == 0 && flags_ok(wf, tg, 1u, lane, 48)) relay_set(L, tg, 1u); }
      asm volatile("s_waitcnt lgkmcnt(0)" : "+v"(t[0]), "+v"(t[1]), "+v"(t[2]), "+v"(t[3]), "+v"(t[4]), "+v"(t[5]), "+v"(t[6]), "+v"(t[7]) :: "memory");
      if constexpr (rho == 0) swap64(rs, (uq || uv) && lane == 0 ? 0u : OOB, (FL_Q + ucb * 3) * 128u + (uq ? uk : 32 + (uk >> 1)) * 8u, v2u{tg.lo | 1u, tg.hi});
      if constexpr (rho == 2) wpoll();
      if constexpr (rho + 3 < 8) xround(rho + 3);
      if constexpr (rho == 4) {
        asm volatile("s_nop 4\n\tbuffer_atomic_add_x2 %0, %1, %2, %3 offen sc0" : "+v"(sgr) : "v"(r * 16 + h * 8), "s"(rs), "s"(OFF_ST0 + Tq * 512u) : "memory");
        early = relay_get(L, tg, 1u); if (early) wdma();
      }
      if constexpr (rho == 5) { if (!early) wpoll(); }
      if constexpr (rho == 7) { if (!early && w == 0 && flags_ok(wf, tg, 1u, lane, 48)) relay_set(L, tg, 1u); }
#pragma unroll
      for (int i = 0; i < 4; ++i) { const unsigned a0 = t[2 * i].x, a1 = t[2 * i].y, a2 = t[2 * i + 1].x, a3 = t[2 * i + 1].y; x0[4 * rho + i] = v4u{a0, a1, a2, a3}; }
      if constexpr (rho >= 1 && rho < 5) { CAUSAL2(4 * (rho - 1)); CAUSAL2(4 * (rho - 1) + 2); }
      SB();
    });
    if (!early) {
      if (w == 0 && !relay_get(L, tg, 1u)) { wait_flags(rs, wfs, tg, 1u, lane, 48); relay_set(L, tg, 1u); }
      relay_wait(L, tg, 1u);
      wdma();
    }
    STAMP(0);
    if (!__all(sgr.y == t32)) {
#pragma nounroll
      for (unsigned spins = 0; spins < SPIN_CAP; ++spins) { sgr = poll_ld(rs, r * 16 + h * 8, OFF_ST0 + Tq * 512u); if (__all(sgr.y == t32)) break; }
    }
    const unsigned sv = sgr.x;
    const auto svs = __builtin_amdgcn_permlane32_swap(sv, sv, false, false);
    const Stats st = finish_stats(__uint_as_float(svs[0]), __uint_as_float(svs[1]), h);
    rstd1 = st.rstd;
    BOUNCE_WR(st);
    ONES_WR();
    BOUNCE_RD();
    VMWAIT(0);
    BAR();
    STAMP(2);
    PREP_ROLES();
    const kargs_t ka = kargs();
    const unsigned pld = uty == 0 ? FF * 2 : 1024;
    const rsrc_t ra = __builtin_amdgcn_make_buffer_rsrc((void*)(uty == 0 ? ka->wg : (uty == 1 ? ka->wd : ka->wo)), 0, uty == 2 ? DM * 1024 : FF * 1024, 0x00020000);
    const rsrc_t rb = __builtin_amdgcn_make_buffer_rsrc((void*)ka->wu, 0, FF * 1024, 0x00020000);
    const unsigned pvo = (lane >> 3) * pld + (lane & 7) * 16, pso = 16 * pks * pld + pcb * 128;
    unsigned vwa = R4V + ((lane >> 4) & 1) * 512 + (h * 16 + (lane & 15)) * 16; OPQV(vwa);
    float cq = 0.f, cvp = 0.f;
    v4u cf = lds16(L, R0 + lane16 + 4 * w * 1024);
    f32x2 cs = {0.f, 0.f}, cpart[4];
    v4u wq[4], wv[3];
    auto qaddr = [&](int s) { return R0 + lane16 + (((s >> 2) + rot) & 7) * 4096 + (s & 3) * 1024; };
    wq[0] = lds16(L, qaddr(0)); wq[1] = lds16(L, qaddr(1)); wq[2] = lds16(L, qaddr(2));
    cqk = f32x16{}; cva = f32x4{}; cvb = f32x4{};
    v2u aqk;
    SB();
    sfor<0, 32>([&](auto I) {
      constexpr int s = I;
      cqk = mfma16(wq[s & 3], x0[s], cqk); OPQV(cqk);
      if constexpr (s + 3 < 32) wq[(s + 3) & 3] = lds16(L, qaddr(s + 3));
      if constexpr (!(s & 1)) wv[(s >> 1) % 3] = lds16(L, vwa + (((s >> 2) + rot) & 7) * 2048 + (s & 2) * 512);
      if constexpr (!(s & 1) && s >= 2) swap16(x0[s - 2], x0[s - 1]);
      if constexpr ((s & 1) && s >= 3) {
        cva = mfma16s(x0[s - 3], wv[((s - 3) >> 1) % 3], cva); OPQV(cva);
        cvb = mfma16s(x0[s - 2], wv[((s - 3) >> 1) % 3], cvb); OPQV(cvb);
      }
      if constexpr (s >= 8 && s < 24 && !(s & 1)) {
        constexpr int i = (s - 8) >> 1;
        if (w < 4 && (i < 4 || uty == 0)) dma1p(i < 4 ? ra : rb, lb + R2 + w * 8192u + i * 1024u, pso + 8 * (i & 3) * pld, pvo);
      }
      if constexpr (s >= 1 && s < 7) {
        if constexpr (s < 5) { cq = sum8(cf, cq); OPQV(cq); } else { cvp = sum8(cf, cvp); OPQV(cvp); }
        if constexpr (s < 4) cf = lds16(L, R0 + lane16 + (4 * w + s) * 1024);
        else if constexpr (s < 6) cf = lds16(L, vwa + (2 * w + s - 4) * 1024);
        else *(LAS f32x2*)(L + R4S + 2048 + w * 512 + lane * 8) = f32x2{cq, cvp};
      }
      if constexpr (s == 12) BAR();
      if constexpr (s == 13 || s == 16) {
#pragma unroll
        for (int i = 0; i < 4; ++i) cpart[i] = *(LAS f32x2*)(L + R4S + 2048 + (i + (s == 16 ? 4 : 0)) * 512 + lane * 8);
      }
      if constexpr (s == 15 || s == 18) { cs += (cpart[0] + cpart[1]) + (cpart[2] + cpart[3]); OPQV(cs); }
      if constexpr (s == 20) { aqk = split_aug(xhalf_add(cs[0])); OPQV(aqk); }
      if constexpr (s == 22) {
        csv = xhalf_add(xrow_add(cs[1])); OPQV(csv);
        st16(v4u{aqk.x, aqk.y, __float_as_uint(csv), 0u}, rs, w == 4 ? lane16 : OOB, OFF_CSQ + un * 1024u);
      }
      if constexpr (s == 24) { cqk = mfma8(aqk, st.aug, cqk); OPQV(cqk); }
      SB();
    });
    swap16(x0[30], x0[31]);
    cva = mfma16s(x0[30], wv[15 % 3], cva); cvb = mfma16s(x0[31], wv[15 % 3], cvb);
  }

  for (int layer0 = 0;; ++layer0) {
    {
      ROLES(); LANE_CONSTS();
      const unsigned own = R1 + (8 + tbl) * 2048;
      STAMP(3);
      const bool up = (lane & 16) != 0;
      const unsigned kvb = OFF_KV + layer * (4 * MB) + (head * 64 + Tq) * 2048u;
      const unsigned kvo = sg < 7 ? lane16 : OOB;
      v4u qf, kf, vf;
      float q2 = 0.f, k2 = 0.f;
#pragma unroll
      for (int i = 0; i < 8; ++i) cqk[8 + i] *= rstd1;
      kf.x = cvtpk(cqk[8], cqk[9]); kf.y = cvtpk(cqk[10], cqk[11]); kf.z = cvtpk(cqk[12], cqk[13]); kf.w = cvtpk(cqk[14], cqk[15]);
      st16(kf, rs, kvo, kvb);
#pragma unroll
      for (int i = 0; i < 4; ++i) { cva[i] = (cva[i] - mma[i] * csv) * rra[i]; cvb[i] = (cvb[i] - mmb[i] * csv) * rrb[i]; }
      {
        const auto t0 = __builtin_amdgcn_permlane32_swap(cvtpk(cva[0], cva[1]), cvtpk(cvb[0], cvb[1]), false, false);
        const auto t1 = __builtin_amdgcn_permlane32_swap(cvtpk(cva[2], cva[3]), cvtpk(cvb[2], cvb[3]), false, false);
        vf = v4u{t0[0], t1[0], t0[1], t1[1]};
      }
      st16(vf, rs, kvo + 1024, kvb);
      SB();
      *(LAS v4u*)(L + own + lane16) = kf;
      *(LAS v4u*)(L + own + 1024 + lane16) = vf;
      const unsigned vstep = up ? 0u : 2048u, vbase = up ? R4S : R1 + 1024 + (h * 16 + (lane & 15)) * 16;
      const v4u vd0 = lds16(L, vbase + vstep * (8 + tbl)), vd1 = lds16(L, vbase + vstep * (8 + tbl) + 512);
      const float qs = rstd1 * (0.25f * LOG2E);
#pragma unroll
      for (int i = 0; i < 8; ++i) { cqk[i] *= qs; q2 += cqk[i] * cqk[i]; k2 += cqk[8 + i] * cqk[8 + i]; }
      qf.x = cvtpk(cqk[0], cqk[1]); qf.y = cvtpk(cqk[2], cqk[3]); qf.z = cvtpk(cqk[4], cqk[5]); qf.w = cvtpk(cqk[6], cqk[7]);
      const f32x16 sd = mfma16(kf, qf, cm);
      q2 = xhalf_add(q2) * 1.02f;
      const unsigned kr = row_umax(__float_as_uint(xhalf_add(k2) * 1.02f));
      const unsigned km0 = (umax(__builtin_amdgcn_readlane(kr, 0), __builtin_amdgcn_readlane(kr, 16)) >> 16) + 1u;
      const unsigned kmax16 = km0 < 0x7fc0u ? km0 : 0x7fc0u;
      *(LAS unsigned*)(L + R4S + 1024 + (8 + tbl) * 4) = kmax16 << 16;
      const unsigned fkv = (FL_KV + (layer * 32 + head) * 8 + sg) * 128u;
      f32x16 o = {};
      bool ok = __all(q2 * __uint_as_float(kmax16 << 16) <= SAFE2);
      exp_pv(sd, vd0, vd1, o);
      if (w < 4) {
        VMWAIT(0);
        if (sg < 7) set_flag(rs, fkv + tbl * 8, tg, kmax16, lane);
      }
      STAMP(4);
      BAR();
      STAMP(5);
      unsigned kaddr = R1 + lane16; OPQV(kaddr);
      ok = ok && __all(q2 * __uint_as_float(row_umax(*(LAS unsigned*)(L + R4S + 1024 + (8 + (lane & 7)) * 4))) <= SAFE2);
      auto late_kv = [&] { VMWAIT(0); if (sg < 7) set_flag(rs, fkv + tbl * 8, tg, kmax16, lane); };
      if (ok && tbl > 0) attn_full<8>(L, qf, tbl, kaddr, vbase, vstep, o, [&](auto K) { if (K == 2 && w >= 4) late_kv(); });
      if (!ok && w >= 4) late_kv();
      STAMP(6);
      if (w < 4) {
        if (layer == 0) {
          PREP_ROLES();
          const int g = lane >> 4, q = (lane & 15) >> 2, pp = lane & 3;
          const unsigned ta = R2 + w * 8192 + (4 * (g >> 1) + q) * 128 + pp * 8 + (g & 1) * (uty == 0 ? 4096 : 32), tstep = uty == 0 ? 32 : 64;
          const unsigned dstep = uty == 1 ? 1024u : 32768u;
          float cp[4] = {0.f, 0.f, 0.f, 0.f};
#pragma unroll
          for (int ki = 0; ki < 2; ++ki) {
            const int ks = pks + ki, mm = ks / 7, cc = ks % 7;
            const unsigned dst = uty == 0 ? OFF_WGU + (4 * pcb * 32 + ks) * 1024u
                               : (uty == 1 ? OFF_WD + (pcb * 224 + cc * 32 + (mm >> 3) * 16 + (mm & 7) * 2) * 1024u : OFF_WO + (pcb * 64 + ks) * 1024u);
#pragma unroll
            for (int tl = 0; tl < 4; ++tl) {
              if (tl < 2 || uty == 0) {
                const v4u f = trfrag(L, ta + ki * 2048 + tl * tstep, 1024);
                st16(f, rs, lane16, dst + tl * dstep);
                cp[tl] = sum8(f, cp[tl]);
              }
            }
          }
#pragma unroll
          for (int tl = 0; tl < 4; ++tl) *(LAS float*)(L + R4S + 2048 + ((w * 4 + tl) * 32 + r) * 4) = xhalf_add(cp[tl]);
        }
        if (sg > 0) {
          const v2u f = wait_flags(rs, fkv - 128 + 16 * w, tg, 1u, lane, 2);
          dma4(rs, lb + R1 + w * 4096u, OFF_KV + layer * (4 * MB) + (head * 64 + 8 * (sg - 1) + 2 * w) * 2048u, lane16);
          if (lane < 2) *(LAS unsigned*)(L + R4S + 1024 + (2 * w + lane) * 4) = f.x << 16;
        }
        STAMP(7);
        VMWAIT(0);
      }
      STAMP(8);
      BAR();
      STAMP(9);
      if (layer == 0) {
        if (w == 7) {
          PREP_ROLES(); (void)pks;
          if (uty == 0) {
            f32x2 cs = {0.f, 0.f};
#pragma unroll
            for (int i = 0; i < 4; ++i) { cs[0] += *(LAS float*)(L + R4S + 2048 + (i * 128 + lane) * 4); cs[1] += *(LAS float*)(L + R4S + 2048 + (i * 128 + 64 + lane) * 4); }
            __builtin_amdgcn_raw_buffer_store_b32(__float_as_uint(cs[0]), rs, lane16, OFF_AUG + pcb * 2048u + pkq * 4u, 16);
            __builtin_amdgcn_raw_buffer_store_b32(__float_as_uint(cs[1]), rs, lane16 + 1024, OFF_AUG + pcb * 2048u + pkq * 4u, 16);
            VMWAIT(0);
          }
          set_flag(rs, (FL_P + (uty == 0 ? 0 : (uty == 1 ? 28 : 36)) + pcb) * 128u + pkq * 8u, tg, 1u, lane);
        }
      } else if (w >= 4) {
        dma_tile(rs, lb + R2, OFF_WO + nblk * 65536u, w - 4, lane16);
        dma_tile(rs, lb + R3, OFF_WO + nblk * 65536u + 32768u, w - 4, lane16);
        dma_tile(rs, lb + R0, OFF_WGU + mem * 7 * 32768u, w - 4, lane16);
      }
      if (sg > 0) ok = ok && __all(q2 * __uint_as_float(row_umax(*(LAS unsigned*)(L + R4S + 1024 + (lane & 7) * 4))) <= SAFE2);
      if (ok) {
        if (sg > 0) {
#pragma unroll
          for (int i = 0; i < 16; ++i) cm[i] = NEG_BIG - cm[i];
          attn_bounded<7, -1>(L, qf, cm, tbl, 7 - tbl, kaddr, vbase, vstep, o, [&](auto) {});
        }
      } else {
        float m = NEG_BIG;
#pragma unroll
        for (int i = 0; i < 16; ++i) o[i] = 0.f;
        attn_general(L, qf, cm, 8 + tbl, 8, tbl, kaddr, vbase, vstep, m, o);
        if (sg > 0) {
#pragma unroll
          for (int i = 0; i < 16; ++i) cm[i] = NEG_BIG - cm[i];
          attn_general(L, qf, cm, tbl, tbl + 1, 7 - tbl, kaddr, vbase, vstep, m, o);
        }
      }
      STAMP(10);
      const float il = __builtin_amdgcn_rcpf(o[8]);
      v4u of;
      of.x = cvtpk(o[0] * il, o[1] * il); of.y = cvtpk(o[2] * il, o[3] * il); of.z = cvtpk(o[4] * il, o[5] * il); of.w = cvtpk(o[6] * il, o[7] * il);
      st16(of, rs, lane16, OFF_O + layer * (2 * MB) + (Tq * 32 + head) * 1024u);
      VMWAIT(0);
      set_flag(rs, (FL_O + (layer * 64 + Tq) * 2 + (head >> 4)) * 128u + (head & 15) * 8, tg, 1u, lane);
      STAMP(11);
    }
    if (w0 >= 4) {
      ROLES(); LANE_CONSTS();
      const unsigned lw = w - 4;
      const unsigned gu = OFF_WGU + mem * 7 * 32768u, wd = OFF_WD + nblk * 7 * 32768u;
      unsigned wlo = lw * 8192u + lane16, whi = wlo + R2; OPQV(wlo); OPQV(whi);
      v4u sa[4][8];
      auto stage = [&](auto B, unsigned src, auto PACED) {
        constexpr int b = B;
        sfor<0, 8>([&](auto I) {
          constexpr int i = I;
          sa[b][i] = ld16(rs, lane16 + (i & 3) * 1024, src + lw * 8192u + (i >> 2) * 4096u);
          if constexpr (PACED) asm volatile(DMA_W ::: "memory"); else SB();
        });
      };
      auto put = [&](auto B, unsigned base) { constexpr int b = B; sfor<0, 8>([&](auto I) { constexpr int i = I; *(LAS v4u*)(L + base + i * 1024) = sa[b][i]; }); };
      if (layer) {
        stage(IC(0), gu + 1 * 32768u, IC(0));
        BAR();
        stage(IC(1), gu + 2 * 32768u, IC(1));
        put(IC(0), wlo + R1);
        stage(IC(2), gu + 3 * 32768u, IC(1));
        STAMP(14); BAR();
      } else {
        if (lw == 3) { wait_flags(rs, (FL_P + 36 + nblk) * 128u, tg, 1u, lane, 4); relay_set(L, tg, 2u); }
        else relay_wait(L, tg, 2u);
        dma_tile(rs, lb + R2, OFF_WO + nblk * 65536u, lw, lane16);
        dma_tile(rs, lb + R3, OFF_WO + nblk * 65536u + 32768u, lw, lane16);
        BAR();
        if (lw == 0) {
          const unsigned c0 = (7 * mem) >> 2, nw = 4 * (((7 * mem + 6) >> 2) - c0 + 1);
          wait_words(rs, (unsigned)lane < nw ? (lane >> 2) * 128 + (lane & 3) * 8 : OOB, (FL_P + c0) * 128u, tg, 1u, (unsigned)lane < nw);
        }
        VMWAIT(0);
        BAR();
        dma_tile(rs, lb + R0, gu, lw, lane16);
        stage(IC(0), gu + 1 * 32768u, IC(1));
        STAMP(14); BAR();
        stage(IC(1), gu + 2 * 32768u, IC(1));
        put(IC(0), wlo + R1);
        stage(IC(2), gu + 3 * 32768u, IC(1));
      }
      put(IC(1), whi); put(IC(2), whi + 32768u);
      STAMP(17); BAR();
      if (layer == 0) {
        if (lw == 0) wait_flags(rs, (FL_P + 28 + nblk) * 128u, tg, 1u, lane, 14);
      }
      STAMP(18); BAR();
      dma_tile(rs, lb + R0, gu + 4 * 32768u, lw, lane16);
      dma_tile(rs, lb + R1, gu + 5 * 32768u, lw, lane16);
      STAMP(19); BAR();
      dma_tile(rs, lb + R2, gu + 6 * 32768u, lw, lane16);
      stage(IC(0), wd + 3 * 32768u, IC(0));
      VMWAIT(24); STAMP(20); BAR();
      dma_tile(rs, lb + R3, wd + 0 * 32768u, lw, lane16);
      stage(IC(1), wd + 4 * 32768u, IC(0));
      VMWAIT(32); BAR();
      dma_tile(rs, lb + R0, wd + 1 * 32768u, lw, lane16);
      stage(IC(2), wd + 5 * 32768u, IC(0));
      VMWAIT(40); STAMP(21); BAR();
      dma_tile(rs, lb + R1, wd + 2 * 32768u, lw, lane16);
      stage(IC(3), wd + 6 * 32768u, IC(0));
      VMWAIT(40); STAMP(22); BAR();
      put(IC(0), whi);
      VMWAIT(24); STAMP(25); BAR();
      put(IC(1), whi + 32768u);
      VMWAIT(8); BAR();
      put(IC(2), wlo);
      STAMP(26); BAR();
      put(IC(3), wlo + R1);
      BAR();
      STAMP(27); BAR();
      BAR();
      STAMP(28); BAR();
      if (layer == 0) {
        dma4_paced(rs, lb + R0 + lw * 8192u, OFF_WQK + head * 32768u + lw * 8192u, lane16);
        dma4_paced(rs, lb + R0 + lw * 8192u + 4096u, OFF_WQK + head * 32768u + lw * 8192u + 4096u, lane16);
        dma4(rs, lb + R4V + lw * 4096u, OFF_WV + head * 16384u + lw * 4096u, lane16);
      } else if ((sg | j) == 0 && w == 4) {
#pragma unroll
        for (int i = 0; i < 4; ++i) wait_flags(rs, FL_SEEN * 128u + i * 512u, tg, 1u, lane, 64);
        set_flag(rs, FL_EPOCH * 128u, tg, 0u, lane);
      }
    } else {
      BAR();
      STAMP(12);
      {
        ROLES(); LANE_CONSTS();
        const unsigned xfo = (tb2 * 32 + 4 * nblk + 2 * kh) * 1024u;
        v4u r0, r1;
        if (layer) { r0 = ld16(rs, lane16, OFF_X2 + xfo); r1 = ld16(rs, lane16 + 1024, OFF_X2 + xfo); }
        else {
          const rsrc_t rx = __builtin_amdgcn_make_buffer_rsrc((void*)kargs()->x, 0, SEQ * DM * 2, 0x00020000);
          const unsigned vo = r * 1024 + h * 8, so = tb2 * 32768u + nblk * 128u + kh * 64u;
          const v2u t0 = ld8p(rx, vo, so), t1 = ld8p(rx, vo + 16, so), t2 = ld8p(rx, vo + 32, so), t3 = ld8p(rx, vo + 48, so);
          r0 = v4u{t0.x, t0.y, t1.x, t1.y}; r1 = v4u{t2.x, t2.y, t3.x, t3.y};
        }
        unsigned wa0 = R2 + kh * (16384 + 32768) + lane16, wa1 = wa0 ^ 32768u; OPQV(wa0); OPQV(wa1);
        auto wread = [&](auto U) -> v4u { constexpr int u = U; return lds16(L, ((u & 1) ? wa1 : wa0) + (u >> 1) * 1024); };
        v4u ar[6];
        f32x16 c0 = unpack_tile(r0, r1), c1 = {};
        OPQV(c0);
        wait_flags(rs, (FL_O + (layer * 64 + tb2) * 2 + kh) * 128u, tg, 1u, lane, 16);
        STAMP(13);
        const unsigned ob = OFF_O + layer * (2 * MB) + (tb2 * 32 + 16 * kh) * 1024u;
        auto oload = [&](auto I) -> v4u { constexpr int i = I; return ld16(rs, lane16 + (i & 3) * 1024, ob + (i >> 2) * 4096u); };
        v4u ofr[16];
        sfor<0, D2>([&](auto I) { constexpr int i = I; ofr[i] = oload(I); });
        if (layer == 0) BAR();
        sfor<0, 6>([&](auto I) { constexpr int u = I; ar[u] = wread(I); });
        SB();
        sfor<0, 32>([&](auto I) {
          constexpr int u = I;
          if constexpr (u & 1) { c1 = mfma16(ar[u % 6], ofr[u >> 1], c1); OPQV(c1); } else { c0 = mfma16(ar[u % 6], ofr[u >> 1], c0); OPQV(c0); }
          if constexpr (u + 6 < 32) ar[u % 6] = wread(IC(u + 6));
          if constexpr (!(u & 1) && (u >> 1) + D2 < 16) ofr[(u >> 1) + D2] = oload(IC((u >> 1) + D2));
          SB();
        });
        unsigned sown = R4S + cw * 4096 + lane16; OPQV(sown);
#pragma unroll
        for (int g = 0; g < 4; ++g) *(LAS f32x4*)(L + sown + g * 1024) = f32x4{c1[4 * g], c1[4 * g + 1], c1[4 * g + 2], c1[4 * g + 3]};
        OPQV(c0);
        STAMP(14);
        BAR();
        STAMP(15);
#pragma unroll
        for (int g = 0; g < 4; ++g) { const f32x4 t = *(LAS f32x4*)(L + (sown ^ 4096u) + g * 1024); c0[4 * g] += t[0]; c0[4 * g + 1] += t[1]; c0[4 * g + 2] += t[2]; c0[4 * g + 3] += t[3]; }
        v4u y0, y1;
        pack_tile(c0, y0, y1);
        const unsigned xo = OFF_X1 + layer * (2 * MB) + xfo;
        st16(y0, rs, lane16, xo); st16(y1, rs, lane16 + 1024, xo);
        st8(tile_stats(c0), rs, h ? OOB : r * 8, OFF_ST + ((layer * 128 + tb2) * 16 + 2 * nblk + kh) * 256u);
        VMWAIT(0);
        set_flag(rs, (FL_X + layer * 128 + tb2) * 128u + (2 * nblk + kh) * 8, tg, 1u, lane);
      }
      {
        ROLES(); LANE_CONSTS();
        STAMP(16);
        v2u fpre;
        v4u ar[6];
        unsigned lhi = lane16 + R2; OPQV(lhi);
        unsigned lp0 = lane16 + kh * (16384 + 1024), lp1 = lp0 ^ 1024u;
        OPQV(lp0); OPQV(lp1);
        auto aread = [&](auto U) -> v4u {
          constexpr int u = U;
          if constexpr (u < 64) return lds16(L, ((u & 1) ? R1 : R0) + (u >> 1) * 1024 + lane16);
          else if constexpr (u < 224) {
            constexpr int T = 2 + (u - 64) / 32, s = (u - 64) % 32;
            if constexpr (T == 4 || T == 5) return lds16(L, (T - 4) * 32768 + s * 1024 + lane16);
            else return lds16(L, (T == 3 ? 32768 : 0) + s * 1024 + lhi);
          } else return lds16(L, R3 + ((u - 224) >> 1) * 2048 + ((u & 1) ? lp1 : lp0));
        };
        const unsigned augb = OFF_AUG + mem * 7 * 512u;
        v4u pg0 = ld16(rs, r * 16, augb), pg1 = ld16(rs, r * 16 + 512, augb);
        v2u agn = make_aug(pg0), ag1 = make_aug(pg1);
        OPQV(agn); OPQV(ag1);
        wait_flags(rs, (FL_X + layer * 128 + tb3) * 128u, tg, 1u, lane, 16);
        STAMP(17);
        v2u sp[8];
        const unsigned xb = OFF_X1 + layer * (2 * MB) + tb3 * 32768u;
        auto xload = [&](auto I) -> v4u { constexpr int i = I; return ld16(rs, lane16 + (i & 3) * 1024, xb + (i >> 2) * 4096u); };
        v4u x[32];
        sfor<0, D3>([&](auto I) { constexpr int i = I; x[i] = xload(I); });
        STAMP(23);
        BAR();
        STAMP(24);
        sfor<0, 6>([&](auto I) { constexpr int u = I; ar[u] = aread(I); });
        SB();
        const unsigned hb = OFF_H + layer * (7 * MB) + (tb3 * 112 + mem * 7) * 1024u;
        const unsigned fh_mine = (FL_H + (layer * 64 + tb3) * 2 + (mem >> 3)) * 128u + (mem & 7) * 8;
        const unsigned fh_poll = (FL_H + (layer * 64 + tb2) * 2 + kh) * 128u;
        Stats st;
        float cg, cu;
        f32x16 acc[3] = {};
        float ee[4], uu[4]; v4u hf;
        auto epi = [&](auto P_, auto E_, auto G_) {
          constexpr int p = P_, e = E_, g = G_;
          if constexpr (g == 0) { ee[e & 3] = ex2(acc[p % 3][e] * cg); OPQV(ee[e & 3]); }
          if constexpr (g == 1) { ee[e & 3] = __builtin_amdgcn_rcpf(1.0f + ee[e & 3]); uu[e & 3] = acc[p % 3][8 + e] * cu; OPQV(ee[e & 3]); OPQV(uu[e & 3]); }
          if constexpr (g == 2) {
            uu[e & 3] *= ee[e & 3]; OPQV(uu[e & 3]);
            if constexpr (e & 1) hf[e >> 1] = cvtpk(uu[(e - 1) & 3], uu[e & 3]);
            if constexpr (e == 7) st16(hf, rs, lane16 + (p & 3) * 1024, hb + (p >> 2) * 4096);
          }
        };
        SB();
        sfor<0, 224>([&](auto I) {
          constexpr int u = I;
          constexpr int T = u < 64 ? (u & 1) : 2 + (u - 64) / 32, s = u < 64 ? (u >> 1) : (u - 64) % 32;
          constexpr int left = u < 64 ? 64 - u : 32 - s;
          if constexpr (left == 4) {
            BAR();
            ar[(u + 4) % 6] = aread(IC(u + 4));
            ar[(u + 5) % 6] = aread(IC(u + 5));
          }
          if constexpr (u == 64) STAMP(18);
          if constexpr (u == 96) STAMP(19);
          if constexpr (u == 128) STAMP(20);
          if constexpr (u == 192) STAMP(21);
          if constexpr (u >= 64 && s == 0) { const f32x16 z = {}; acc[T % 3] = mfma8(agn, st.aug, z); }
          acc[T % 3] = mfma16(ar[u % 6], x[s], acc[T % 3]); OPQV(acc[T % 3]);
          if constexpr (left > 6 || left <= 4) ar[u % 6] = aread(IC(u + 6));
          if constexpr (u < 64 && !(u & 1) && s + D3 < 32) x[s + D3] = xload(IC(s + D3));
          if constexpr (u < 16 && (u & 1)) sp[s] = ld8(rs, h * 2048 + r * 8 + s * 256, OFF_ST + (layer * 128 + tb3) * 4096u);
          if constexpr (u == 31) { st = make_stats(sp, h); cg = st.rstd * -LOG2E; cu = st.rstd; OPQV(cg); OPQV(st.aug); }
          if constexpr (u == 36) { acc[1] = mfma8(ag1, st.aug, acc[1]); OPQV(acc[1]); }
          if constexpr (u == 37) { acc[0] = mfma8(agn, st.aug, acc[0]); OPQV(acc[0]); pg0 = ld16(rs, r * 16 + 1024, augb); }
          if constexpr (left == 5 && u < 200) { agn = make_aug(pg0); OPQV(agn); }
          if constexpr (left == 3 && u < 168) { constexpr int n2 = (u < 64 ? 3 : T + 2); pg0 = ld16(rs, r * 16 + (n2 & 1) * 512, augb + (n2 >> 1) * 1024); }
          if constexpr (T == 2 && u >= 64) {
            sfor<0, 16>([&](auto Q) { constexpr int q = Q, g = s - 2 - (3 * q) / 2; if constexpr (g >= 0 && g < 3) epi(IC(q / 8), IC(q % 8), IC(g)); });
          } else if constexpr (T > 2) {
            sfor<0, 8>([&](auto Q) { constexpr int q = Q, g = s - 2 - 3 * q; if constexpr (g >= 0 && g < 3) epi(IC(T - 1), Q, IC(g)); });
            if constexpr (s == 27 && T == 5) { VMWAIT(1); set_flag(rs, fh_mine, tg, 4u, lane); }
            if constexpr (s == 14 && T == 6) fpre = poll_ld(rs, lane < 8 ? lane * 8 : OOB, fh_poll);
            if constexpr (s == 27 && T == 6) { VMWAIT(1); set_flag(rs, fh_mine, tg, 5u, lane); }
          }
          SB();
        });
        STAMP(22);
        const unsigned xfo = (tb2 * 32 + 4 * nblk + 2 * kh) * 1024u;
        if (!flags_ok(fpre, tg, 4u, lane, 8)) wait_flags(rs, fh_poll, tg, 4u, lane, 8);
        const unsigned hb4 = OFF_H + layer * (7 * MB) + (tb2 * 112 + 56 * kh) * 1024u;
        v4u hr[D4];
        unsigned lane16b = lane16 + 4096; OPQV(lane16b);
        auto hload = [&](auto Q) {
          constexpr int q = Q, c = q >> 3;
          hr[q % D4] = ld16(rs, (c < 4 ? lane16 : lane16b) + (c & 3) * 1024, hb4 + (q & 7) * 7168u);
        };
        const unsigned xr1 = OFF_X1 + layer * (2 * MB) + xfo;
        const v4u r0 = ld16(rs, lane16, xr1), r1 = ld16(rs, lane16 + 1024, xr1);
        sfor<0, 6>(hload);
        SB();
        sfor<0, 10>([&](auto S_) { sfor<0, 8>([&](auto Q) { constexpr int g = S_ - Q; if constexpr (g >= 0 && g < 3) epi(IC(6), Q, IC(g)); }); });
        sfor<6, D4>(hload);
        f32x16 c0 = unpack_tile(r0, r1), c1 = {};
        unsigned lp0h = lp0 + R2, lp1h = lp1 + R2; OPQV(lp0h); OPQV(lp1h);
        auto dread = [&](auto U) -> v4u {
          constexpr int u = U, c = u / 16, i = (u % 16) >> 1;
          if constexpr (c == 0 || c == 4) return lds16(L, 32768 + i * 2048 + ((u & 1) ? lp1h : lp0h));
          else if constexpr (c == 3) return lds16(L, i * 2048 + ((u & 1) ? lp1h : lp0h));
          else return lds16(L, ((c == 2 || c == 6) ? 32768 : 0) + i * 2048 + ((u & 1) ? lp1 : lp0));
        };
        SB();
        sfor<0, 112>([&](auto I) {
          constexpr int u = I, c = u / 16, v = u % 16, q = u >> 1;
          if constexpr (v == 12 && c < 6) {
            BAR();
            ar[(224 + u + 4) % 6] = dread(IC(u + 4));
            ar[(224 + u + 5) % 6] = dread(IC(u + 5));
          }
          constexpr int U5 = 2 * (32 - D4) - 1, U7 = 2 * (40 - D4) - 1;
          if constexpr (u == U5 - 15 || u == U7 - 15) fpre = poll_ld(rs, lane < 8 ? lane * 8 : OOB, fh_poll);
          if constexpr (u == 16) STAMP(25);
          if constexpr (u == 48) STAMP(26);
          if constexpr (u == 80) STAMP(27);
          if constexpr (u & 1) { c1 = mfma16(ar[(224 + u) % 6], hr[q % D4], c1); OPQV(c1); } else { c0 = mfma16(ar[(224 + u) % 6], hr[q % D4], c0); OPQV(c0); }
          if constexpr ((v < 10 || v >= 12) && u + 6 < 112) ar[(224 + u) % 6] = dread(IC(u + 6));
          if constexpr (u == 11) { asm volatile("s_waitcnt vmcnt(%0)" :: "i"(D4 - 6 + 5) : "memory"); set_flag(rs, fh_mine, tg, 7u, lane); }
          if constexpr (u == U5) { if (!flags_ok(fpre, tg, 5u, lane, 8)) wait_flags(rs, fh_poll, tg, 5u, lane, 8); }
          if constexpr (u == U7) { if (!flags_ok(fpre, tg, 7u, lane, 8)) wait_flags(rs, fh_poll, tg, 7u, lane, 8); }
          if constexpr ((u & 1) && q + D4 < 56) hload(IC(q + D4));
          SB();
        });
        unsigned sown = R4S + cw * 4096 + lane16; OPQV(sown);
#pragma unroll
        for (int g = 0; g < 4; ++g) *(LAS f32x4*)(L + sown + g * 1024) = f32x4{c1[4 * g], c1[4 * g + 1], c1[4 * g + 2], c1[4 * g + 3]};
        OPQV(c0);
        STAMP(28);
        BAR();
        STAMP(29);
#pragma unroll
        for (int g = 0; g < 4; ++g) { const f32x4 t = *(LAS f32x4*)(L + (sown ^ 4096u) + g * 1024); c0[4 * g] += t[0]; c0[4 * g + 1] += t[1]; c0[4 * g + 2] += t[2]; c0[4 * g + 3] += t[3]; }
        v4u y0, y1;
        pack_tile(c0, y0, y1);
        if (layer == 0) {
          st16(y0, rs, lane16, OFF_X2 + xfo); st16(y1, rs, lane16 + 1024, OFF_X2 + xfo);
          st8(tile_stats(c0), rs, h ? OOB : r * 8, OFF_ST + ((64 + tb2) * 16 + 2 * nblk + kh) * 256u);
          VMWAIT(0);
          set_flag(rs, (FL_X + 64 + tb2) * 128u + (2 * nblk + kh) * 8, tg, 1u, lane);
        } else {
          const rsrc_t ro = __builtin_amdgcn_make_buffer_rsrc((void*)kargs()->out, 0, SEQ * DM * 2, 0x00020000);
          const unsigned a0 = y0.x, a1 = y0.y, a2 = y0.z, a3 = y0.w, b0 = y1.x, b1 = y1.y, b2 = y1.z, b3 = y1.w;
          const auto p0 = __builtin_amdgcn_permlane32_swap(a0, a2, false, false), p1 = __builtin_amdgcn_permlane32_swap(a1, a3, false, false);
          const auto p2 = __builtin_amdgcn_permlane32_swap(b0, b2, false, false), p3 = __builtin_amdgcn_permlane32_swap(b1, b3, false, false);
          const unsigned vo = r * 1024 + h * 16, so = tb2 * 32768u + nblk * 128u + kh * 64u;
          const v4u z0 = {p0[0], p1[0], p0[1], p1[1]}, z1 = {p2[0], p3[0], p2[1], p3[1]};
          __builtin_amdgcn_raw_buffer_store_b128(z0, ro, vo, so, OUTAUX); __builtin_amdgcn_raw_buffer_store_b128(z1, ro, vo + 32, so, OUTAUX);
          asm volatile("s_nop 1" :: "v"(z0), "v"(z1) : "memory");
        }
        STAMP(30);
      }
    }
    if (layer0 == 1) break;
    {
      ROLES(); LANE_CONSTS();
      STAMP(32);
        const unsigned xb = OFF_X2 + Tq * 32768u;
        const v4u pcs = ld16(rs, lane16, OFF_CSQ + (sg * 32 + j) * 1024u);
        wait_flags(rs, (FL_X + 64 + Tq) * 128u, tg, 1u, lane, 16);
        STAMP(33);
        auto xload = [&](auto I) -> v4u { constexpr int i = I; return ld16(rs, lane16 + (i & 3) * 1024, xb + (i >> 2) * 4096u); };
        v4u xr[D1];
        sfor<0, D1>([&](auto I) { constexpr int i = I; xr[i] = xload(I); });
        v2u sp[8];
        asm volatile("s_waitcnt vmcnt(%0)" :: "i"(D1) : "memory");
        BAR();
        STAMP(34);
        ONES_WR();
        unsigned vwa = R4V + ((lane >> 4) & 1) * 512 + (h * 16 + (lane & 15)) * 16; OPQV(vwa);
        v4u wq[4], wv[3];
        wq[0] = lds16(L, R0 + lane16); wq[1] = lds16(L, R0 + lane16 + 1024); wq[2] = lds16(L, R0 + lane16 + 2048);
        csv = __uint_as_float(pcs.z);
        Stats st;
        cqk = f32x16{}; cva = f32x4{}; cvb = f32x4{};
        SB();
        sfor<0, 32>([&](auto I) {
          constexpr int s = I;
          cqk = mfma16(wq[s & 3], xr[s % D1], cqk); OPQV(cqk);
          if constexpr (s + 3 < 32) wq[(s + 3) & 3] = lds16(L, R0 + lane16 + (s + 3) * 1024);
          if constexpr (!(s & 1)) wv[(s >> 1) % 3] = lds16(L, vwa + s * 512);
          if constexpr (!(s & 1) && s >= 2) swap16(xr[(s - 2) % D1], xr[(s - 1) % D1]);
          if constexpr ((s & 1) && s >= 3) {
            cva = mfma16s(xr[(s - 3) % D1], wv[((s - 3) >> 1) % 3], cva); OPQV(cva);
            cvb = mfma16s(xr[(s - 2) % D1], wv[((s - 3) >> 1) % 3], cvb); OPQV(cvb);
            if constexpr (s - 3 + D1 < 32) xr[(s - 3) % D1] = xload(IC(s - 3 + D1));
            if constexpr (s - 2 + D1 < 32) xr[(s - 2) % D1] = xload(IC(s - 2 + D1));
          }
          if constexpr (s >= 4 && s < 12) sp[s - 4] = ld8(rs, h * 2048 + r * 8 + (s - 4) * 256, OFF_ST + (64 + Tq) * 4096u);
          if constexpr (s == 20) { st = make_stats(sp, h); OPQV(st.aug); rstd1 = st.rstd; BOUNCE_WR(st); }
          if constexpr (s == 22) { cqk = mfma8(v2u{pcs.x, pcs.y}, st.aug, cqk); OPQV(cqk); BOUNCE_RD(); }
          if constexpr (s >= 23 && s < 31) CAUSAL2(2 * (s - 23));
          SB();
        });
        swap16(xr[30 % D1], xr[31 % D1]);
        cva = mfma16s(xr[30 % D1], wv[15 % 3], cva); cvb = mfma16s(xr[31 % D1], wv[15 % 3], cvb);
    }
  }
  STAMP_OUT();
}

extern "C" void kernel_launch(void* const* d_in, const int* in_sizes, int n_in, void* d_out, int out_size, void* d_ws, size_t ws_size,
                              hipStream_t stream) {
  Args a;
  a.ws = (unsigned char*)d_ws; a.out = (u16*)d_out;
  a.x = (const u16*)d_in[0]; a.wqkv = (const u16*)d_in[1]; a.wo = (const u16*)d_in[2];
  a.wg = (const u16*)d_in[4]; a.wu = (const u16*)d_in[5]; a.wd = (const u16*)d_in[6];
  (void)hipFuncSetAttribute((const void*)main_k, hipFuncAttributeMaxDynamicSharedMemorySize, LDS_BYTES);
  main_k<<<256, 512, LDS_BYTES, stream>>>(a);
}
```

```cpp
#include <hip/hip_runtime.h>
#include <stdint.h>
#include <type_traits>

typedef unsigned short u16;
typedef float f32x16 __attribute__((ext_vector_type(16)));
typedef float f32x4 __attribute__((ext_vector_type(4)));
typedef float f32x2 __attribute__((ext_vector_type(2)));
typedef __bf16 bf16x8 __attribute__((ext_vector_type(8)));
typedef __bf16 bf16x2 __attribute__((ext_vector_type(2)));
typedef short s16x4 __attribute__((ext_vector_type(4)));
typedef unsigned v4u __attribute__((ext_vector_type(4)));
typedef unsigned v2u __attribute__((ext_vector_type(2)));
#define GAS __attribute__((address_space(1)))
#define LAS __attribute__((address_space(3)))
#define DI __device__ __forceinline__
#define RLX_AGENT __ATOMIC_RELAXED, __HIP_MEMORY_SCOPE_AGENT
#define IC(n) std::integral_constant<int, (n)>{}
typedef __amdgpu_buffer_rsrc_t rsrc_t;

constexpr int SEQ = 2048, DM = 512, FF = 1792;
constexpr unsigned KB = 1024u, MB = 1u << 20;
constexpr unsigned FL_EPOCH = 0;
constexpr unsigned FL_SEEN = 2;
constexpr unsigned FL_P = 32;
#define RQV 4
#define RXV 4
#define ROV 2
constexpr unsigned RQ = RQV, RX = RXV, RO = ROV;
constexpr unsigned FL_Q = FL_P + 44;
constexpr unsigned FL_X = FL_Q + RQ * 24;
constexpr unsigned FL_O = FL_X + 2 * RX * 192;
constexpr unsigned FL_H = FL_O + RO * 256;
constexpr unsigned FL_KV = FL_H + RO * 256;
constexpr unsigned FL_LINES = FL_KV + 2 * 32 * 8;
static_assert(FL_LINES * 128 <= 768 * KB, "flags");
constexpr unsigned OFF_AUG = 768 * KB;
constexpr unsigned OFF_ST0 = 896 * KB;
constexpr unsigned OFF_CSQ = 10 * MB;
constexpr unsigned OFF_CSP = 10 * MB + 512 * KB;
constexpr unsigned OFF_WQK = 1 * MB, OFF_WV = 2 * MB, OFF_WO = 2 * MB + 512 * KB, OFF_WGU = 3 * MB, OFF_WD = 6 * MB + 512 * KB;
constexpr unsigned OFF_ST = 9 * MB;
constexpr unsigned OFF_X1 = 11 * MB, OFF_X2 = 15 * MB, OFF_O = 17 * MB, OFF_KV = 21 * MB, OFF_H = 29 * MB;
constexpr unsigned WS_BYTES = 43 * MB;
constexpr unsigned OOB = 0x40000000u;
constexpr unsigned R0 = 0, R1 = 32 * KB, R2 = 64 * KB, R3 = 96 * KB, R4V = 128 * KB, R4S = 144 * KB, LDS_BYTES = 160 * KB;
constexpr float NEG_BIG = -1.0e30f;
constexpr float LOG2E = 1.4426950408889634f;
constexpr float SAFE2 = 2500.0f;
constexpr unsigned SPIN_CAP = 1u << 18;
#define D1V 10
#define D2V 8
#define D3V 8
#define D4V 10
#define OUTAUX 0
#define TLBN 0
#define TLBW 1
#define TLBSTEP 0
#define PRHO 3
#define DSLEEP 24
#define WOEARLY 1
#define PPOLL 0
#define CODEPF 1
constexpr int D1 = D1V, D2 = D2V, D3 = D3V, D4 = D4V;

template <int B, int E, class F> DI void sfor(F&& f) {
  if constexpr (B < E) { f(std::integral_constant<int, B>{}); sfor<B + 1, E>(f); }
}
DI bf16x8 as_bf8(v4u v) { return __builtin_bit_cast(bf16x8, v); }
DI bf16x2 as_bf2(unsigned v) { return __builtin_bit_cast(bf16x2, v); }
DI s16x4 as_s4(v2u v) { return __builtin_bit_cast(s16x4, v); }
DI f32x16 mfma16(v4u a, v4u b, f32x16 c) { return __builtin_amdgcn_mfma_f32_32x32x16_bf16(as_bf8(a), as_bf8(b), c, 0, 0, 0); }
DI f32x16 mfma8(v2u a, v2u b, f32x16 c) { return __builtin_amdgcn_mfma_f32_32x32x8bf16_1k(as_s4(a), as_s4(b), c, 0, 0, 0); }
DI f32x4 mfma16s(v4u a, v4u b, f32x4 c) { return __builtin_amdgcn_mfma_f32_16x16x32_bf16(as_bf8(a), as_bf8(b), c, 0, 0, 0); }
DI unsigned cvtpk(float lo, float hi) { f32x2 f = {lo, hi}; bf16x2 p = __builtin_convertvector(f, bf16x2); return __builtin_bit_cast(unsigned, p); }
DI float bflo(unsigned v) { return __uint_as_float(v << 16); }
DI float bfhi(unsigned v) { return __uint_as_float(v & 0xffff0000u); }
DI float dot2(unsigned a, unsigned b, float c) { return __builtin_amdgcn_fdot2_f32_bf16(as_bf2(a), as_bf2(b), c, false); }
DI float ex2(float v) { return __builtin_amdgcn_exp2f(v); }
DI float xhalf_add(float v) { auto r = __builtin_amdgcn_permlane32_swap(__float_as_uint(v), __float_as_uint(v), false, false); return __uint_as_float(r[0]) + __uint_as_float(r[1]); }
DI float xhalf_max(float v) { auto r = __builtin_amdgcn_permlane32_swap(__float_as_uint(v), __float_as_uint(v), false, false); return fmaxf(__uint_as_float(r[0]), __uint_as_float(r[1])); }
DI unsigned umax(unsigned a, unsigned b) { return a > b ? a : b; }
DI unsigned row_umax(unsigned v) {
  v = umax(v, (unsigned)__builtin_amdgcn_update_dpp(0, (int)v, 0x121, 0xf, 0xf, true));
  v = umax(v, (unsigned)__builtin_amdgcn_update_dpp(0, (int)v, 0x122, 0xf, 0xf, true));
  v = umax(v, (unsigned)__builtin_amdgcn_update_dpp(0, (int)v, 0x124, 0xf, 0xf, true));
  v = umax(v, (unsigned)__builtin_amdgcn_update_dpp(0, (int)v, 0x128, 0xf, 0xf, true));
  return v;
}
DI v4u ld16(rsrc_t rs, unsigned voff, unsigned soff) { return __builtin_amdgcn_raw_buffer_load_b128(rs, voff, soff, 16); }
DI v4u ld16p(rsrc_t rs, unsigned voff, unsigned soff) { return __builtin_amdgcn_raw_buffer_load_b128(rs, voff, soff, 0); }
DI v2u ld8p(rsrc_t rs, unsigned voff, unsigned soff) { return __builtin_bit_cast(v2u, __builtin_amdgcn_raw_buffer_load_b64(rs, voff, soff, 0)); }
DI v2u ld8(rsrc_t rs, unsigned voff, unsigned soff) { return __builtin_bit_cast(v2u, __builtin_amdgcn_raw_buffer_load_b64(rs, voff, soff, 16)); }
DI void st16(v4u v, rsrc_t rs, unsigned voff, unsigned soff) {
  __builtin_amdgcn_raw_buffer_store_b128(v, rs, voff, soff, 16);
  asm volatile("s_nop 1" :: "v"(v) : "memory");
}
DI void st8(v2u v, rsrc_t rs, unsigned voff, unsigned soff) { __builtin_amdgcn_raw_buffer_store_b64(v, rs, voff, soff, 16); }
DI v4u lds16(LAS unsigned char* L, unsigned off) { return *(LAS v4u*)(L + off); }
#define SB() __builtin_amdgcn_sched_barrier(0)
#define OPQV(x) asm volatile("" : "+v"(x))
#define OPQS(x) asm volatile("" : "+s"(x))
#define LANE_CONSTS() int lane = threadIdx.x & 63; OPQV(lane); const int r = lane & 31, h = lane >> 5; const unsigned lane16 = lane * 16; (void)r; (void)h
#define BAR() asm volatile("s_waitcnt lgkmcnt(0)\n\ts_barrier" ::: "memory")
#define VMWAIT(n) asm volatile("s_waitcnt vmcnt(" #n ")" ::: "memory")
#define DMA_HEAD "s_nop 4\n\ts_mov_b32 m0, %0\n\ts_nop 0\n\t"
#define DMA_LD(o) "buffer_load_dwordx4 %1, %2, %3 offen offset:" #o " sc1 lds\n\t"
#define DMA_ARGS :: "s"(lds), "v"(voff), "s"(rs), "s"(soff) : "memory"
DI void dma4(rsrc_t rs, unsigned lds, unsigned soff, unsigned voff) { asm volatile(DMA_HEAD DMA_LD(0) DMA_LD(1024) DMA_LD(2048) DMA_LD(3072) DMA_ARGS); }
DI void dma2(rsrc_t rs, unsigned lds, unsigned soff, unsigned voff) { asm volatile(DMA_HEAD DMA_LD(0) DMA_LD(1024) DMA_ARGS); }
DI void dma1p(rsrc_t rs, unsigned lds, unsigned soff, unsigned voff) { asm volatile(DMA_HEAD "buffer_load_dwordx4 %1, %2, %3 offen lds" DMA_ARGS); }
#define PACE 5
#define STR2(x) #x
#define STR(x) STR2(x)
#define DMA_W "s_waitcnt vmcnt(" STR(PACE) ")\n\t"
DI void dma4_paced(rsrc_t rs, unsigned lds, unsigned soff, unsigned voff) {
  asm volatile(DMA_HEAD DMA_LD(0) DMA_W DMA_LD(1024) DMA_W DMA_LD(2048) DMA_W DMA_LD(3072) DMA_W DMA_ARGS);
}
DI void dma_tile(rsrc_t rs, unsigned lds, unsigned soff, unsigned lw, unsigned lane16) {
  dma4(rs, lds + lw * 8192u, soff + lw * 8192u, lane16);
  dma4(rs, lds + lw * 8192u + 4096u, soff + lw * 8192u + 4096u, lane16);
}
extern "C" __device__ long long wsatomic_add64(long long, rsrc_t, int, int, int) __asm("llvm.amdgcn.raw.ptr.buffer.atomic.add.i64");
struct Tag { unsigned lo, hi; };
DI v2u poll_ld(rsrc_t rs, unsigned voff, unsigned soff) {
  long long z = 0; OPQV(z);
  return __builtin_bit_cast(v2u, wsatomic_add64(z, rs, voff, soff, 0));
}
DI bool word_ok(v2u f, Tag t, unsigned target) { const unsigned a = f.x, b = f.y; return b == t.hi && (a - t.lo - target) <= 0xffffu - target; }
DI bool flags_ok(v2u f, Tag t, unsigned target, int lane, int n) { return __all(word_ok(f, t, target) || lane >= n); }
DI v2u wait_words(rsrc_t rs, unsigned vo, unsigned soff, Tag t, unsigned target, bool act) {
  v2u f = {0u, 0u};
#pragma nounroll
  for (unsigned spins = 0; spins < SPIN_CAP; ++spins) {
    f = poll_ld(rs, vo, soff);
    if (__all(word_ok(f, t, target) || !act)) break;
  }
  asm volatile("" ::: "memory");
  return f;
}
DI v2u wait_flags(rsrc_t rs, unsigned soff, Tag t, unsigned target, int lane, int n) {
  return wait_words(rs, lane < n ? lane * 8 : OOB, soff, t, target, lane < n);
}
DI void relay_set(LAS unsigned char* L, Tag t, unsigned id) { *(volatile LAS v2u*)(L + R4S + 1536) = v2u{t.lo | id, t.hi}; }
DI bool relay_get(LAS unsigned char* L, Tag t, unsigned id) {
  const v2u v = *(volatile LAS v2u*)(L + R4S + 1536);
  const unsigned a = v.x, b = v.y;
  return __builtin_amdgcn_readfirstlane(a) == (t.lo | id) && __builtin_amdgcn_readfirstlane(b) == t.hi;
}
DI void relay_wait(LAS unsigned char* L, Tag t, unsigned id) {
#pragma nounroll
  for (unsigned spins = 0; spins < 64 * SPIN_CAP && !relay_get(L, t, id); ++spins) __builtin_amdgcn_s_sleep(1);
  asm volatile("" ::: "memory");
}
DI void swap64(rsrc_t rs, unsigned voff, unsigned soff, v2u v) {
  asm volatile("s_nop 4\n\tbuffer_atomic_swap_x2 %0, %1, %2, %3 offen\n\ts_nop 1" :: "v"(v), "v"(voff), "s"(rs), "s"(soff) : "memory");
}
DI void set_flag(rsrc_t rs, unsigned off, Tag t, unsigned payload, int lane, unsigned nrep = 1, unsigned rstep = 0) {
  swap64(rs, (unsigned)lane < nrep ? lane * rstep * 128u : OOB, off, v2u{t.lo | payload, t.hi});
}

struct Stats { float rstd, mean; v2u aug; };
DI Stats finish_stats(float s1, float s2, int h) {
  const float mean = s1 * (1.0f / DM);
  const float var = fmaxf(s2 * (1.0f / DM) - mean * mean, 0.0f);
  Stats st; st.rstd = __builtin_amdgcn_rsqf(var + 1e-5f); st.mean = mean;
  const float nm = h ? 0.0f : -mean;
  const unsigned hi = cvtpk(nm, nm) & 0xffffu;
  const float lo = nm - __uint_as_float(hi << 16);
  const unsigned w = hi | (cvtpk(lo, lo) << 16);
  st.aug.x = w; st.aug.y = w;
  return st;
}
DI Stats make_stats(const v2u (&sp)[8], int h) {
  float s1 = 0.f, s2 = 0.f;
#pragma unroll
  for (int i = 0; i < 8; ++i) { const unsigned a = sp[i].x, b = sp[i].y; s1 += __uint_as_float(a); s2 += __uint_as_float(b); }
  return finish_stats(xhalf_add(s1), xhalf_add(s2), h);
}
DI float xrow_add(float v) { auto r = __builtin_amdgcn_permlane16_swap(__float_as_uint(v), __float_as_uint(v), false, false); return __uint_as_float(r[0]) + __uint_as_float(r[1]); }
DI float wave_add(float v) {
  v += __int_as_float(__builtin_amdgcn_update_dpp(0, __float_as_int(v), 0x121, 0xf, 0xf, true));
  v += __int_as_float(__builtin_amdgcn_update_dpp(0, __float_as_int(v), 0x122, 0xf, 0xf, true));
  v += __int_as_float(__builtin_amdgcn_update_dpp(0, __float_as_int(v), 0x124, 0xf, 0xf, true));
  v += __int_as_float(__builtin_amdgcn_update_dpp(0, __float_as_int(v), 0x128, 0xf, 0xf, true));
  return xhalf_add(xrow_add(v));
}
DI v2u trd(LAS unsigned char* L, unsigned off) { return __builtin_bit_cast(v2u, __builtin_amdgcn_ds_read_tr16_b64_v4i16((LAS s16x4*)(L + off))); }
DI v4u trfrag(LAS unsigned char* L, unsigned off, unsigned second) { const v2u a = trd(L, off), b = trd(L, off + second); return v4u{a.x, a.y, b.x, b.y}; }
DI float sum8(v4u f, float c) {
  const unsigned a = f.x, b = f.y, d = f.z, e = f.w;
  return dot2(a, 0x3f803f80u, dot2(b, 0x3f803f80u, dot2(d, 0x3f803f80u, dot2(e, 0x3f803f80u, c))));
}
DI v2u split_aug(float cs) {
  const unsigned hi = cvtpk(cs, cs);
  const float lo = cs - __uint_as_float(hi << 16);
  return v2u{hi, cvtpk(lo, lo)};
}
DI float sum4(v4u p) {
  const unsigned a = p.x, b = p.y, c = p.z, d = p.w;
  return (__uint_as_float(a) + __uint_as_float(b)) + (__uint_as_float(c) + __uint_as_float(d));
}
DI void swap16(v4u& a, v4u& b) {
#pragma unroll
  for (int i = 0; i < 4; ++i) { const unsigned x = a[i], y = b[i]; const auto t = __builtin_amdgcn_permlane16_swap(x, y, false, false); a[i] = t[0]; b[i] = t[1]; }
}
template <int I0> DI void swap16h(v4u& a, v4u& b) {
#pragma unroll
  for (int i = I0; i < I0 + 2; ++i) { const unsigned x = a[i], y = b[i]; const auto t = __builtin_amdgcn_permlane16_swap(x, y, false, false); a[i] = t[0]; b[i] = t[1]; }
  OPQV(a); OPQV(b);
}
DI v2u make_aug(v4u p) { return split_aug(sum4(p)); }

DI float max16(const f32x16& s, float m) {
  m = fmaxf(fmaxf(m, s[0]), s[1]);   m = fmaxf(fmaxf(m, s[2]), s[3]);
  float n = fmaxf(s[4], s[5]);       n = fmaxf(fmaxf(n, s[6]), s[7]);
  m = fmaxf(fmaxf(m, s[8]), s[9]);   n = fmaxf(fmaxf(n, s[10]), s[11]);
  m = fmaxf(fmaxf(m, s[12]), s[13]); n = fmaxf(fmaxf(n, s[14]), s[15]);
  return fmaxf(m, n);
}
DI void exp_pv(const f32x16& s, v4u v0, v4u v1, f32x16& o) {
  v4u p0, p1;
  p0.x = cvtpk(ex2(s[0]), ex2(s[1])); p0.y = cvtpk(ex2(s[2]), ex2(s[3])); p0.z = cvtpk(ex2(s[4]), ex2(s[5])); p0.w = cvtpk(ex2(s[6]), ex2(s[7]));
  o = mfma16(v0, p0, o);
  p1.x = cvtpk(ex2(s[8]), ex2(s[9])); p1.y = cvtpk(ex2(s[10]), ex2(s[11])); p1.z = cvtpk(ex2(s[12]), ex2(s[13])); p1.w = cvtpk(ex2(s[14]), ex2(s[15]));
  o = mfma16(v1, p1, o);
}
DI void attn_general(LAS unsigned char* L, v4u q, const f32x16& cm, int bm, int bf0, int nf, unsigned kaddr, unsigned vbase, unsigned vstep,
                     float& m, f32x16& o) {
  const f32x16 zero = {};
  float mx = max16(mfma16(lds16(L, kaddr + bm * 2048), q, cm), m);
#pragma nounroll
  for (int b = 0; b < nf; ++b) mx = max16(mfma16(lds16(L, kaddr + (bf0 + b) * 2048), q, zero), mx);
  mx = xhalf_max(mx);
  const float f = ex2(m - mx);
#pragma unroll
  for (int i = 0; i < 16; ++i) o[i] *= f;
  m = mx;
  f32x16 c;
#pragma unroll
  for (int i = 0; i < 16; ++i) c[i] = cm[i] - mx;
  unsigned va = vbase + vstep * bm;
  exp_pv(mfma16(lds16(L, kaddr + bm * 2048), q, c), lds16(L, va), lds16(L, va + 512), o);
#pragma unroll
  for (int i = 0; i < 16; ++i) c[i] = -mx;
#pragma nounroll
  for (int b = 0; b < nf; ++b) {
    va = vbase + vstep * (bf0 + b);
    exp_pv(mfma16(lds16(L, kaddr + (bf0 + b) * 2048), q, c), lds16(L, va), lds16(L, va + 512), o);
  }
}
template <int K, int S0, int DIR, class H>
DI void attn_chain(LAS unsigned char* L, v4u q, int n, unsigned kaddr, unsigned va, unsigned vstep, const f32x16& s, v4u v0, v4u v1, v4u kn,
                   f32x16& o, H&& hook) {
  if constexpr (K == 7) { hook(IC(K)); exp_pv(s, v0, v1, o); }
  else {
    const f32x16 zero = {};
    f32x16 sn = mfma16(kn, q, zero);
    v4u w0 = lds16(L, va), w1 = lds16(L, va + 512);
    if constexpr (K < 6) kn = lds16(L, kaddr + (S0 + DIR * (K + 1)) * 2048);
    hook(IC(K));
    SB();
    exp_pv(s, v0, v1, o);
    asm volatile("" : "+v"(sn), "+v"(w0), "+v"(w1), "+v"(kn), "+v"(o));
    if (n > K) attn_chain<K + 1, S0, DIR>(L, q, n, kaddr, DIR > 0 ? va + vstep : va - vstep, vstep, sn, w0, w1, kn, o, hook);
  }
}
template <int S0, int DIR, class H>
DI void attn_bounded(LAS unsigned char* L, v4u q, const f32x16& cm, int bm, int n, unsigned kaddr, unsigned vbase, unsigned vstep, f32x16& o, H&& hook) {
  const unsigned vm = vbase + vstep * bm;
  const v4u km = lds16(L, kaddr + bm * 2048), kn = lds16(L, kaddr + S0 * 2048), v0 = lds16(L, vm), v1 = lds16(L, vm + 512);
  attn_chain<0, S0, DIR>(L, q, n, kaddr, vbase + vstep * S0, vstep, mfma16(km, q, cm), v0, v1, kn, o, hook);
}
template <int S0, class H>
DI void attn_full(LAS unsigned char* L, v4u q, int n, unsigned kaddr, unsigned vbase, unsigned vstep, f32x16& o, H&& hook) {
  const f32x16 zero = {};
  const unsigned va = vbase + vstep * S0;
  const v4u k0 = lds16(L, kaddr + S0 * 2048), kn = lds16(L, kaddr + (S0 + 1) * 2048), v0 = lds16(L, va), v1 = lds16(L, va + 512);
  attn_chain<1, S0, 1>(L, q, n, kaddr, va + vstep, vstep, mfma16(k0, q, zero), v0, v1, kn, o, hook);
}

DI f32x16 unpack_tile(v4u r0, v4u r1) {
  f32x16 k;
  k[0] = bflo(r0.x); k[1] = bfhi(r0.x); k[2] = bflo(r0.y); k[3] = bfhi(r0.y); k[4] = bflo(r0.z); k[5] = bfhi(r0.z); k[6] = bflo(r0.w); k[7] = bfhi(r0.w);
  k[8] = bflo(r1.x); k[9] = bfhi(r1.x); k[10] = bflo(r1.y); k[11] = bfhi(r1.y); k[12] = bflo(r1.z); k[13] = bfhi(r1.z); k[14] = bflo(r1.w); k[15] = bfhi(r1.w);
  return k;
}
DI void pack_tile(const f32x16& k, v4u& y0, v4u& y1) {
  y0.x = cvtpk(k[0], k[1]); y0.y = cvtpk(k[2], k[3]); y0.z = cvtpk(k[4], k[5]); y0.w = cvtpk(k[6], k[7]);
  y1.x = cvtpk(k[8], k[9]); y1.y = cvtpk(k[10], k[11]); y1.z = cvtpk(k[12], k[13]); y1.w = cvtpk(k[14], k[15]);
}
DI v2u tile_stats(const f32x16& k) {
  f32x2 a = {k[0], k[1]}, b = a * a;
#pragma unroll
  for (int i = 1; i < 8; ++i) { const f32x2 t = {k[2 * i], k[2 * i + 1]}; a += t; b += t * t; }
  return v2u{__float_as_uint(xhalf_add(a[0] + a[1])), __float_as_uint(xhalf_add(b[0] + b[1]))};
}

namespace sched {
constexpr int T4 = 128, T5 = 160, T6 = 192, P4 = 224, RES = T6 + 16;
constexpr int store_at(int p) { return p < 4 ? 96 + 32 * (p / 2) + ((p & 1) ? 26 : 14) : (p < 6 ? 96 + 32 * (p - 2) + 25 : P4 + 13); }
constexpr int flag(int g) { return g == T4 + 12 ? 2 : g == T5 + 14 ? 4 : g == T6 + 14 ? 5 : g == P4 + 10 ? 6 : g == P4 + 34 ? 7 : 0; }
constexpr int hload(int g) { return g >= T6 + 17 && g < T6 + 17 + D4 ? g - T6 - 17 : (g > P4 && (g & 1) && (g - P4) / 2 + D4 < 56 ? (g - P4) / 2 + D4 : -1); }
constexpr int need(int c) { return P4 + 2 * (8 * c - D4) - 1; }
constexpr int check(int g) { return g == T6 + 15 ? 2 : g == need(2) ? 4 : g == need(4) ? 5 : g == need(5) ? 6 : g == need(6) ? 7 : 0; }
constexpr bool poll_a(int g) { return g == T5 + 2 || g == need(4) - 25 || g == need(6) - 23; }
constexpr bool poll_b(int g) { return g == T6 + 10 || g == need(5) - 25; }
constexpr int pre(int g) { return (poll_a(g) ? 1 : 0) + (poll_b(g) ? 1 : 0); }
constexpr int post(int g) { return (g == RES ? 2 : 0) + (hload(g) >= 0 ? 1 : 0); }
constexpr bool is_store(int g) { for (int p = 0; p < 7; ++p) if (store_at(p) == g) return true; return false; }
constexpr int younger(int gs, int gw) {
  int n = post(gs) + pre(gw);
  for (int g = gs + 1; g < gw; ++g) n += pre(g) + (flag(g) ? 1 : 0) + (is_store(g) ? 1 : 0) + post(g);
  return n;
}
static_assert(D4 <= 14 && need(2) > P4 && need(4) - 25 > T6 + 15 && need(5) - 25 > need(2) && need(6) - 23 > need(4), "poll registers are reused in order");
}
struct Args { unsigned char* ws; u16* out; const u16 *x, *wqkv, *wo, *wg, *wu, *wd; };
#define STAMP(id) do {} while (0)
#define STAMP_DECL() do {} while (0)
#define STAMP_OUT() do {} while (0)

typedef const __attribute__((address_space(4))) Args* kargs_t;
DI kargs_t kargs() { kargs_t p = (kargs_t)__builtin_amdgcn_kernarg_segment_ptr(); OPQS(p); return p; }
__global__ void __launch_bounds__(512, 2) main_k(Args a) {
  extern __shared__ __attribute__((aligned(16))) unsigned char smem[];
  LAS unsigned char* L = (LAS unsigned char*)smem;
  const unsigned lb = (unsigned)(size_t)L;
  const int w0 = __builtin_amdgcn_readfirstlane(threadIdx.x >> 6);
  STAMP_DECL();
  const int xcd = blockIdx.x & 7, idx = blockIdx.x >> 3;
  const int sg0 = (xcd & ~1) | (idx >> 4), i4 = idx & 15, j0 = ((i4 >> 2) << 3) | ((xcd & 1) << 2) | (i4 & 3);
  const rsrc_t rs = __builtin_amdgcn_make_buffer_rsrc(a.ws, 0, WS_BYTES, 0x00020000);
#define ROLES() int w = w0, sg = sg0, j = j0, layer = layer0; OPQS(w); OPQS(sg); OPQS(j); OPQS(layer); Tag tg = tg0; OPQS(tg.lo); OPQS(tg.hi); \
  const int head = j, tbl = w < 4 ? w : 11 - w, Tq = 8 * sg + tbl;              \
  const int grp = 2 * sg + (j >> 4), mem = j & 15, nblk = j & 7;                \
  const int cw = w & 3, th = cw >> 1, kh = cw & 1; \
  const int tb2 = 4 * grp + 2 * ((j >> 3) & 1) + th, tb3 = 4 * grp + cw;        \
  (void)head; (void)Tq; (void)mem; (void)nblk; (void)kh; (void)tb2; (void)tb3
#define PREP_ROLES() const int pun = sg * 32 + j, uty = pun < 112 ? 0 : (pun < 224 ? 1 : 2); \
  const int pcb = uty == 0 ? pun >> 2 : (uty == 1 ? (pun - 112) / 14 : (pun - 224) >> 2), pkq = uty == 1 ? (pun - 112) % 14 : pun & 3, pks = 8 * pkq + 2 * (w & 3)
  Tag tg0 = {0u, 0u};
  f32x16 cqk, cm;
  f32x4 cva, cvb, rra, rrb, mma, mmb;
  float rstd1, csv;
#define CAUSAL2(i) do { float a0 = (((i) & 3) + 8 * ((i) >> 2) + 4 * h <= r) ? 0.0f : NEG_BIG, a1 = ((((i) + 1) & 3) + 8 * ((i) >> 2) + 4 * h <= r) ? 0.0f : NEG_BIG; \
    OPQV(a0); OPQV(a1); cm[i] = a0; cm[(i) + 1] = a1; } while (0)
#define BOUNCE_WR(st) *(LAS float*)(L + R4S + 8192 + w * 256 + h * 128 + r * 4) = h ? (st).mean : (st).rstd
#define BOUNCE_RD() do { const unsigned ba = R4S + 8192 + w * 256 + (lane >> 4) * 16; \
    rra = *(LAS f32x4*)(L + ba); rrb = *(LAS f32x4*)(L + ba + 64); mma = *(LAS f32x4*)(L + ba + 128); mmb = *(LAS f32x4*)(L + ba + 192); } while (0)
#define VSTEP(s, X, D) do { \
    if constexpr ((s) >= 2 && (s) < 34) { constexpr int p_ = (((s) >> 1) - 1) * 2; swap16h<((s) & 1) * 2>(X[p_ % (D)], X[(p_ + 1) % (D)]); } \
    if constexpr ((s) >= 4) { constexpr int p_ = (((s) >> 1) - 2) * 2 + ((s) & 1); \
      if constexpr ((s) & 1) { cvb = mfma16s(X[p_ % (D)], wv[(p_ >> 1) & 3], cvb); OPQV(cvb); } else { cva = mfma16s(X[p_ % (D)], wv[(p_ >> 1) & 3], cva); OPQV(cva); } } } while (0)
#define ONES_WR() do { unsigned o2 = 0x3f803f80u; OPQV(o2); *(LAS v4u*)(L + R4S + (lane & 1) * 512) = v4u{o2, o2, o2, o2}; } while (0)
  {
    const int layer0 = 0;
    STAMP(31);
    ROLES(); LANE_CONSTS();
    const rsrc_t rx = __builtin_amdgcn_make_buffer_rsrc((void*)a.x, 0, SEQ * DM * 2, 0x00020000);
    const rsrc_t rw = __builtin_amdgcn_make_buffer_rsrc((void*)a.wqkv, 0, DM * 3072, 0x00020000);
    const int un = sg * 32 + j;
    const bool uq = w == 0, uv = w == 1 && un < 128;
    const int ucb = uq ? un >> 5 : un >> 4, uk = uq ? un & 31 : 2 * (un & 15);
    const bool uw = uq || uv;
    const unsigned uvo = (lane >> 3) * 3072 + (lane & 7) * 16, uso = 16 * uk * 3072 + ucb * 128 + (uq ? 0 : 2048);
    const unsigned ulds = R1 + w * 12288 + 8192, u2 = uq ? 1024 : 16 * 3072;
    if (uw) {
      dma1p(rw, lb + ulds, uso, uvo); dma1p(rw, lb + ulds + 1024, uso + 8 * 3072, uvo);
      dma1p(rw, lb + ulds + 2048, uso + u2, uvo); dma1p(rw, lb + ulds + 3072, uso + u2 + 8 * 3072, uvo);
    }
    if (w == 7) {
      unsigned long long pc; unsigned len;
      asm volatile("s_getpc_b64 %0\n.Lmk_here:\n\ts_mov_b32 %1, .Lmk_last-.Lmk_here" : "=s"(pc), "=s"(len));
      const rsrc_t rc = __builtin_amdgcn_make_buffer_rsrc((void*)(pc & ~127ull), 0, len & ~15u, 0x00020000);
      dma1p(rc, lb + R4S + 12288u, (blockIdx.x >> 3) * 2048u, lane16); dma1p(rc, lb + R4S + 13312u, (blockIdx.x >> 3) * 2048u + 1024u, lane16);
    }
    const unsigned token = 8 * un + w;
    v2u ep; v4u row;
    asm volatile("s_nop 4\n\tbuffer_load_dwordx2 %0, off, %2, 0 sc1\n\tbuffer_load_dwordx4 %1, %3, %4, %5 offen"
                 : "=&v"(ep), "=&v"(row) : "s"(rs), "v"(lane16), "s"(rx), "s"(token * 1024u) : "memory");
    const unsigned xv0 = (lane >> 3) * 1024 + (((lane & 7) ^ (lane >> 4)) * 16), xv1 = (xv0 ^ 64u) + 7168, xv2 = xv0 + 14336, xv3 = xv1 + 14336;
    const unsigned rot = (w + j) & 7;
    auto xround = [&](int rho) {
      asm volatile("s_nop 4\n\ts_mov_b32 m0, %0\n\ts_nop 0\n\tbuffer_load_dwordx4 %1, %5, %6 offen lds\n\tbuffer_load_dwordx4 %2, %5, %6 offen offset:1024 lds\n\t"
                   "buffer_load_dwordx4 %3, %5, %6 offen offset:2048 lds\n\tbuffer_load_dwordx4 %4, %5, %6 offen offset:3072 lds"
                   :: "s"(lb + R1 + w * 12288u + (rho % 3) * 4096u), "v"(xv0), "v"(xv1), "v"(xv2), "v"(xv3), "s"(rx), "s"(Tq * 32768u + ((rho + rot) & 7) * 128u) : "memory");
    };
    xround(0);
    asm volatile("s_waitcnt vmcnt(4)" : "+v"(ep), "+v"(row) :: "memory");
    const unsigned e0 = __builtin_amdgcn_readfirstlane(ep.x) & 0xffff0000u, e1 = __builtin_amdgcn_readfirstlane(ep.y);
    tg.lo = e0 + 0x10000u; tg.hi = e1 + (tg.lo == 0u ? 1u : 0u);
    tg0 = tg;
    if (uw) {
      const int g = lane >> 4, q = (lane & 15) >> 2, pp = lane & 3;
      const unsigned ta = ulds + pp * 8 + q * 128 + (uq ? (g & 1) * 2048 + (g >> 1) * 512 : (g >> 1) * 2048 + (g & 1) * 512);
      const unsigned dst = uq ? OFF_WQK + (4 * ucb * 32 + uk) * 1024u : OFF_WV + 4 * ucb * 16384u + uk * 512u, dstep = uq ? 32768u : 16384u;
#pragma unroll
      for (int tl = 0; tl < 4; ++tl) {
        const v4u f = trfrag(L, ta + tl * 32, 1024);
        st16(f, rs, lane16, dst + tl * dstep);
        const float p = xhalf_add(sum8(f, 0.f)), pv = xrow_add(p);
        __builtin_amdgcn_raw_buffer_store_b32(__float_as_uint(uq ? p : pv), rs, lane < (uq ? 32 : 16) ? lane * 4 : OOB,
                                              uq ? OFF_CSP + ((4 * ucb + tl) * 32 + uk) * 128u : OFF_CSP + 131072u + ((4 * ucb + tl) * 16 + (uk >> 1)) * 64u, 16);
      }
    }
    const unsigned t32 = (tg.lo >> 16) | (tg.hi << 16);
    {
      const unsigned a0 = row.x, a1 = row.y, a2 = row.z, a3 = row.w;
      const float s1 = wave_add(sum8(row, 0.f)), s2 = wave_add(dot2(a0, a0, dot2(a1, a1, dot2(a2, a2, dot2(a3, a3, 0.f)))));
      st8(v2u{__float_as_uint(lane ? s2 : s1), t32}, rs, lane < 2 ? lane * 8 : OOB, OFF_ST0 + token * 16u);
    }
    xround(1); xround(2);
    unsigned xa[4];
#pragma unroll
    for (int i = 0; i < 4; ++i) { xa[i] = R1 + w * 12288 + (r >> 3) * 1024 + (r & 7) * 128 + (((2 * i) ^ ((r >> 1) & 7)) << 4) + h * 8; OPQV(xa[i]); }
    v4u x0[32];
    const unsigned wfs = (FL_Q + (sg % RQ) * 24 + (head >> 2) * 3) * 128u;
    auto wdma = [&] {
      dma4(rs, lb + R0 + w * 4096u, OFF_WQK + head * 32768u + w * 4096u, lane16);
      dma2(rs, lb + R4V + w * 2048u, OFF_WV + head * 16384u + w * 2048u, lane16);
    };
    bool early = false;
    SB();
    sfor<0, 8>([&](auto RHO) {
      constexpr int rho = RHO;
      if constexpr (rho == 0) {
        if (uw) { VMWAIT(8); set_flag(rs, (FL_Q + ucb * 3) * 128u + (uq ? uk : 32 + (uk >> 1)) * 8u, tg, 1u, lane, RQ, 24); }
        else VMWAIT(9);
      } else if constexpr (rho < 6) VMWAIT(8);
      else if constexpr (rho == 6) { if (early) VMWAIT(10); else VMWAIT(4); }
      else { if (early) VMWAIT(6); else VMWAIT(0); }
      v2u t[8];
#pragma unroll
      for (int i = 0; i < 4; ++i) { t[2 * i] = *(LAS v2u*)(L + xa[i] + (rho % 3) * 4096); t[2 * i + 1] = *(LAS v2u*)(L + (xa[i] ^ 16u) + (rho % 3) * 4096); }
      asm volatile("s_waitcnt lgkmcnt(0)" : "+v"(t[0]), "+v"(t[1]), "+v"(t[2]), "+v"(t[3]), "+v"(t[4]), "+v"(t[5]), "+v"(t[6]), "+v"(t[7]) :: "memory");
      if constexpr (rho + 3 < 8) xround(rho + 3);
      if constexpr (rho == PRHO) { if (w == 2) { wait_flags(rs, wfs, tg, 1u, lane, 48); relay_set(L, tg, 1u); } }
      if constexpr (rho == 5) { early = relay_get(L, tg, 1u); if (early) wdma(); }
#pragma unroll
      for (int i = 0; i < 4; ++i) { const unsigned a0 = t[2 * i].x, a1 = t[2 * i].y, a2 = t[2 * i + 1].x, a3 = t[2 * i + 1].y; x0[4 * rho + i] = v4u{a0, a1, a2, a3}; }
      if constexpr (rho >= 1 && rho < 5) { CAUSAL2(4 * (rho - 1)); CAUSAL2(4 * (rho - 1) + 2); }
      SB();
    });
    if (!early) { relay_wait(L, tg, 1u); wdma(); }
    STAMP(0);
    v2u sgr = ld8(rs, r * 16 + h * 8, OFF_ST0 + Tq * 512u);
    {
      const unsigned ca = __builtin_amdgcn_raw_buffer_load_b32(rs, h * 256 + r * 4, OFF_CSP + (head * 32 + 4 * w) * 128u, 16);
      const unsigned cb = __builtin_amdgcn_raw_buffer_load_b32(rs, h * 256 + r * 4 + 128, OFF_CSP + (head * 32 + 4 * w) * 128u, 16);
      const unsigned cc = __builtin_amdgcn_raw_buffer_load_b32(rs, lane < 32 ? lane * 4 : OOB, OFF_CSP + 131072u + (head * 16 + 2 * w) * 64u, 16);
      *(LAS f32x2*)(L + R4S + 2048 + w * 512 + lane * 8) = f32x2{__uint_as_float(ca) + __uint_as_float(cb), __uint_as_float(cc)};
    }
    VMWAIT(0);
    if (!__all(sgr.y == t32)) {
#pragma nounroll
      for (unsigned spins = 0; spins < SPIN_CAP; ++spins) {
        asm volatile("s_sleep 4" ::: "memory");
        sgr = ld8(rs, r * 16 + h * 8, OFF_ST0 + Tq * 512u);
        if (__all(sgr.y == t32)) break;
      }
    }
    const unsigned sv = sgr.x;
    const auto svs = __builtin_amdgcn_permlane32_swap(sv, sv, false, false);
    const Stats st = finish_stats(__uint_as_float(svs[0]), __uint_as_float(svs[1]), h);
    rstd1 = st.rstd;
    BOUNCE_WR(st);
    ONES_WR();
    BOUNCE_RD();
    *(LAS unsigned*)(L + R4S + 1552) = 0u;
    BAR();
    STAMP(2);
    if (w < 4) {
      if (w == 3) set_flag(rs, FL_SEEN * 128u + un * 8u, tg, 1u, lane);
      PREP_ROLES();
      const kargs_t ka = kargs();
      const unsigned pld = uty == 0 ? FF * 2 : 1024;
      const rsrc_t ra = __builtin_amdgcn_make_buffer_rsrc((void*)(uty == 0 ? ka->wg : (uty == 1 ? ka->wd : ka->wo)), 0, uty == 2 ? DM * 1024 : FF * 1024, 0x00020000);
      const rsrc_t rb = __builtin_amdgcn_make_buffer_rsrc((void*)ka->wu, 0, FF * 1024, 0x00020000);
      const unsigned pvo = (lane >> 3) * pld + (lane & 7) * 16, pso = 16 * pks * pld + pcb * 128;
#pragma unroll
      for (int i = 0; i < 4; ++i) dma1p(ra, lb + R1 + (w + 4) * 12288u + i * 1024u, pso + 8 * i * pld, pvo);
      if (uty == 0) {
#pragma unroll
        for (int i = 0; i < 4; ++i) dma1p(rb, lb + R1 + (w + 4) * 12288u + 4096u + i * 1024u, pso + 8 * i * pld, pvo);
      }
    }
    unsigned vwa = R4V + ((lane >> 4) & 1) * 512 + (h * 16 + (lane & 15)) * 16; OPQV(vwa);
    f32x2 cs = {0.f, 0.f}, cpart[4];
    v4u wq[4], wv[4];
    auto qaddr = [&](int s) { return R0 + lane16 + (((s >> 2) + rot) & 7) * 4096 + (s & 3) * 1024; };
    auto vaddr = [&](int s) { return vwa + (((s >> 2) + rot) & 7) * 2048 + (s & 2) * 512; };
    wq[0] = lds16(L, qaddr(0)); wq[1] = lds16(L, qaddr(1)); wq[2] = lds16(L, qaddr(2)); wv[0] = lds16(L, vaddr(0));
    cqk = f32x16{}; cva = f32x4{}; cvb = f32x4{};
    v2u aqk;
    SB();
    sfor<0, 36>([&](auto I) {
      constexpr int s = I;
      if constexpr (s < 32) { cqk = mfma16(wq[s & 3], x0[s], cqk); OPQV(cqk); }
      if constexpr (s + 3 < 32) wq[(s + 3) & 3] = lds16(L, qaddr(s + 3));
      if constexpr (!(s & 1) && s + 2 < 32) wv[((s >> 1) + 1) & 3] = lds16(L, vaddr(s + 2));
      VSTEP(s, x0, 32);
      if constexpr (s == 2 || s == 6) {
#pragma unroll
        for (int i = 0; i < 4; ++i) cpart[i] = *(LAS f32x2*)(L + R4S + 2048 + (i + (s == 6 ? 4 : 0)) * 512 + lane * 8);
      }
      if constexpr (s == 5 || s == 9) { cs += (cpart[0] + cpart[1]) + (cpart[2] + cpart[3]); OPQV(cs); }
      if constexpr (s == 11) { cs[0] = xhalf_add(cs[0]); OPQV(cs); }
      if constexpr (s == 13) { aqk = split_aug(cs[0]); OPQV(aqk); }
      if constexpr (s == 15) { cs[1] = xrow_add(cs[1]); OPQV(cs); }
      if constexpr (s == 17) { csv = xhalf_add(cs[1]); OPQV(csv); }
      if constexpr (s == 19) { if (w == 4) st16(v4u{aqk.x, aqk.y, __float_as_uint(csv), 0u}, rs, lane16, OFF_CSQ + un * 1024u); }
      if constexpr (s == 24) { cqk = mfma8(aqk, st.aug, cqk); OPQV(cqk); }
      SB();
    });
  }

  for (int layer0 = 0;; ++layer0) {
    {
      ROLES(); LANE_CONSTS();
      const unsigned own = R1 + (8 + tbl) * 2048;
      STAMP(3);
      const bool up = (lane & 16) != 0;
      const unsigned kvb = OFF_KV + layer * (4 * MB) + (head * 64 + Tq) * 2048u;
      const unsigned kvo = sg < 7 ? lane16 : OOB;
      v4u qf, kf, vf;
      float q2 = 0.f, k2 = 0.f;
#pragma unroll
      for (int i = 0; i < 8; ++i) cqk[8 + i] *= rstd1;
      kf.x = cvtpk(cqk[8], cqk[9]); kf.y = cvtpk(cqk[10], cqk[11]); kf.z = cvtpk(cqk[12], cqk[13]); kf.w = cvtpk(cqk[14], cqk[15]);
      st16(kf, rs, kvo, kvb);
#pragma unroll
      for (int i = 0; i < 4; ++i) { cva[i] = (cva[i] - mma[i] * csv) * rra[i]; cvb[i] = (cvb[i] - mmb[i] * csv) * rrb[i]; }
      {
        const auto t0 = __builtin_amdgcn_permlane32_swap(cvtpk(cva[0], cva[1]), cvtpk(cvb[0], cvb[1]), false, false);
        const auto t1 = __builtin_amdgcn_permlane32_swap(cvtpk(cva[2], cva[3]), cvtpk(cvb[2], cvb[3]), false, false);
        vf = v4u{t0[0], t1[0], t0[1], t1[1]};
      }
      st16(vf, rs, kvo + 1024, kvb);
      SB();
      *(LAS v4u*)(L + own + lane16) = kf;
      *(LAS v4u*)(L + own + 1024 + lane16) = vf;
      const unsigned vstep = up ? 0u : 2048u, vbase = up ? R4S : R1 + 1024 + (h * 16 + (lane & 15)) * 16;
      const v4u vd0 = lds16(L, vbase + vstep * (8 + tbl)), vd1 = lds16(L, vbase + vstep * (8 + tbl) + 512);
      const float qs = rstd1 * (0.25f * LOG2E);
#pragma unroll
      for (int i = 0; i < 8; ++i) { cqk[i] *= qs; q2 += cqk[i] * cqk[i]; k2 += cqk[8 + i] * cqk[8 + i]; }
      qf.x = cvtpk(cqk[0], cqk[1]); qf.y = cvtpk(cqk[2], cqk[3]); qf.z = cvtpk(cqk[4], cqk[5]); qf.w = cvtpk(cqk[6], cqk[7]);
      const f32x16 sd = mfma16(kf, qf, cm);
      q2 = xhalf_add(q2) * 1.02f;
      const unsigned kr = row_umax(__float_as_uint(xhalf_add(k2) * 1.02f));
      const unsigned km0 = (umax(__builtin_amdgcn_readlane(kr, 0), __builtin_amdgcn_readlane(kr, 16)) >> 16) + 1u;
      const unsigned kmax16 = km0 < 0x7fc0u ? km0 : 0x7fc0u;
      *(LAS unsigned*)(L + R4S + 1024 + (8 + tbl) * 4) = kmax16 << 16;
      const unsigned fkv = (FL_KV + (layer * 32 + head) * 8 + sg) * 128u;
      f32x16 o = {};
      bool ok = __all(q2 * __uint_as_float(kmax16 << 16) <= SAFE2);
      exp_pv(sd, vd0, vd1, o);
      if (w < 4) {
        VMWAIT(0);
        if (sg < 7) set_flag(rs, fkv + tbl * 8, tg, kmax16, lane);
        if (layer == 0) {
          PREP_ROLES();
          const int g = lane >> 4, q = (lane & 15) >> 2, pp = lane & 3;
          const unsigned ta = R1 + (w + 4) * 12288 + (4 * (g >> 1) + q) * 128 + pp * 8 + (g & 1) * (uty == 0 ? 4096 : 32), tstep = uty == 0 ? 32 : 64;
          const unsigned dstep = uty == 1 ? 1024u : 32768u;
          float cp[4] = {0.f, 0.f, 0.f, 0.f};
#pragma unroll
          for (int ki = 0; ki < 2; ++ki) {
            const int ks = pks + ki, mm = ks / 7, cc = ks % 7;
            const unsigned dst = uty == 0 ? OFF_WGU + (4 * pcb * 32 + ks) * 1024u
                               : (uty == 1 ? OFF_WD + (pcb * 224 + cc * 32 + (mm >> 3) * 16 + (mm & 7) * 2) * 1024u : OFF_WO + (pcb * 64 + ks) * 1024u);
#pragma unroll
            for (int tl = 0; tl < 4; ++tl) {
              if (tl < 2 || uty == 0) {
                const v4u f = trfrag(L, ta + ki * 2048 + tl * tstep, 1024);
                st16(f, rs, lane16, dst + tl * dstep);
                cp[tl] = sum8(f, cp[tl]);
              }
            }
          }
#pragma unroll
          for (int tl = 0; tl < 4; ++tl) *(LAS float*)(L + R4S + 6144 + ((w * 4 + tl) * 32 + r) * 4) = xhalf_add(cp[tl]);
        }
      }
      STAMP(4);
      BAR();
      STAMP(5);
      unsigned kaddr = R1 + lane16; OPQV(kaddr);
      ok = ok && __all(q2 * __uint_as_float(row_umax(*(LAS unsigned*)(L + R4S + 1024 + (8 + (lane & 7)) * 4))) <= SAFE2);
      auto late_kv = [&] { VMWAIT(0); if (sg < 7) set_flag(rs, fkv + tbl * 8, tg, kmax16, lane); };
      if (ok && tbl > 0) attn_full<8>(L, qf, tbl, kaddr, vbase, vstep, o, [&](auto K) { if (K == 2 && w >= 4) late_kv(); });
      if (!ok && w >= 4) late_kv();
      STAMP(6);
      if (w < 4) {
        if (layer == 0) {
          VMWAIT(0);
          unsigned old = 0;
          if (lane == 0) old = __hip_atomic_fetch_add((LAS unsigned*)(L + R4S + 1552), 1u, __ATOMIC_RELAXED, __HIP_MEMORY_SCOPE_WORKGROUP);
          if (__builtin_amdgcn_readfirstlane(old) == 3u) {
            PREP_ROLES(); (void)pks;
            if (uty == 0) {
              f32x2 cs = {0.f, 0.f};
#pragma unroll
              for (int i = 0; i < 4; ++i) { cs[0] += *(LAS float*)(L + R4S + 6144 + (i * 128 + lane) * 4); cs[1] += *(LAS float*)(L + R4S + 6144 + (i * 128 + 64 + lane) * 4); }
              __builtin_amdgcn_raw_buffer_store_b32(__float_as_uint(cs[0]), rs, lane16, OFF_AUG + pcb * 2048u + pkq * 4u, 16);
              __builtin_amdgcn_raw_buffer_store_b32(__float_as_uint(cs[1]), rs, lane16 + 1024, OFF_AUG + pcb * 2048u + pkq * 4u, 16);
              VMWAIT(0);
            }
            set_flag(rs, (FL_P + (uty == 0 ? 0 : (uty == 1 ? 28 : 36)) + pcb) * 128u + pkq * 8u, tg, 1u, lane);
          }
        }
        if (sg > 0) {
          const v2u f = wait_flags(rs, fkv - 128 + 16 * w, tg, 1u, lane, 2);
          dma4(rs, lb + R1 + w * 4096u, OFF_KV + layer * (4 * MB) + (head * 64 + 8 * (sg - 1) + 2 * w) * 2048u, lane16);
          if (lane < 2) *(LAS unsigned*)(L + R4S + 1024 + (2 * w + lane) * 4) = f.x << 16;
        }
        STAMP(7);
        if (layer == 0 && w == 0) {
          const unsigned c0 = (7 * mem) >> 2, nw = 4 * (((7 * mem + 6) >> 2) - c0 + 1) + 4;
          wait_words(rs, lane < 4 ? (36 + nblk) * 128 + lane * 8 : ((unsigned)lane < nw ? (c0 + ((lane - 4) >> 2)) * 128 + (lane & 3) * 8 : OOB), FL_P * 128u, tg, 1u, (unsigned)lane < nw);
        }
        VMWAIT(0);
      }
      STAMP(8);
      BAR();
      STAMP(9);
      if (w >= 4) {
        dma_tile(rs, lb + R2, OFF_WO + nblk * 65536u, w - 4, lane16);
        dma_tile(rs, lb + R3, OFF_WO + nblk * 65536u + 32768u, w - 4, lane16);
        dma_tile(rs, lb + R0, OFF_WGU + mem * 7 * 32768u, w - 4, lane16);
      }
      if (sg > 0) ok = ok && __all(q2 * __uint_as_float(row_umax(*(LAS unsigned*)(L + R4S + 1024 + (lane & 7) * 4))) <= SAFE2);
      if (ok) {
        if (sg > 0) {
#pragma unroll
          for (int i = 0; i < 16; ++i) cm[i] = NEG_BIG - cm[i];
          attn_bounded<7, -1>(L, qf, cm, tbl, 7 - tbl, kaddr, vbase, vstep, o, [&](auto) {});
        }
      } else {
        float m = NEG_BIG;
#pragma unroll
        for (int i = 0; i < 16; ++i) o[i] = 0.f;
        attn_general(L, qf, cm, 8 + tbl, 8, tbl, kaddr, vbase, vstep, m, o);
        if (sg > 0) {
#pragma unroll
          for (int i = 0; i < 16; ++i) cm[i] = NEG_BIG - cm[i];
          attn_general(L, qf, cm, tbl, tbl + 1, 7 - tbl, kaddr, vbase, vstep, m, o);
        }
      }
      STAMP(10);
      const float il = __builtin_amdgcn_rcpf(o[8]);
      v4u of;
      of.x = cvtpk(o[0] * il, o[1] * il); of.y = cvtpk(o[2] * il, o[3] * il); of.z = cvtpk(o[4] * il, o[5] * il); of.w = cvtpk(o[6] * il, o[7] * il);
      st16(of, rs, lane16, OFF_O + layer * (2 * MB) + (Tq * 32 + head) * 1024u);
      VMWAIT(0);
      set_flag(rs, (FL_O + (layer * 64 + Tq) * 2 + (head >> 4)) * 128u + (head & 15) * 8, tg, 1u, lane, RO, 256);
      STAMP(11);
    }
    if (w0 >= 4) {
      ROLES(); LANE_CONSTS();
      const unsigned lw = w - 4;
      const unsigned gu = OFF_WGU + mem * 7 * 32768u, wd = OFF_WD + nblk * 7 * 32768u;
      unsigned wlo = lw * 8192u + lane16, whi = wlo + R2; OPQV(wlo); OPQV(whi);
      v4u sa[4][8];
      auto stage = [&](auto B, unsigned src, auto PACED) {
        constexpr int b = B;
        sfor<0, 8>([&](auto I) {
          constexpr int i = I;
          sa[b][i] = ld16(rs, lane16 + (i & 3) * 1024, src + lw * 8192u + (i >> 2) * 4096u);
          if constexpr (PACED) asm volatile(DMA_W ::: "memory"); else SB();
        });
      };
      auto put = [&](auto B, unsigned base) { constexpr int b = B; sfor<0, 8>([&](auto I) { constexpr int i = I; *(LAS v4u*)(L + base + i * 1024) = sa[b][i]; }); };
      stage(IC(0), gu + 1 * 32768u, IC(1));
      stage(IC(1), gu + 2 * 32768u, IC(1));
      BAR();
      put(IC(0), wlo + R1);
      stage(IC(2), gu + 3 * 32768u, IC(1));
      STAMP(14); BAR();
      put(IC(1), whi); put(IC(2), whi + 32768u);
      {
        const v4u p0 = ld16(rs, r * 16 + lw * 1024, OFF_AUG + mem * 7 * 512u), p1 = ld16(rs, lw < 3 ? r * 16 + lw * 1024 + 512 : OOB, OFF_AUG + mem * 7 * 512u);
        stage(IC(0), gu + 4 * 32768u, IC(1));
        *(LAS v2u*)(L + R4V + lw * 512 + r * 8) = make_aug(p0);
        *(LAS v2u*)(L + R4V + lw * 512 + 256 + r * 8) = make_aug(p1);
      }
      STAMP(17); BAR();
      if (layer == 0) {
        if (lw == 0) { wait_flags(rs, (FL_P + 28 + nblk) * 128u, tg, 1u, lane, 14); relay_set(L, tg, 3u); }
        else relay_wait(L, tg, 3u);
      }
      __builtin_amdgcn_s_sleep(DSLEEP);
      stage(IC(1), wd + 3 * 32768u, IC(1));
      STAMP(18); BAR();
      put(IC(0), wlo);
      dma_tile(rs, lb + R1, gu + 5 * 32768u, lw, lane16);
      dma_tile(rs, lb + R2, gu + 6 * 32768u, lw, lane16);
      STAMP(20); BAR();
      dma_tile(rs, lb + R3, wd + 0 * 32768u, lw, lane16);
      stage(IC(2), wd + 4 * 32768u, IC(0));
      VMWAIT(24); BAR();
      dma_tile(rs, lb + R0, wd + 1 * 32768u, lw, lane16);
      stage(IC(3), wd + 5 * 32768u, IC(0));
      VMWAIT(32); STAMP(21); BAR();
      dma_tile(rs, lb + R1, wd + 2 * 32768u, lw, lane16);
      stage(IC(0), wd + 6 * 32768u, IC(0));
      VMWAIT(40); STAMP(22); BAR();
      put(IC(1), whi);
      VMWAIT(24); STAMP(25); BAR();
      put(IC(2), whi + 32768u);
      VMWAIT(8); BAR();
      put(IC(3), wlo);
      STAMP(26); BAR();
      put(IC(0), wlo + R1);
      BAR();
      STAMP(27); BAR();
      BAR();
      STAMP(28); BAR();
      if (layer == 0) {
        dma4_paced(rs, lb + R0 + lw * 8192u, OFF_WQK + head * 32768u + lw * 8192u, lane16);
        dma4_paced(rs, lb + R0 + lw * 8192u + 4096u, OFF_WQK + head * 32768u + lw * 8192u + 4096u, lane16);
        dma4(rs, lb + R4V + lw * 4096u, OFF_WV + head * 16384u + lw * 4096u, lane16);
      } else if ((sg | j) == 0 && w == 4) {
#pragma unroll
        for (int i = 0; i < 4; ++i) wait_flags(rs, FL_SEEN * 128u + i * 512u, tg, 1u, lane, 64);
        set_flag(rs, FL_EPOCH * 128u, tg, 0u, lane);
      }
    } else {
      {
        ROLES(); LANE_CONSTS();
        const unsigned xfo = (tb2 * 32 + 4 * nblk + 2 * kh) * 1024u;
        v4u r0, r1;
        if (layer) { r0 = ld16(rs, lane16, OFF_X2 + xfo); r1 = ld16(rs, lane16 + 1024, OFF_X2 + xfo); }
        else {
          const rsrc_t rx = __builtin_amdgcn_make_buffer_rsrc((void*)kargs()->x, 0, SEQ * DM * 2, 0x00020000);
          const unsigned vo = r * 1024 + h * 8, so = tb2 * 32768u + nblk * 128u + kh * 64u;
          const v2u t0 = ld8p(rx, vo, so), t1 = ld8p(rx, vo + 16, so), t2 = ld8p(rx, vo + 32, so), t3 = ld8p(rx, vo + 48, so);
          r0 = v4u{t0.x, t0.y, t1.x, t1.y}; r1 = v4u{t2.x, t2.y, t3.x, t3.y};
        }
        unsigned wa0 = R2 + kh * (16384 + 32768) + lane16, wa1 = wa0 ^ 32768u; OPQV(wa0); OPQV(wa1);
        auto wread = [&](auto U) -> v4u { constexpr int u = U; return lds16(L, ((u & 1) ? wa1 : wa0) + (u >> 1) * 1024); };
        v4u ar[6];
        f32x16 c0 = unpack_tile(r0, r1), c1 = {};
        OPQV(c0);
        wait_flags(rs, (FL_O + (nblk % RO) * 256 + (layer * 64 + tb2) * 2 + kh) * 128u, tg, 1u, lane, 16);
        STAMP(13);
        const unsigned ob = OFF_O + layer * (2 * MB) + (tb2 * 32 + 16 * kh) * 1024u;
        auto oload = [&](auto I) -> v4u { constexpr int i = I; return ld16(rs, lane16 + (i & 3) * 1024, ob + (i >> 2) * 4096u); };
        v4u ofr[16];
        sfor<0, D2>([&](auto I) { constexpr int i = I; ofr[i] = oload(I); });
        BAR();
        STAMP(12);
        sfor<0, 6>([&](auto I) { constexpr int u = I; ar[u] = wread(I); });
        SB();
        sfor<0, 32>([&](auto I) {
          constexpr int u = I;
          if constexpr (u & 1) { c1 = mfma16(ar[u % 6], ofr[u >> 1], c1); OPQV(c1); } else { c0 = mfma16(ar[u % 6], ofr[u >> 1], c0); OPQV(c0); }
          if constexpr (u + 6 < 32) ar[u % 6] = wread(IC(u + 6));
          if constexpr (!(u & 1) && (u >> 1) + D2 < 16) ofr[(u >> 1) + D2] = oload(IC((u >> 1) + D2));
          SB();
        });
        unsigned sown = R4S + cw * 4096 + lane16; OPQV(sown);
#pragma unroll
        for (int g = 0; g < 4; ++g) *(LAS f32x4*)(L + sown + g * 1024) = f32x4{c1[4 * g], c1[4 * g + 1], c1[4 * g + 2], c1[4 * g + 3]};
        OPQV(c0);
        STAMP(14);
        BAR();
        STAMP(15);
#pragma unroll
        for (int g = 0; g < 4; ++g) { const f32x4 t = *(LAS f32x4*)(L + (sown ^ 4096u) + g * 1024); c0[4 * g] += t[0]; c0[4 * g + 1] += t[1]; c0[4 * g + 2] += t[2]; c0[4 * g + 3] += t[3]; }
        v4u y0, y1;
        pack_tile(c0, y0, y1);
        const unsigned xo = OFF_X1 + layer * (2 * MB) + xfo;
        st16(y0, rs, lane16, xo); st16(y1, rs, lane16 + 1024, xo);
        st8(tile_stats(c0), rs, h ? OOB : r * 8, OFF_ST + ((layer * 128 + tb2) * 16 + 2 * nblk + kh) * 256u);
        VMWAIT(0);
        set_flag(rs, (FL_X + layer * 128 + tb2) * 128u + (2 * nblk + kh) * 8, tg, 1u, lane, RX, 192);
      }
      {
        ROLES(); LANE_CONSTS();
        STAMP(16);
        v2u fpa, fpb;
        v4u ar[6];
        unsigned lhi = lane16 + R2; OPQV(lhi);
        unsigned lp0 = lane16 + kh * (16384 + 1024), lp1 = lp0 ^ 1024u;
        unsigned lp0h = lp0 + R2, lp1h = lp1 + R2;
        OPQV(lp0); OPQV(lp1); OPQV(lp0h); OPQV(lp1h);
        auto aread = [&](auto G) -> v4u {
          constexpr int g = G;
          if constexpr (g < 96) { constexpr int T = g % 3, s = g / 3; if constexpr (T < 2) return lds16(L, T * 32768 + s * 1024 + lane16); else return lds16(L, s * 1024 + lhi); }
          else if constexpr (g < 224) {
            constexpr int T = 3 + (g - 96) / 32, s = (g - 96) % 32;
            if constexpr (T == 4 || T == 5) return lds16(L, (T - 4) * 32768 + s * 1024 + lane16);
            else return lds16(L, (T == 3 ? 32768 : 0) + s * 1024 + lhi);
          } else {
            constexpr int u = g - 224, c = u / 16, i = (u % 16) >> 1;
            if constexpr (c == 0 || c == 4) return lds16(L, 32768 + i * 2048 + ((u & 1) ? lp1h : lp0h));
            else if constexpr (c == 3) return lds16(L, i * 2048 + ((u & 1) ? lp1h : lp0h));
            else return lds16(L, ((c == 2 || c == 6) ? 32768 : 0) + i * 2048 + ((u & 1) ? lp1 : lp0));
          }
        };
        unsigned aga = R4V + r * 8; OPQV(aga);
        wait_flags(rs, (FL_X + (mem % RX) * 192 + layer * 128 + tb3) * 128u, tg, 1u, lane, 16);
        STAMP(17);
        v2u sp[8];
        const unsigned xb = OFF_X1 + layer * (2 * MB) + tb3 * 32768u;
        auto xload = [&](auto I) -> v4u { constexpr int i = I; return ld16(rs, lane16 + (i & 3) * 1024, xb + (i >> 2) * 4096u); };
        v4u x[32];
        sfor<0, D3>([&](auto I) { constexpr int i = I; x[i] = xload(I); });
        STAMP(23);
        BAR();
        STAMP(24);
        sfor<0, 6>([&](auto I) { constexpr int u = I; ar[u] = aread(I); });
        SB();
        const unsigned hb = OFF_H + layer * (7 * MB) + (tb3 * 112 + mem * 7) * 1024u;
        const unsigned fh_mine = (FL_H + (layer * 64 + tb3) * 2 + (mem >> 3)) * 128u + (mem & 7) * 8;
        const unsigned fh_poll = (FL_H + (nblk % RO) * 256 + (layer * 64 + tb2) * 2 + kh) * 128u;
        const unsigned xfo = (tb2 * 32 + 4 * nblk + 2 * kh) * 1024u;
        const unsigned hb4 = OFF_H + layer * (7 * MB) + (tb2 * 112 + 56 * kh) * 1024u;
        Stats st;
        float cg, cu;
        f32x16 acc[4] = {};
        f32x16 c0, c1;
        v2u agn;
        v4u hr[D4], r0, r1;
        float ee[4], uu[4]; v4u hf;
        auto epi = [&](auto P_, auto E_, auto K_) {
          constexpr int p = P_, e = E_, k = K_;
          if constexpr (k == 0) { ee[e & 3] = ex2(acc[p & 3][e] * cg); OPQV(ee[e & 3]); }
          if constexpr (k == 1) { ee[e & 3] = __builtin_amdgcn_rcpf(1.0f + ee[e & 3]); uu[e & 3] = acc[p & 3][8 + e] * cu; OPQV(ee[e & 3]); OPQV(uu[e & 3]); }
          if constexpr (k == 2) {
            uu[e & 3] *= ee[e & 3]; OPQV(uu[e & 3]);
            if constexpr (e & 1) hf[e >> 1] = cvtpk(uu[(e - 1) & 3], uu[e & 3]);
            if constexpr (e == 7) st16(hf, rs, lane16 + (p & 3) * 1024, hb + (p >> 2) * 4096);
          }
        };
        SB();
        sfor<0, 336>([&](auto I) {
          constexpr int g = I;
          constexpr bool P4 = g >= 224;
          constexpr int T = g < 96 ? g % 3 : (P4 ? 7 : 3 + (g - 96) / 32), s = g < 96 ? g / 3 : (g - 96) % 32, u = g - 224;
          constexpr int left = g < 96 ? 96 - g : (P4 ? 16 - u % 16 : 32 - s);
          if constexpr (left == 4 && g < 328) {
            BAR();
            ar[(g + 4) % 6] = aread(IC(g + 4));
            ar[(g + 5) % 6] = aread(IC(g + 5));
          }
          if constexpr (g == 96) STAMP(18);
          if constexpr (g == 128) STAMP(20);
          if constexpr (g == 192) STAMP(21);
          if constexpr (g == 224) STAMP(22);
          if constexpr (g == 240) STAMP(25);
          if constexpr (g == 272) STAMP(26);
          if constexpr (g == 304) STAMP(27);
          if constexpr (sched::poll_a(g)) fpa = poll_ld(rs, lane < 8 ? lane * 8 : OOB, fh_poll);
          if constexpr (sched::poll_b(g)) fpb = poll_ld(rs, lane < 8 ? lane * 8 : OOB, fh_poll);
          if constexpr (!P4) {
            if constexpr (g >= 96 && s == 0) { const f32x16 z = {}; acc[T & 3] = mfma8(agn, st.aug, z); }
            acc[T & 3] = mfma16(ar[g % 6], x[s], acc[T & 3]); OPQV(acc[T & 3]);
          } else if constexpr (u == 1) { const f32x16 z = {}; c1 = mfma16(ar[g % 6], hr[0], z); OPQV(c1); }
          else if constexpr (u & 1) { c1 = mfma16(ar[g % 6], hr[(u >> 1) % D4], c1); OPQV(c1); }
          else { c0 = mfma16(ar[g % 6], hr[(u >> 1) % D4], c0); OPQV(c0); }
          if constexpr ((left > 6 || left <= 4) && g + 6 < 336) ar[g % 6] = aread(IC(g + 6));
          if constexpr (g < 96 && T == 0 && s + D3 < 32) x[s + D3] = xload(IC(s + D3));
          if constexpr (g < 24 && T == 1) sp[s] = ld8(rs, h * 2048 + r * 8 + s * 256, OFF_ST + (layer * 128 + tb3) * 4096u);
          if constexpr (g == 50) { st = make_stats(sp, h); cg = st.rstd * -LOG2E; cu = st.rstd; OPQV(cg); OPQV(st.aug); }
          if constexpr (g == 52 || g == 56 || g == 60) agn = *(LAS v2u*)(L + aga + ((g - 52) / 4) * 256);
          if constexpr (g == 55 || g == 59 || g == 63) { constexpr int t = (g - 55) / 4; acc[t] = mfma8(agn, st.aug, acc[t]); OPQV(acc[t]); }
          if constexpr (left == 8 && g < 200) agn = *(LAS v2u*)(L + aga + (g < 96 ? 3 : T + 1) * 256);
          if constexpr (sched::flag(g) > 0) {
            asm volatile("s_waitcnt vmcnt(%0)" :: "i"(sched::younger(sched::store_at(sched::flag(g) - 1), g)) : "memory");
            set_flag(rs, fh_mine, tg, sched::flag(g), lane, RO, 256);
          }
          if constexpr (sched::check(g) > 0) {
            if (!flags_ok((sched::check(g) & 1) || sched::check(g) == 2 ? fpa : fpb, tg, sched::check(g), lane, 8)) wait_flags(rs, fh_poll, tg, sched::check(g), lane, 8);
          }
          if constexpr (T == 3 || (T == 4 && g >= 96)) {
            sfor<0, 16>([&](auto Q) { constexpr int q = Q, k = s - 2 - (3 * q) / 2; if constexpr (k >= 0 && k < 3) epi(IC(2 * (T - 3) + q / 8), IC(q % 8), IC(k)); });
          } else if constexpr (T == 5 || T == 6) {
            sfor<0, 8>([&](auto Q) { constexpr int q = Q, k = s - 2 - 3 * q; if constexpr (k >= 0 && k < 3) epi(IC(T - 1), Q, IC(k)); });
          } else if constexpr (P4) {
            sfor<0, 8>([&](auto Q) { constexpr int q = Q, k = u - 1 - (3 * q) / 2; if constexpr (k >= 0 && k < 3) epi(IC(6), Q, IC(k)); });
          }
          if constexpr (g == sched::RES) { const unsigned xr1 = OFF_X1 + layer * (2 * MB) + xfo; r0 = ld16(rs, lane16, xr1); r1 = ld16(rs, lane16 + 1024, xr1); }
          if constexpr (sched::hload(g) >= 0) {
            constexpr int q = sched::hload(g), c = q >> 3;
            hr[q % D4] = ld16(rs, lane16 + (c & 3) * 1024, hb4 + (c >> 2) * 4096u + (q & 7) * 7168u);
          }
          if constexpr (g == 222) { c0 = unpack_tile(r0, r1); OPQV(c0); }
          SB();
        });
        unsigned sown = R4S + cw * 4096 + lane16; OPQV(sown);
#pragma unroll
        for (int g = 0; g < 4; ++g) *(LAS f32x4*)(L + sown + g * 1024) = f32x4{c1[4 * g], c1[4 * g + 1], c1[4 * g + 2], c1[4 * g + 3]};
        OPQV(c0);
        STAMP(28);
        BAR();
        STAMP(29);
#pragma unroll
        for (int g = 0; g < 4; ++g) { const f32x4 t = *(LAS f32x4*)(L + (sown ^ 4096u) + g * 1024); c0[4 * g] += t[0]; c0[4 * g + 1] += t[1]; c0[4 * g + 2] += t[2]; c0[4 * g + 3] += t[3]; }
        v4u y0, y1;
        pack_tile(c0, y0, y1);
        if (layer == 0) {
          st16(y0, rs, lane16, OFF_X2 + xfo); st16(y1, rs, lane16 + 1024, OFF_X2 + xfo);
          st8(tile_stats(c0), rs, h ? OOB : r * 8, OFF_ST + ((64 + tb2) * 16 + 2 * nblk + kh) * 256u);
          VMWAIT(0);
          set_flag(rs, (FL_X + 64 + tb2) * 128u + (2 * nblk + kh) * 8, tg, 1u, lane, 2 * RX, 192);
        } else {
          const rsrc_t ro = __builtin_amdgcn_make_buffer_rsrc((void*)kargs()->out, 0, SEQ * DM * 2, 0x00020000);
          const unsigned a0 = y0.x, a1 = y0.y, a2 = y0.z, a3 = y0.w, b0 = y1.x, b1 = y1.y, b2 = y1.z, b3 = y1.w;
          const auto p0 = __builtin_amdgcn_permlane32_swap(a0, a2, false, false), p1 = __builtin_amdgcn_permlane32_swap(a1, a3, false, false);
          const auto p2 = __builtin_amdgcn_permlane32_swap(b0, b2, false, false), p3 = __builtin_amdgcn_permlane32_swap(b1, b3, false, false);
          const unsigned vo = r * 1024 + h * 16, so = tb2 * 32768u + nblk * 128u + kh * 64u;
          const v4u z0 = {p0[0], p1[0], p0[1], p1[1]}, z1 = {p2[0], p3[0], p2[1], p3[1]};
          __builtin_amdgcn_raw_buffer_store_b128(z0, ro, vo, so, OUTAUX); __builtin_amdgcn_raw_buffer_store_b128(z1, ro, vo + 32, so, OUTAUX);
          asm volatile("s_nop 1" :: "v"(z0), "v"(z1) : "memory");
        }
        STAMP(30);
      }
    }
    if (layer0 == 1) break;
    {
      ROLES(); LANE_CONSTS();
      STAMP(32);
        const unsigned xb = OFF_X2 + Tq * 32768u;
        const v4u pcs = ld16(rs, lane16, OFF_CSQ + (sg * 32 + j) * 1024u);
        VMWAIT(0);
        BAR();
        STAMP(33);
        ONES_WR();
        unsigned vwa = R4V + ((lane >> 4) & 1) * 512 + (h * 16 + (lane & 15)) * 16; OPQV(vwa);
        v4u wq[4], wv[4];
        wq[0] = lds16(L, R0 + lane16); wq[1] = lds16(L, R0 + lane16 + 1024); wq[2] = lds16(L, R0 + lane16 + 2048); wv[0] = lds16(L, vwa);
        sfor<0, 8>([&](auto I) { CAUSAL2(2 * I); });
        wait_flags(rs, (FL_X + (j % (2 * RX)) * 192 + 64 + Tq) * 128u, tg, 1u, lane, 16);
        STAMP(34);
        auto xload = [&](auto I) -> v4u { constexpr int i = I; return ld16(rs, lane16 + (i & 3) * 1024, xb + (i >> 2) * 4096u); };
        v4u xr[D1];
        sfor<0, D1>([&](auto I) { constexpr int i = I; xr[i] = xload(I); });
        v2u sp[8];
        csv = __uint_as_float(pcs.z);
        Stats st;
        cqk = f32x16{}; cva = f32x4{}; cvb = f32x4{};
        SB();
        sfor<0, 36>([&](auto I) {
          constexpr int s = I;
          if constexpr (s < 32) { cqk = mfma16(wq[s & 3], xr[s % D1], cqk); OPQV(cqk); }
          if constexpr (s + 3 < 32) wq[(s + 3) & 3] = lds16(L, R0 + lane16 + (s + 3) * 1024);
          if constexpr (!(s & 1) && s + 2 < 32) wv[((s >> 1) + 1) & 3] = lds16(L, vwa + (s + 2) * 512);
          VSTEP(s, xr, D1);
          if constexpr (s >= 4 && s - 4 + D1 < 32) xr[(s - 4) % D1] = xload(IC(s - 4 + D1));
          if constexpr (s >= 4 && s < 12) sp[s - 4] = ld8(rs, h * 2048 + r * 8 + (s - 4) * 256, OFF_ST + (64 + Tq) * 4096u);
          if constexpr (s == 20) { st = make_stats(sp, h); OPQV(st.aug); rstd1 = st.rstd; BOUNCE_WR(st); }
          if constexpr (s == 22) { cqk = mfma8(v2u{pcs.x, pcs.y}, st.aug, cqk); OPQV(cqk); BOUNCE_RD(); }
          if constexpr (s == 35) asm volatile(".Lmk_last:");
          SB();
        });

    }
  }
  STAMP_OUT();
}

extern "C" void kernel_launch(void* const* d_in, const int* in_sizes, int n_in, void* d_out, int out_size, void* d_ws, size_t ws_size,
                              hipStream_t stream) {
  Args a;
  a.ws = (unsigned char*)d_ws; a.out = (u16*)d_out;
  a.x = (const u16*)d_in[0]; a.wqkv = (const u16*)d_in[1]; a.wo = (const u16*)d_in[2];
  a.wg = (const u16*)d_in[4]; a.wu = (const u16*)d_in[5]; a.wd = (const u16*)d_in[6];
  (void)hipFuncSetAttribute((const void*)main_k, hipFuncAttributeMaxDynamicSharedMemorySize, LDS_BYTES);
  main_k<<<256, 512, LDS_BYTES, stream>>>(a);
}
```

```cpp
#define SPINTO 8000
#define SPINMIN
#include <hip/hip_runtime.h>
#include <stdint.h>
#include <type_traits>

typedef unsigned short u16;
typedef float f32x16 __attribute__((ext_vector_type(16)));
typedef float f32x4 __attribute__((ext_vector_type(4)));
typedef float f32x2 __attribute__((ext_vector_type(2)));
typedef __bf16 bf16x8 __attribute__((ext_vector_type(8)));
typedef __bf16 bf16x2 __attribute__((ext_vector_type(2)));
typedef short s16x4 __attribute__((ext_vector_type(4)));
typedef unsigned v4u __attribute__((ext_vector_type(4)));
typedef unsigned v2u __attribute__((ext_vector_type(2)));
#define GAS __attribute__((address_space(1)))
#define LAS __attribute__((address_space(3)))
#define DI __device__ __forceinline__
#define RLX_AGENT __ATOMIC_RELAXED, __HIP_MEMORY_SCOPE_AGENT
#define IC(n) std::integral_constant<int, (n)>{}
typedef __amdgpu_buffer_rsrc_t rsrc_t;

constexpr int SEQ = 2048, DM = 512, FF = 1792;
constexpr unsigned KB = 1024u, MB = 1u << 20;
constexpr unsigned FL_EPOCH = 0;
constexpr unsigned FL_SEEN = 2;
constexpr unsigned FL_P = 32;
#define RQV 4
#define RXV 4
#define ROV 2
constexpr unsigned RQ = RQV, RX = RXV, RO = ROV;
constexpr unsigned FL_Q = FL_P + 44;
constexpr unsigned FL_X = FL_Q + RQ * 16;
constexpr unsigned FL_O = FL_X + RX * 192;
constexpr unsigned FL_H = FL_O + RO * 256;
constexpr unsigned FL_KV = FL_H + RO * 256;
constexpr unsigned FL_LINES = FL_KV + 2 * 16 * 16;
static_assert(FL_LINES * 128 <= 768 * KB, "flags");
constexpr unsigned OFF_AUG = 768 * KB;
constexpr unsigned OFF_ST0 = 896 * KB;
constexpr unsigned OFF_CSP = 10 * MB;
constexpr unsigned OFF_WT = 1 * MB, OFF_WO = 2 * MB + 512 * KB, OFF_WGU = 3 * MB, OFF_WD = 6 * MB + 512 * KB;
constexpr unsigned OFF_ST = 9 * MB;
constexpr unsigned OFF_X1 = 11 * MB, OFF_X2 = 15 * MB, OFF_O = 17 * MB, OFF_KV = 21 * MB, OFF_H = 29 * MB;
constexpr unsigned WS_BYTES = 43 * MB;
constexpr unsigned OOB = 0x40000000u;
constexpr unsigned R0 = 0, R1 = 32 * KB, R2 = 64 * KB, R3 = 96 * KB, R4V = 128 * KB, R4S = 144 * KB, LDS_BYTES = 160 * KB;
constexpr unsigned KVS = R3, S_ONES = R4S, S_KMAX = R4S + 2048, S_RELAY = R4S + 2304, S_DONE = R4S + 2368, S_AUGQ = R4S + 2560, S_RSTD = R4S + 3584,
                   S_Q = R4S + 4096, S_RDY = R4V + 4096, S_ATT = R4S + 2432, S_AUGQ1 = R3, S_PART = R4S + 12288, S_DUMMY = R4S + 14336;
constexpr float NEG_BIG = -1.0e30f;
constexpr float LOG2E = 1.4426950408889634f;
constexpr float SAFE2 = 10000.0f;
constexpr unsigned SPIN_CAP = 1u << 18;
#define D1V 8
#define D2V 8
#define D3V 8
#define D4V 10
#define OUTAUX 0
#define CODEPF 1
#define XDELAY 3
#define SDELAY 0
#define W2DMA dma4_paced
#define PRIO 1
#define WODMA dma4
#define PDELAY 0
#define F7AT 32
#define RSWAP 1
#define FA 25
#define F5 17
#define F6 10
#define PB 12
#define PA 11
#define PUTGAP 6
#define KVEARLY 0
#define ODELAY 0
constexpr int D1 = D1V, D2 = D2V, D3 = D3V, D4 = D4V;

template <int B, int E, class F> DI void sfor(F&& f) {
  if constexpr (B < E) { f(std::integral_constant<int, B>{}); sfor<B + 1, E>(f); }
}
DI bf16x8 as_bf8(v4u v) { return __builtin_bit_cast(bf16x8, v); }
DI bf16x2 as_bf2(unsigned v) { return __builtin_bit_cast(bf16x2, v); }
DI s16x4 as_s4(v2u v) { return __builtin_bit_cast(s16x4, v); }
DI f32x16 mfma16(v4u a, v4u b, f32x16 c) { return __builtin_amdgcn_mfma_f32_32x32x16_bf16(as_bf8(a), as_bf8(b), c, 0, 0, 0); }
DI f32x16 mfma8(v2u a, v2u b, f32x16 c) { return __builtin_amdgcn_mfma_f32_32x32x8bf16_1k(as_s4(a), as_s4(b), c, 0, 0, 0); }
DI unsigned cvtpk(float lo, float hi) { f32x2 f = {lo, hi}; bf16x2 p = __builtin_convertvector(f, bf16x2); return __builtin_bit_cast(unsigned, p); }
DI float bflo(unsigned v) { return __uint_as_float(v << 16); }
DI float bfhi(unsigned v) { return __uint_as_float(v & 0xffff0000u); }
DI float dot2(unsigned a, unsigned b, float c) { return __builtin_amdgcn_fdot2_f32_bf16(as_bf2(a), as_bf2(b), c, false); }
DI float ex2(float v) { return __builtin_amdgcn_exp2f(v); }
DI float xhalf_add(float v) { auto r = __builtin_amdgcn_permlane32_swap(__float_as_uint(v), __float_as_uint(v), false, false); return __uint_as_float(r[0]) + __uint_as_float(r[1]); }
DI float xhalf_max(float v) { auto r = __builtin_amdgcn_permlane32_swap(__float_as_uint(v), __float_as_uint(v), false, false); return fmaxf(__uint_as_float(r[0]), __uint_as_float(r[1])); }
DI bool all_lanes(bool p) { return __builtin_amdgcn_ballot_w64(!p) == 0ull; }
DI unsigned umax(unsigned a, unsigned b) { return a > b ? a : b; }
DI unsigned row_umax(unsigned v) {
  v = umax(v, (unsigned)__builtin_amdgcn_update_dpp(0, (int)v, 0x121, 0xf, 0xf, true));
  v = umax(v, (unsigned)__builtin_amdgcn_update_dpp(0, (int)v, 0x122, 0xf, 0xf, true));
  v = umax(v, (unsigned)__builtin_amdgcn_update_dpp(0, (int)v, 0x124, 0xf, 0xf, true));
  v = umax(v, (unsigned)__builtin_amdgcn_update_dpp(0, (int)v, 0x128, 0xf, 0xf, true));
  return v;
}
DI v4u ld16(rsrc_t rs, unsigned voff, unsigned soff) { return __builtin_amdgcn_raw_buffer_load_b128(rs, voff, soff, 16); }
DI v4u ld16p(rsrc_t rs, unsigned voff, unsigned soff) { return __builtin_amdgcn_raw_buffer_load_b128(rs, voff, soff, 0); }
DI v2u ld8p(rsrc_t rs, unsigned voff, unsigned soff) { return __builtin_bit_cast(v2u, __builtin_amdgcn_raw_buffer_load_b64(rs, voff, soff, 0)); }
DI v2u ld8(rsrc_t rs, unsigned voff, unsigned soff) { return __builtin_bit_cast(v2u, __builtin_amdgcn_raw_buffer_load_b64(rs, voff, soff, 16)); }
DI void st16(v4u v, rsrc_t rs, unsigned voff, unsigned soff) {
  __builtin_amdgcn_raw_buffer_store_b128(v, rs, voff, soff, 16);
  asm volatile("s_nop 1" :: "v"(v) : "memory");
}
DI void st16n(v4u v, rsrc_t rs, unsigned voff, unsigned soff) { __builtin_amdgcn_raw_buffer_store_b128(v, rs, voff, soff, 16); }
#define KEEP(x) asm volatile("" :: "v"(x))
DI void st8(v2u v, rsrc_t rs, unsigned voff, unsigned soff) { __builtin_amdgcn_raw_buffer_store_b64(v, rs, voff, soff, 16); }
DI v4u lds16(LAS unsigned char* L, unsigned off) { return *(LAS v4u*)(L + off); }
#define SB() __builtin_amdgcn_sched_barrier(0)
#define OPQV(x) asm volatile("" : "+v"(x))
#define OPQS(x) asm volatile("" : "+s"(x))
#define LANE_CONSTS() int lane; asm volatile("v_mbcnt_lo_u32_b32 %0, -1, 0\n\tv_mbcnt_hi_u32_b32 %0, -1, %0" : "=v"(lane)); const int r = lane & 31, h = lane >> 5; const unsigned lane16 = lane * 16; (void)r; (void)h
#define BAR() asm volatile("s_waitcnt lgkmcnt(0)\n\ts_barrier" ::: "memory")
#define VMWAIT(n) asm volatile("s_waitcnt vmcnt(" #n ")" ::: "memory")
#define DMA_HEAD "s_nop 4\n\ts_mov_b32 m0, %0\n\ts_nop 0\n\t"
#define DMA_LD(o) "buffer_load_dwordx4 %1, %2, %3 offen offset:" #o " sc1 lds\n\t"
#define DMA_ARGS :: "s"(lds), "v"(voff), "s"(rs), "s"(soff) : "memory"
DI void dma4(rsrc_t rs, unsigned lds, unsigned soff, unsigned voff) { asm volatile(DMA_HEAD DMA_LD(0) DMA_LD(1024) DMA_LD(2048) DMA_LD(3072) DMA_ARGS); }
DI void dma2(rsrc_t rs, unsigned lds, unsigned soff, unsigned voff) { asm volatile(DMA_HEAD DMA_LD(0) DMA_LD(1024) DMA_ARGS); }
DI void dma1p(rsrc_t rs, unsigned lds, unsigned soff, unsigned voff) { asm volatile(DMA_HEAD "buffer_load_dwordx4 %1, %2, %3 offen lds" DMA_ARGS); }
#define PACE 6
#define STR2(x) #x
#define STR(x) STR2(x)
#define DMA_W "s_waitcnt vmcnt(" STR(PACE) ")\n\t"
static_assert(PACE <= 8, "some hand-offs count on the pacing waits");
DI void dma4_paced(rsrc_t rs, unsigned lds, unsigned soff, unsigned voff) {
  asm volatile(DMA_HEAD DMA_LD(0) DMA_W DMA_LD(1024) DMA_W DMA_LD(2048) DMA_W DMA_LD(3072) DMA_W DMA_ARGS);
}
DI void dma_tile(rsrc_t rs, unsigned lds, unsigned soff, unsigned lw, unsigned lane16) {
  dma4(rs, lds + lw * 8192u, soff + lw * 8192u, lane16);
  dma4(rs, lds + lw * 8192u + 4096u, soff + lw * 8192u + 4096u, lane16);
}
extern "C" __device__ long long wsatomic_add64(long long, rsrc_t, int, int, int) __asm("llvm.amdgcn.raw.ptr.buffer.atomic.add.i64");
struct Tag { unsigned lo, hi; };
DI v2u poll_ld(rsrc_t rs, unsigned voff, unsigned soff) {
  long long z = 0; OPQV(z);
  return __builtin_bit_cast(v2u, wsatomic_add64(z, rs, voff, soff, 0));
}
DI bool word_ok(v2u f, Tag t, unsigned target) { const unsigned a = f.x, b = f.y; return b == t.hi && (a - t.lo - target) <= 0xffffu - target; }
DI bool flags_ok(v2u f, Tag t, unsigned target, int lane, int n) { return all_lanes(word_ok(f, t, target) || lane >= n); }
DI v2u wait_words(rsrc_t rs, unsigned vo, unsigned soff, Tag t, unsigned target, bool act) {
  v2u f = {0u, 0u};
#pragma nounroll
  for (unsigned spins = 0; spins < SPIN_CAP; ++spins) {
    f = poll_ld(rs, vo, soff);
    if (all_lanes(word_ok(f, t, target) || !act)) break;
  }
  asm volatile("" ::: "memory");
  return f;
}
DI v2u wait_flags(rsrc_t rs, unsigned soff, Tag t, unsigned target, int lane, int n) {
  return wait_words(rs, lane < n ? lane * 8 : OOB, soff, t, target, lane < n);
}
DI void relay_set(LAS unsigned char* L, Tag t, unsigned id) { *(volatile LAS v2u*)(L + S_RELAY) = v2u{t.lo | id, t.hi}; }
DI bool relay_get(LAS unsigned char* L, Tag t, unsigned id) {
  const v2u v = *(volatile LAS v2u*)(L + S_RELAY);
  const unsigned a = v.x, b = v.y;
  return __builtin_amdgcn_readfirstlane(a) == (t.lo | id) && __builtin_amdgcn_readfirstlane(b) == t.hi;
}
DI void relay_wait(LAS unsigned char* L, Tag t, unsigned id) {
#pragma nounroll
  for (unsigned spins = 0; spins < 64 * SPIN_CAP && !relay_get(L, t, id); ++spins) __builtin_amdgcn_s_sleep(1);
  asm volatile("" ::: "memory");
}
DI void ready_set(LAS unsigned char* L, unsigned at, int lw, Tag t, unsigned id) {
  asm volatile("s_waitcnt lgkmcnt(0)" ::: "memory");
  *(volatile LAS v2u*)(L + at + lw * 8) = v2u{t.lo | id, t.hi};
}
DI void ready_wait(LAS unsigned char* L, unsigned at, int lane, Tag t, unsigned id) {
#pragma nounroll
  for (unsigned spins = 0; spins < 64 * SPIN_CAP; ++spins) {
    const v2u d = *(volatile LAS v2u*)(L + at + (lane & 3) * 8);
    const unsigned d0 = d.x, d1 = d.y;
    if (all_lanes(d0 == (t.lo | id) && d1 == t.hi)) break;
    __builtin_amdgcn_s_sleep(1);
  }
  asm volatile("" ::: "memory");
}
DI void swap64(rsrc_t rs, unsigned voff, unsigned soff, v2u v) {
  asm volatile("s_nop 4\n\tbuffer_atomic_swap_x2 %0, %1, %2, %3 offen\n\ts_nop 1" :: "v"(v), "v"(voff), "s"(rs), "s"(soff) : "memory");
}
DI void set_flag(rsrc_t rs, unsigned off, Tag t, unsigned payload, int lane, unsigned nrep = 1, unsigned rstep = 0) {
  swap64(rs, (unsigned)lane < nrep ? lane * rstep * 128u : OOB, off, v2u{t.lo | payload, t.hi});
}

struct Stats { float rstd, mean; v2u aug; };
DI Stats finish_stats(float s1, float s2, int h) {
  const float mean = s1 * (1.0f / DM);
  const float var = fmaxf(s2 * (1.0f / DM) - mean * mean, 0.0f);
  Stats st; st.rstd = __builtin_amdgcn_rsqf(var + 1e-5f); st.mean = mean;
  const float nm = h ? 0.0f : -mean;
  const unsigned hi = cvtpk(nm, nm) & 0xffffu;
  const float lo = nm - __uint_as_float(hi << 16);
  const unsigned w = hi | (cvtpk(lo, lo) << 16);
  st.aug.x = w; st.aug.y = w;
  return st;
}
DI Stats make_stats(const v2u (&sp)[8], int h) {
  float s1 = 0.f, s2 = 0.f;
#pragma unroll
  for (int i = 0; i < 8; ++i) { const unsigned a = sp[i].x, b = sp[i].y; s1 += __uint_as_float(a); s2 += __uint_as_float(b); }
  return finish_stats(xhalf_add(s1), xhalf_add(s2), h);
}
DI float xrow_add(float v) { auto r = __builtin_amdgcn_permlane16_swap(__float_as_uint(v), __float_as_uint(v), false, false); return __uint_as_float(r[0]) + __uint_as_float(r[1]); }
DI float wave_add(float v) {
  v += __int_as_float(__builtin_amdgcn_update_dpp(0, __float_as_int(v), 0x121, 0xf, 0xf, true));
  v += __int_as_float(__builtin_amdgcn_update_dpp(0, __float_as_int(v), 0x122, 0xf, 0xf, true));
  v += __int_as_float(__builtin_amdgcn_update_dpp(0, __float_as_int(v), 0x124, 0xf, 0xf, true));
  v += __int_as_float(__builtin_amdgcn_update_dpp(0, __float_as_int(v), 0x128, 0xf, 0xf, true));
  return xhalf_add(xrow_add(v));
}
DI v2u trd(LAS unsigned char* L, unsigned off) { return __builtin_bit_cast(v2u, __builtin_amdgcn_ds_read_tr16_b64_v4i16((LAS s16x4*)(L + off))); }
DI v4u trfrag(LAS unsigned char* L, unsigned off, unsigned second) { const v2u a = trd(L, off), b = trd(L, off + second); return v4u{a.x, a.y, b.x, b.y}; }
DI float sum8(v4u f, float c) {
  const unsigned a = f.x, b = f.y, d = f.z, e = f.w;
  return dot2(a, 0x3f803f80u, dot2(b, 0x3f803f80u, dot2(d, 0x3f803f80u, dot2(e, 0x3f803f80u, c))));
}
DI v2u split_aug(float cs) {
  const unsigned hi = cvtpk(cs, cs);
  const float lo = cs - __uint_as_float(hi << 16);
  return v2u{hi, cvtpk(lo, lo)};
}
DI float sum4(v4u p) {
  const unsigned a = p.x, b = p.y, c = p.z, d = p.w;
  return (__uint_as_float(a) + __uint_as_float(b)) + (__uint_as_float(c) + __uint_as_float(d));
}
DI v2u make_aug(v4u p) { return split_aug(sum4(p)); }

DI float max16(const f32x16& s, float m) {
  m = fmaxf(fmaxf(m, s[0]), s[1]);   m = fmaxf(fmaxf(m, s[2]), s[3]);
  float n = fmaxf(s[4], s[5]);       n = fmaxf(fmaxf(n, s[6]), s[7]);
  m = fmaxf(fmaxf(m, s[8]), s[9]);   n = fmaxf(fmaxf(n, s[10]), s[11]);
  m = fmaxf(fmaxf(m, s[12]), s[13]); n = fmaxf(fmaxf(n, s[14]), s[15]);
  return fmaxf(m, n);
}
DI void exp_pv(const f32x16& s, v4u v0, v4u v1, f32x16& o) {
  v4u p0, p1;
  p0.x = cvtpk(ex2(s[0]), ex2(s[1])); p0.y = cvtpk(ex2(s[2]), ex2(s[3])); p0.z = cvtpk(ex2(s[4]), ex2(s[5])); p0.w = cvtpk(ex2(s[6]), ex2(s[7]));
  o = mfma16(v0, p0, o);
  p1.x = cvtpk(ex2(s[8]), ex2(s[9])); p1.y = cvtpk(ex2(s[10]), ex2(s[11])); p1.z = cvtpk(ex2(s[12]), ex2(s[13])); p1.w = cvtpk(ex2(s[14]), ex2(s[15]));
  o = mfma16(v1, p1, o);
}
DI void attn_general(LAS unsigned char* L, v4u q, const f32x16& cm, int bm, int bf0, int nf, unsigned kaddr, unsigned vbase, unsigned vstep,
                     float& m, f32x16& o) {
  const f32x16 zero = {};
  float mx = max16(mfma16(lds16(L, kaddr + bm * 4096), q, cm), m);
#pragma nounroll
  for (int b = 0; b < nf; ++b) mx = max16(mfma16(lds16(L, kaddr + (bf0 + b) * 4096), q, zero), mx);
  mx = xhalf_max(mx);
  const float f = ex2(m - mx);
#pragma unroll
  for (int i = 0; i < 16; ++i) o[i] *= f;
  m = mx;
  f32x16 c;
#pragma unroll
  for (int i = 0; i < 16; ++i) c[i] = cm[i] - mx;
  unsigned va = vbase + vstep * bm;
  exp_pv(mfma16(lds16(L, kaddr + bm * 4096), q, c), lds16(L, va), lds16(L, va + 1024), o);
#pragma unroll
  for (int i = 0; i < 16; ++i) c[i] = -mx;
#pragma nounroll
  for (int b = 0; b < nf; ++b) {
    va = vbase + vstep * (bf0 + b);
    exp_pv(mfma16(lds16(L, kaddr + (bf0 + b) * 4096), q, c), lds16(L, va), lds16(L, va + 1024), o);
  }
}
template <int K, int KMAX, int S0, int DIR, class H>
DI void attn_chain(LAS unsigned char* L, v4u q, int n, unsigned kaddr, unsigned va, unsigned vstep, const f32x16& s, v4u v0, v4u v1, v4u kn,
                   f32x16& o, H&& hook) {
  if constexpr (K == KMAX) { hook(IC(K)); exp_pv(s, v0, v1, o); }
  else {
    const f32x16 zero = {};
    f32x16 sn = mfma16(kn, q, zero);
    v4u w0 = lds16(L, va), w1 = lds16(L, va + 1024);
    if constexpr (K + 1 < KMAX) kn = lds16(L, kaddr + (S0 + DIR * (K + 1)) * 4096);
    hook(IC(K));
    SB();
    exp_pv(s, v0, v1, o);
    asm volatile("" : "+v"(sn), "+v"(w0), "+v"(w1), "+v"(kn), "+v"(o));
    if ((DIR < 0 && K < 4) || n > K) attn_chain<K + 1, KMAX, S0, DIR>(L, q, n, kaddr, DIR > 0 ? va + vstep : va - vstep, vstep, sn, w0, w1, kn, o, hook);
  }
}

DI f32x16 unpack_tile(v4u r0, v4u r1) {
  f32x16 k;
  k[0] = bflo(r0.x); k[1] = bfhi(r0.x); k[2] = bflo(r0.y); k[3] = bfhi(r0.y); k[4] = bflo(r0.z); k[5] = bfhi(r0.z); k[6] = bflo(r0.w); k[7] = bfhi(r0.w);
  k[8] = bflo(r1.x); k[9] = bfhi(r1.x); k[10] = bflo(r1.y); k[11] = bfhi(r1.y); k[12] = bflo(r1.z); k[13] = bfhi(r1.z); k[14] = bflo(r1.w); k[15] = bfhi(r1.w);
  return k;
}
DI void pack_tile(const f32x16& k, v4u& y0, v4u& y1) {
  y0.x = cvtpk(k[0], k[1]); y0.y = cvtpk(k[2], k[3]); y0.z = cvtpk(k[4], k[5]); y0.w = cvtpk(k[6], k[7]);
  y1.x = cvtpk(k[8], k[9]); y1.y = cvtpk(k[10], k[11]); y1.z = cvtpk(k[12], k[13]); y1.w = cvtpk(k[14], k[15]);
}
DI v2u tile_stats(const f32x16& k) {
  f32x2 a = {k[0], k[1]}, b = a * a;
#pragma unroll
  for (int i = 1; i < 8; ++i) { const f32x2 t = {k[2 * i], k[2 * i + 1]}; a += t; b += t * t; }
  return v2u{__float_as_uint(xhalf_add(a[0] + a[1])), __float_as_uint(xhalf_add(b[0] + b[1]))};
}

constexpr int rslot(int f) { return RSWAP ? (f % 6) ^ ((f / 6) & 1) : f % 6; }
constexpr int rnext(int u) { return !RSWAP ? u + 6 : (u & 1) ? u + 5 : u + 7; }
#define RING_FILL(rd) sfor<0, 6>([&](auto I_) { constexpr int f_ = I_ ^ 1; ar[f_] = rd(IC(f_)); })
#define RING_LATE(rd, B) do { ar[rslot((B) + 1)] = rd(IC((B) + 1)); ar[rslot(B)] = rd(IC(B)); } while (0)
namespace sched {
constexpr int T4 = 128, T5 = 160, T6 = 192, P4 = 224, RES = T6 + 19;
constexpr int epi_at(int n) { return n < 40 ? 98 + 2 * n : (n < 48 ? T6 + 2 + 2 * (n - 40) : P4 + 1 + (3 * (n - 48)) / 2); }
constexpr int store_at(int p) { return epi_at(8 * p + 7) + 4; }
constexpr int flag(int g) { return g == T4 + FA ? 2 : g == T5 + FA ? 4 : g == T6 + F5 ? 5 : g == P4 + F6 ? 6 : g == P4 + F7AT ? 7 : 0; }
constexpr int hload(int g) { return g >= T6 + 21 && g < T6 + 21 + D4 ? g - T6 - 21 : (g > P4 && (g & 1) && (g - P4) / 2 + D4 < 56 ? (g - P4) / 2 + D4 : -1); }
constexpr int need(int c) { return P4 + 2 * (8 * c - D4) - 1; }
constexpr int check(int g) { return g == T6 + 18 ? 2 : g == need(2) ? 4 : 0; }
constexpr bool poll_a(int g) { return g == T5 + PA; }
constexpr bool poll_b(int g) { return g == T6 + PB; }
constexpr int pre(int g) { return (poll_a(g) ? 1 : 0) + (poll_b(g) ? 1 : 0); }
constexpr int post(int g) { return (g == RES ? 2 : 0) + (hload(g) >= 0 ? 1 : 0); }
constexpr bool is_store(int g) { for (int p = 0; p < 7; ++p) if (store_at(p) == g) return true; return false; }
constexpr int younger(int gs, int gw) {
  int n = post(gs) + pre(gw);
  for (int g = gs + 1; g < gw; ++g) n += pre(g) + (flag(g) ? 1 : 0) + (is_store(g) ? 1 : 0) + post(g);
  return n;
}
static_assert(D4 <= 10 && need(2) > P4, "chunk c is first requested behind the barrier that ends chunk c - 2");
}
struct Args { unsigned char* ws; u16* out; const u16 *x, *wqkv, *wo, *wg, *wu, *wd; };
#define STAMP(id) do {} while (0)
#define STAMP_DECL() do {} while (0)
#define STAMP_OUT() do {} while (0)

typedef const __attribute__((address_space(4))) Args* kargs_t;
DI kargs_t kargs() { kargs_t p = (kargs_t)__builtin_amdgcn_kernarg_segment_ptr(); OPQS(p); return p; }
__global__ void __launch_bounds__(512, 2) main_k(Args a) {
  extern __shared__ __attribute__((aligned(16))) unsigned char smem[];
  LAS unsigned char* L = (LAS unsigned char*)smem;
  const unsigned lb = (unsigned)(size_t)L;
  const int w0 = __builtin_amdgcn_readfirstlane(threadIdx.x >> 6);
  STAMP_DECL();
  const unsigned long long rts = __builtin_amdgcn_s_memrealtime();
  const int xcd = blockIdx.x & 7, idx = blockIdx.x >> 3;
  const int grp0 = (xcd & ~1) * 2 + (idx >> 3), mem0 = ((idx & 4) << 1) | ((xcd & 1) << 2) | (idx & 3);
  const rsrc_t rs = __builtin_amdgcn_make_buffer_rsrc(a.ws, 0, WS_BYTES, 0x00020000);
  if (threadIdx.x == 0) __builtin_amdgcn_raw_buffer_store_b32((unsigned)rts, rs, 0, 10 * MB + 768 * KB + blockIdx.x * 4, 16);
#define UNI(x) (x)
#define ROLES() int w = w0, grp = grp0, mem = mem0, layer = UNI(layer0); OPQS(w); OPQS(grp); OPQS(mem); OPQS(layer); Tag tg = {(unsigned)UNI(tg0.lo), (unsigned)UNI(tg0.hi)}; OPQS(tg.lo); OPQS(tg.hi); \
  const int nblk = mem & 7, cw = w & 3, th = cw >> 1, kh = cw & 1;              \
  const int tb2 = 4 * grp + 2 * (mem >> 3) + th, tb3 = 4 * grp + cw;            \
  (void)nblk; (void)kh; (void)tb2; (void)tb3
  Tag tg0 = {0u, 0u};
  f32x16 cmc;
#define CAUSAL2(cm, i) do { float a0 = (((i) & 3) + 8 * ((i) >> 2) + 4 * h <= r) ? 0.0f : NEG_BIG, a1 = ((((i) + 1) & 3) + 8 * ((i) >> 2) + 4 * h <= r) ? 0.0f : NEG_BIG; \
    OPQV(a0); OPQV(a1); cm[i] = a0; cm[(i) + 1] = a1; } while (0)
#define MAKE_TAG(ep) do { const unsigned e0 = __builtin_amdgcn_readfirstlane((ep).x) & 0xffff0000u, e1 = __builtin_amdgcn_readfirstlane((ep).y); \
    tg.lo = e0 + 0x10000u; tg.hi = e1 + (tg.lo == 0u ? 1u : 0u); tg0 = tg; } while (0)
#define AUGQ_LOAD(cp) do { const unsigned tile_ = (cw == 2 ? 0 : 16 + 16 * cw) + mem;     _Pragma("unroll") for (int i_ = 0; i_ < 16; ++i_) \
    cp[i_] = __builtin_amdgcn_raw_buffer_load_b32(rs, cw < 3 ? h * 2048 + r * 4 + i_ * 128 : OOB, OFF_CSP + tile_ * 4096u, 16); } while (0)
#define AUGQ_PUT(cp, at) do { float s_ = 0.f; _Pragma("unroll") for (int i_ = 0; i_ < 16; ++i_) s_ += __uint_as_float(cp[i_]); \
    *(LAS v2u*)(L + (at) + cw * 256 + r * 8) = split_aug(xhalf_add(s_)); } while (0)

  auto qkv = [&](auto FIRST_, v4u (&x)[32], Stats st, int layer0) {
    constexpr bool FIRST = FIRST_;
    ROLES(); LANE_CONSTS();
    unsigned lhi = lane16 + R2; OPQV(lhi);
    auto wread = [&](auto U) -> v4u { constexpr int u = U; if constexpr (u < 64) return lds16(L, (u & 1) * 32768 + (u >> 1) * 1024 + lane16); else return lds16(L, (u - 64) * 1024 + lhi); };
    const unsigned xb = OFF_X2 + tb3 * 32768u;
    auto xload = [&](auto I) -> v4u { constexpr int i = I; return ld16(rs, lane16 + (i & 3) * 1024, xb + (i >> 2) * 4096u); };
    v4u ar[6];
    if constexpr (!FIRST) {
      sfor<0, 8>([&](auto I) { CAUSAL2(cmc, 2 * I); });
      __builtin_amdgcn_s_sleep(PDELAY);
      wait_flags(rs, (FL_X + (mem % RX) * 192 + 64 + tb3) * 128u, tg, 1u, lane, 16);
      STAMP(34);
      sfor<0, D1>([&](auto I) { constexpr int i = I; x[i] = xload(I); });
      ready_wait(L, S_RDY, lane, tg, 3u);
    }
    RING_FILL(wread);
    constexpr int lay = FIRST ? 0 : 1;
    const unsigned kvb = OFF_KV + lay * (4 * MB) + (mem * 64 + tb3) * 4096u, kvo = grp < 15 ? lane16 : OOB;
    unsigned own = KVS + (8 + cw) * 4096 + lane16; OPQV(own);
    f32x16 ck = {}, cv = {}, cq;
    v2u sp[8], ag;
    f32x4 rr[4];
    v4u kvf[4];
    float k2 = 0.f;
    unsigned kr, k16;
    SB();
    sfor<0, 96>([&](auto I) {
      constexpr int u = I, s = u < 64 ? u >> 1 : u - 64;
      if constexpr (FIRST && u == 28) VMWAIT(0);
      if constexpr (u == 28 || u == 60) { BAR(); RING_LATE(wread, u + 4); }
      if constexpr (u == 64) { const f32x16 z = {}; cq = mfma8(ag, st.aug, z); }
      if constexpr (u >= 64) { cq = mfma16(ar[rslot(u)], x[s], cq); OPQV(cq); }
      else if constexpr (u & 1) { cv = mfma16(x[s], ar[rslot(u)], cv); OPQV(cv); }
      else { ck = mfma16(ar[rslot(u)], x[s], ck); OPQV(ck); }
      if constexpr (rnext(u) < 96 && u != 26 && u != 27 && u != 58 && u != 59) ar[rslot(u)] = wread(IC(rnext(u)));
      if constexpr (!FIRST) {
        if constexpr (u < 64 && (u & 1) && s + D1 < 32) x[s + D1] = xload(IC(s + D1));
        if constexpr (u < 16 && !(u & 1)) sp[s] = ld8(rs, h * 2048 + r * 8 + s * 256, OFF_ST + (64 + tb3) * 4096u);
        if constexpr (u == 36) { st = make_stats(sp, h); OPQV(st.aug); }
      }
      if constexpr (u == 32) { unsigned o2 = 0x3f803f80u; OPQV(o2); *(LAS v4u*)(L + S_ONES + (lane & 1) * 1024) = v4u{o2, o2, o2, o2}; }
      if constexpr (u == 38) *(LAS float*)(L + S_RSTD + cw * 128 + r * 4) = st.rstd;
      if constexpr (u == 40 || u == 45 || u == 50) ag = *(LAS v2u*)(L + (FIRST ? S_AUGQ : S_AUGQ1) + ((u - 40) / 5) * 256 + r * 8);
      if constexpr (u == 43) { ck = mfma8(ag, st.aug, ck); OPQV(ck); }
      if constexpr (u == 48) { cv = mfma8(st.aug, ag, cv); OPQV(cv); }
      if constexpr (u == 52) {
#pragma unroll
        for (int g = 0; g < 4; ++g) rr[g] = *(LAS f32x4*)(L + S_RSTD + cw * 128 + (8 * g + 4 * h) * 4);
      }
      if constexpr (u >= 66 && u < 86 && (u - 66) % 5 < 4) {
        constexpr int n = (u - 66) / 5, i = (n & 1) * 4 + (u - 66) % 5;
        float a0, a1;
        if constexpr (n < 2) { a0 = ck[2 * i] * st.rstd; OPQV(a0); a1 = ck[2 * i + 1] * st.rstd; OPQV(a1); ck[2 * i] = a0; ck[2 * i + 1] = a1; }
        else { a0 = cv[2 * i] * rr[i >> 1][(2 * i) & 3]; OPQV(a0); a1 = cv[2 * i + 1] * rr[i >> 1][(2 * i + 1) & 3]; OPQV(a1); }
        unsigned pk = cvtpk(a0, a1); OPQV(pk);
        kvf[n][i & 3] = pk;
      }
      if constexpr (u >= 70 && u <= 85 && (u - 70) % 5 == 0) {
        st16n(kvf[(u - 70) / 5], rs, kvo + ((u - 70) / 5) * 1024, kvb);
        *(LAS v4u*)(L + own + ((u - 70) / 5) * 1024) = kvf[(u - 70) / 5];
      }
      if constexpr (u >= 71 && u <= 86 && (u - 71) % 5 == 0) KEEP(kvf[(u - 71) / 5]);
      if constexpr (u >= 76 && u <= 90 && !(u & 1)) { constexpr int i = u - 76; k2 = fmaxf(fmaxf(k2, __builtin_fabsf(ck[i])), __builtin_fabsf(ck[i + 1])); OPQV(k2); }
      if constexpr (u == 91) { kr = __float_as_uint(k2); kr = umax(kr, (unsigned)__builtin_amdgcn_update_dpp(0, (int)kr, 0x121, 0xf, 0xf, true)); kr = umax(kr, (unsigned)__builtin_amdgcn_update_dpp(0, (int)kr, 0x122, 0xf, 0xf, true)); OPQV(kr); }
      if constexpr (u == 92) { kr = umax(kr, (unsigned)__builtin_amdgcn_update_dpp(0, (int)kr, 0x124, 0xf, 0xf, true)); kr = umax(kr, (unsigned)__builtin_amdgcn_update_dpp(0, (int)kr, 0x128, 0xf, 0xf, true)); OPQV(kr); }
      if constexpr (u == 94) {
        const unsigned km0 = (umax(umax(__builtin_amdgcn_readlane(kr, 0), __builtin_amdgcn_readlane(kr, 16)), umax(__builtin_amdgcn_readlane(kr, 32), __builtin_amdgcn_readlane(kr, 48))) >> 16) + 1u;
        k16 = km0 < 0x7fc0u ? km0 : 0x7fc0u;
      }
      if constexpr (u == 95 && !FIRST) asm volatile(".Lmk_last:");
      SB();
    });
    STAMP(FIRST ? 3 : 35);
    *(LAS unsigned*)(L + S_KMAX + (8 + cw) * 4) = k16 << 16;
    const float qs = st.rstd * (0.25f * LOG2E);
#pragma unroll
    for (int i = 0; i < 16; ++i) cq[i] *= qs;
    v4u f0, f1;
    pack_tile(cq, f0, f1);
    *(LAS v4u*)(L + S_Q + cw * 2048 + lane16) = f0; *(LAS v4u*)(L + S_Q + cw * 2048 + 1024 + lane16) = f1;
    ready_set(L, S_DONE, cw, tg, 1u + lay);
    VMWAIT(0);
    set_flag(rs, (FL_KV + (lay * 16 + mem) * 16 + grp) * 128u + cw * 8, tg, k16, lane);
  };
#define WQKV_DMA(F, part) do { if ((part) < 2) { F(rs, lb + R0 + (part) * 16384u + cw * 4096u, OFF_WT + (16 + mem) * 32768u + (part) * 16384u + cw * 4096u, lane16); \
                                                 F(rs, lb + R1 + (part) * 16384u + cw * 4096u, OFF_WT + (32 + mem) * 32768u + (part) * 16384u + cw * 4096u, lane16); } \
    else { F(rs, lb + R2 + cw * 8192u, OFF_WT + mem * 32768u + cw * 8192u, lane16); F(rs, lb + R2 + cw * 8192u + 4096u, OFF_WT + mem * 32768u + cw * 8192u + 4096u, lane16); } } while (0)

#define WO_DMA() do { _Pragma("unroll") for (int i_ = 0; i_ < 4; ++i_) \
    WODMA(rs, lb + R0 + cw * 16384u + i_ * 4096u, OFF_WO + nblk * 65536u + cw * 16384u + i_ * 4096u, lane16); } while (0)
    auto attention = [&](int layer0, const f32x16 cm, auto&& hook, auto&& nohook) {
      ROLES(); LANE_CONSTS();
      const int hd = w >> 2, tb = w < 4 ? w : 7 - w;
      const bool up = (lane & 16) != 0;
      unsigned kaddr = KVS + hd * 1024 + lane16; OPQV(kaddr);
      const unsigned vstep = up ? 0u : 4096u, vbase = up ? S_ONES : KVS + 2048 + (h * 32 + hd * 16 + (lane & 15)) * 16;
      STAMP(4);
      if (w >= 6 && grp > 0) {
        const int p = w - 6;
        const bool have = grp - 2 + p >= 0;
        v2u f = {0u, 0u};
        if (have) f = wait_flags(rs, (FL_KV + (layer * 16 + mem) * 16 + grp - 2 + p) * 128u, tg, 1u, lane, 4);
#pragma unroll
        for (int i = 0; i < 4; ++i)
          dma4(rs, lb + KVS + (4 * p + i) * 4096u, OFF_KV + layer * (4 * MB) + (mem * 64 + 4 * (grp - 2 + p) + i) * 4096u, have ? lane16 : OOB);
        if (lane < 4) *(LAS unsigned*)(L + S_KMAX + (4 * p + lane) * 4) = f.x << 16;
        __builtin_amdgcn_s_setprio(1);
        STAMP(7);
      }
      ready_wait(L, S_DONE, lane, tg, 1u + layer);
      STAMP(5);
      const v4u qf = lds16(L, S_Q + tb * 2048 + hd * 1024 + lane16);
      const unsigned km = *(LAS unsigned*)(L + S_KMAX + (8 + (lane & 3)) * 4);
      unsigned vm = vbase + vstep * (8 + tb);
      v4u kd = lds16(L, kaddr + (8 + tb) * 4096), kn = lds16(L, kaddr + 8 * 4096), v0 = lds16(L, vm), v1 = lds16(L, vm + 1024);
      f32x16 s0 = mfma16(kd, qf, cm); OPQV(s0);
      const unsigned q0 = qf.x, q1 = qf.y, q2_ = qf.z, q3 = qf.w;
      const float q2 = xhalf_add(dot2(q0, q0, dot2(q1, q1, 0.f)) + dot2(q2_, q2_, dot2(q3, q3, 0.f))) * 16.5f;
      const float ko = __uint_as_float(row_umax(km));
      bool ok = all_lanes(q2 * ko * ko <= SAFE2);
      f32x16 o = {};
      if (ok) attn_chain<0, 3, 8, 1>(L, qf, tb, kaddr, vbase + vstep * 8, vstep, s0, v0, v1, kn, o, [&](auto) {});
      STAMP(6);
      f32x16 cmo;
#pragma unroll
      for (int i = 0; i < 16; ++i) cmo[i] = grp == 1 ? NEG_BIG : NEG_BIG - cm[i];
      OPQV(cmo);
      vm = vbase + vstep * tb;
      if (w >= 6) VMWAIT(0);
      STAMP(8);
      BAR();
      STAMP(9);
      const int np = grp == 1 ? 4 : 7 - tb;
      if (tb < 2) __builtin_amdgcn_s_setprio(1); else __builtin_amdgcn_s_setprio(0);
      if (grp > 0) {
        const unsigned kp = *(LAS unsigned*)(L + S_KMAX + (lane & 7) * 4);
        kd = lds16(L, kaddr + tb * 4096); kn = lds16(L, kaddr + 7 * 4096); v0 = lds16(L, vm); v1 = lds16(L, vm + 1024);
        s0 = mfma16(kd, qf, cmo); OPQV(s0);
        const float kq = __uint_as_float(row_umax(kp));
        ok = ok && all_lanes(q2 * kq * kq <= SAFE2);
      }
      if (ok) {
        if (grp > 0) attn_chain<0, 7, 7, -1>(L, qf, np, kaddr, vbase + vstep * 7, vstep, s0, v0, v1, kn, o, hook);
        else nohook();
      } else {
        nohook();
        float m = NEG_BIG;
#pragma unroll
        for (int i = 0; i < 16; ++i) o[i] = 0.f;
        attn_general(L, qf, cm, 8 + tb, 8, tb, kaddr, vbase, vstep, m, o);
        if (grp > 0) attn_general(L, qf, cmo, tb, 8 - np, np, kaddr, vbase, vstep, m, o);
      }
      __builtin_amdgcn_s_setprio(0);
      STAMP(10);
      const float il = __builtin_amdgcn_rcpf(o[8]);
      v4u of;
      of.x = cvtpk(o[0] * il, o[1] * il); of.y = cvtpk(o[2] * il, o[3] * il); of.z = cvtpk(o[4] * il, o[5] * il); of.w = cvtpk(o[6] * il, o[7] * il);
      const int head = 2 * mem + hd, Tq = 4 * grp + tb;
      st16(of, rs, lane16, OFF_O + layer * (2 * MB) + (Tq * 32 + head) * 1024u);
      VMWAIT(0);
      set_flag(rs, (FL_O + (layer * 64 + Tq) * 2 + (head >> 4)) * 128u + (head & 15) * 8, tg, 1u, lane, RO, 256);
      ready_set(L, S_ATT + (w >> 2) * 32, cw, tg, 1u + layer);
      STAMP(11);
    };
  if (w0 < 4) {
   {
    const int layer0 = 0;
    STAMP(31);
    ROLES(); LANE_CONSTS();
    const rsrc_t rx = __builtin_amdgcn_make_buffer_rsrc((void*)a.x, 0, SEQ * DM * 2, 0x00020000);
    v2u ep;
    asm volatile("s_nop 4\n\tbuffer_load_dwordx2 %0, off, %1, 0 sc1" : "=&v"(ep) : "s"(rs) : "memory");
    const unsigned xv0 = (lane >> 3) * 1024 + (((lane & 7) ^ (lane >> 4)) * 16), xv1 = (xv0 ^ 64u) + 7168, xv2 = xv0 + 14336, xv3 = xv1 + 14336;
    auto xround = [&](int rho) {
      asm volatile("s_nop 4\n\ts_mov_b32 m0, %0\n\ts_nop 0\n\tbuffer_load_dwordx4 %1, %5, %6 offen lds\n\tbuffer_load_dwordx4 %2, %5, %6 offen offset:1024 lds\n\t"
                   "buffer_load_dwordx4 %3, %5, %6 offen offset:2048 lds\n\tbuffer_load_dwordx4 %4, %5, %6 offen offset:3072 lds"
                   :: "s"(lb + KVS + w * 12288u + (rho % 3) * 4096u), "v"(xv0), "v"(xv1), "v"(xv2), "v"(xv3), "s"(rx), "s"(tb3 * 32768u + rho * 128u) : "memory");
    };
    __builtin_amdgcn_s_sleep(XDELAY);
    const int un = grp * 16 + mem, g = lane >> 4, q = (lane & 15) >> 2, pp = lane & 3;
    const int uty = un < 112 ? 0 : (un < 224 ? 1 : 2);
    const int pcb = uty == 0 ? un >> 2 : (uty == 1 ? (un - 112) / 14 : (un - 224) >> 2), pkq = uty == 1 ? (un - 112) % 14 : un & 3, pks = 8 * pkq + 2 * cw;
    auto prep_load = [&] {
      const unsigned pld = uty == 0 ? FF * 2 : 1024;
      const rsrc_t ra = __builtin_amdgcn_make_buffer_rsrc((void*)(uty == 0 ? a.wg : (uty == 1 ? a.wd : a.wo)), 0, uty == 2 ? DM * 1024 : FF * 1024, 0x00020000);
      const rsrc_t rb = __builtin_amdgcn_make_buffer_rsrc((void*)a.wu, 0, FF * 1024, 0x00020000);
      const unsigned pvo = (lane >> 3) * pld + (lane & 7) * 16, pso = 16 * pks * pld + pcb * 128;
#pragma unroll
      for (int i = 0; i < 4; ++i) dma1p(ra, lb + R2 + cw * 8192u + i * 1024u, pso + 8 * i * pld, pvo);
      if (uty == 0) {
#pragma unroll
        for (int i = 0; i < 4; ++i) dma1p(rb, lb + R2 + cw * 8192u + 4096u + i * 1024u, pso + 8 * i * pld, pvo);
      }
    };
    xround(0);
    prep_load();
    xround(1); xround(2);
    unsigned xa[4];
#pragma unroll
    for (int i = 0; i < 4; ++i) { xa[i] = KVS + w * 12288 + (r >> 3) * 1024 + (r & 7) * 128 + (((2 * i) ^ ((r >> 1) & 7)) << 4) + h * 8; OPQV(xa[i]); }
    sfor<0, 8>([&](auto I) { CAUSAL2(cmc, 2 * I); });
    v4u x0[32];
    SB();
    sfor<0, 8>([&](auto RHO) {
      constexpr int rho = RHO;
      if constexpr (rho == 0) { if (uty == 0) VMWAIT(16); else VMWAIT(12); asm volatile("" : "+v"(ep) :: "memory"); }
      else if constexpr (rho < 6) VMWAIT(8);
      else if constexpr (rho == 6) VMWAIT(4);
      else VMWAIT(0);
      v2u t[8];
#pragma unroll
      for (int i = 0; i < 4; ++i) { t[2 * i] = *(LAS v2u*)(L + xa[i] + (rho % 3) * 4096); t[2 * i + 1] = *(LAS v2u*)(L + (xa[i] ^ 16u) + (rho % 3) * 4096); }
      asm volatile("s_waitcnt lgkmcnt(0)" : "+v"(t[0]), "+v"(t[1]), "+v"(t[2]), "+v"(t[3]), "+v"(t[4]), "+v"(t[5]), "+v"(t[6]), "+v"(t[7]) :: "memory");
      if constexpr (rho + 3 < 8) xround(rho + 3);
#pragma unroll
      for (int i = 0; i < 4; ++i) { const unsigned a0 = t[2 * i].x, a1 = t[2 * i].y, a2 = t[2 * i + 1].x, a3 = t[2 * i + 1].y; x0[4 * rho + i] = v4u{a0, a1, a2, a3}; }
      SB();
    });
    MAKE_TAG(ep);
    STAMP(0);
    const unsigned t32 = (tg.lo >> 16) | (tg.hi << 16);
    v2u sgr = {0u, 0u};
    __builtin_amdgcn_s_sleep(SDELAY);
#pragma nounroll
    for (unsigned spins = 0; spins < SPIN_CAP; ++spins) {
      sgr = ld8(rs, r * 16 + h * 8, OFF_ST0 + tb3 * 512u);
      if (all_lanes(sgr.y == t32)) break;
      asm volatile("s_sleep 4" ::: "memory");
    }
    const unsigned sv = sgr.x;
    const auto svs = __builtin_amdgcn_permlane32_swap(sv, sv, false, false);
    const Stats st = finish_stats(__uint_as_float(svs[0]), __uint_as_float(svs[1]), h);
    {
      const unsigned ta = R2 + cw * 8192 + (4 * (g >> 1) + q) * 128 + pp * 8;
      if (uty == 0) {
        v4u f[8];
#pragma unroll
        for (int i = 0; i < 8; ++i) f[i] = trfrag(L, ta + (g & 1) * 4096 + (i >> 2) * 2048 + (i & 3) * 32, 1024);
#pragma unroll
        for (int i = 0; i < 8; ++i) st16(f[i], rs, lane16 + (i >> 2) * 1024, OFF_WGU + ((4 * pcb + (i & 3)) * 32 + pks) * 1024u);
#pragma unroll
        for (int tl = 0; tl < 4; ++tl) *(LAS float*)(L + S_PART + ((cw * 4 + tl) * 32 + r) * 4) = xhalf_add(sum8(f[tl], sum8(f[4 + tl], 0.f)));
      } else {
        v4u f[4];
#pragma unroll
        for (int i = 0; i < 4; ++i) f[i] = trfrag(L, ta + (g & 1) * 32 + (i >> 1) * 2048 + (i & 1) * 64, 1024);
#pragma unroll
        for (int i = 0; i < 4; ++i) {
          const int ks = pks + (i >> 1), mm = ks / 7, cc = ks % 7;
          st16(f[i], rs, lane16, uty == 1 ? OFF_WD + (pcb * 224 + cc * 32 + (mm >> 3) * 16 + (mm & 7) * 2 + (i & 1)) * 1024u : OFF_WO + ((pcb * 2 + (i & 1)) * 32 + ks) * 1024u);
        }
      }
    }
    if (w == 3) set_flag(rs, FL_SEEN * 128u + (grp * 16 + mem) * 8u, tg, 1u, lane);
    STAMP(1);
    BAR();
    STAMP(2);
    qkv(std::true_type{}, x0, st, 0);
   }
    for (int layer0 = 0;; ++layer0) {
      attention(layer0, cmc, [&](auto) {}, [&] {});
      {
        ROLES(); LANE_CONSTS();
        const unsigned xfo = (tb2 * 32 + 4 * nblk + 2 * kh) * 1024u;
        v4u r0, r1;
        if (layer) { r0 = ld16(rs, lane16, OFF_X2 + xfo); r1 = ld16(rs, lane16 + 1024, OFF_X2 + xfo); }
        else {
          const rsrc_t rx = __builtin_amdgcn_make_buffer_rsrc((void*)kargs()->x, 0, SEQ * DM * 2, 0x00020000);
          const unsigned vo = r * 1024 + h * 8, so = tb2 * 32768u + nblk * 128u + kh * 64u;
          const v2u t0 = ld8p(rx, vo, so), t1 = ld8p(rx, vo + 16, so), t2 = ld8p(rx, vo + 32, so), t3 = ld8p(rx, vo + 48, so);
          r0 = v4u{t0.x, t0.y, t1.x, t1.y}; r1 = v4u{t2.x, t2.y, t3.x, t3.y};
        }
        unsigned wa0 = R0 + kh * (16384 + 32768) + lane16, wa1 = wa0 ^ 32768u; OPQV(wa0); OPQV(wa1);
        auto wread = [&](auto U) -> v4u { constexpr int u = U; return lds16(L, ((u & 1) ? wa1 : wa0) + (u >> 1) * 1024); };
        v4u ar[6];
        f32x16 c0 = unpack_tile(r0, r1), c1 = {};
        OPQV(c0);
        __builtin_amdgcn_s_sleep(ODELAY);
        wait_flags(rs, (FL_O + (nblk % RO) * 256 + (layer * 64 + tb2) * 2 + kh) * 128u, tg, 1u, lane, 16);
        STAMP(13);
        const unsigned ob = OFF_O + layer * (2 * MB) + (tb2 * 32 + 16 * kh) * 1024u;
        auto oload = [&](auto I) -> v4u { constexpr int i = I; return ld16(rs, lane16 + (i & 3) * 1024, ob + (i >> 2) * 4096u); };
        v4u ofr[16];
        sfor<0, D2>([&](auto I) { constexpr int i = I; ofr[i] = oload(I); });
        ready_wait(L, S_RDY, lane, tg, 4u + layer);
        STAMP(12);
        RING_FILL(wread);
        SB();
        sfor<0, 32>([&](auto I) {
          constexpr int u = I;
          if constexpr (u & 1) { c1 = mfma16(ar[rslot(u)], ofr[u >> 1], c1); OPQV(c1); } else { c0 = mfma16(ar[rslot(u)], ofr[u >> 1], c0); OPQV(c0); }
          if constexpr (rnext(u) < 32) ar[rslot(u)] = wread(IC(rnext(u)));
          if constexpr ((u & 1) && (u >> 1) + D2 < 16) ofr[(u >> 1) + D2] = oload(IC((u >> 1) + D2));
          SB();
        });
        unsigned sown = R4S + cw * 4096 + lane16; OPQV(sown);
#pragma unroll
        for (int g = 0; g < 4; ++g) *(LAS f32x4*)(L + sown + g * 1024) = f32x4{c1[4 * g], c1[4 * g + 1], c1[4 * g + 2], c1[4 * g + 3]};
        OPQV(c0);
        STAMP(14);
        BAR();
        STAMP(15);
#pragma unroll
        for (int g = 0; g < 4; ++g) { const f32x4 t = *(LAS f32x4*)(L + (sown ^ 4096u) + g * 1024); c0[4 * g] += t[0]; c0[4 * g + 1] += t[1]; c0[4 * g + 2] += t[2]; c0[4 * g + 3] += t[3]; }
        v4u y0, y1;
        pack_tile(c0, y0, y1);
        const unsigned xo = OFF_X1 + layer * (2 * MB) + xfo;
        st16(y0, rs, lane16, xo); st16(y1, rs, lane16 + 1024, xo);
        st8(tile_stats(c0), rs, h ? OOB : r * 8, OFF_ST + ((layer * 128 + tb2) * 16 + 2 * nblk + kh) * 256u);
        VMWAIT(0);
        set_flag(rs, (FL_X + layer * 128 + tb2) * 128u + (2 * nblk + kh) * 8, tg, 1u, lane, RX, 192);
      }
      {
        ROLES(); LANE_CONSTS();
        STAMP(16);
        v2u fpa, fpb;
        v4u ar[6];
        unsigned lhi = lane16 + R2; OPQV(lhi);
        unsigned lp0 = lane16 + kh * (16384 + 1024), lp1 = lp0 ^ 1024u;
        unsigned lp0h = lp0 + R2, lp1h = lp1 + R2;
        OPQV(lp0); OPQV(lp1); OPQV(lp0h); OPQV(lp1h);
        auto aread = [&](auto G) -> v4u {
          constexpr int g = G;
          if constexpr (g < 224) {
            constexpr int T = g < 96 ? g % 3 : 3 + (g - 96) / 32, s = g < 96 ? g / 3 : (g - 96) % 32, sl = (T + 2) & 3;
            return lds16(L, (sl & 1) * 32768 + s * 1024 + (sl < 2 ? lane16 : lhi));
          } else {
            constexpr int u = g - 224, c = u / 16, i = (u % 16) >> 1, sl = (c + 1) & 3;
            return lds16(L, (sl & 1) * 32768 + i * 2048 + (sl < 2 ? ((u & 1) ? lp1 : lp0) : ((u & 1) ? lp1h : lp0h)));
          }
        };
        unsigned aga = R4V + r * 8; OPQV(aga);
        __builtin_amdgcn_s_sleep(PDELAY);
        wait_flags(rs, (FL_X + (mem % RX) * 192 + layer * 128 + tb3) * 128u, tg, 1u, lane, 16);
        STAMP(17);
        v2u sp[8];
        const unsigned xb = OFF_X1 + layer * (2 * MB) + tb3 * 32768u;
        auto xload = [&](auto I) -> v4u { constexpr int i = I; return ld16(rs, lane16 + (i & 3) * 1024, xb + (i >> 2) * 4096u); };
        v4u x[32];
        sfor<0, D3>([&](auto I) { constexpr int i = I; x[i] = xload(I); });
        ready_wait(L, S_RDY, lane, tg, 1u + layer);
        STAMP(24);
        RING_FILL(aread);
        SB();
        const unsigned hb = OFF_H + layer * (7 * MB) + (tb3 * 112 + mem * 7) * 1024u;
        const unsigned fh_mine = (FL_H + (layer * 64 + tb3) * 2 + (mem >> 3)) * 128u + (mem & 7) * 8;
        const unsigned fh_poll = (FL_H + (nblk % RO) * 256 + (layer * 64 + tb2) * 2 + kh) * 128u;
        const unsigned xfo = (tb2 * 32 + 4 * nblk + 2 * kh) * 1024u;
        const unsigned hb4 = OFF_H + layer * (7 * MB) + (tb2 * 112 + 56 * kh) * 1024u;
        Stats st;
        float cg, cu;
        f32x16 acc[4] = {};
        f32x16 c0 = {}, c1;
        v2u agn;
        v4u hr[D4], r0, r1;
        float ee[2], hv[2]; v4u hf;
        auto epi = [&](auto N_, auto K_) {
          constexpr int p = N_ / 8, e = N_ % 8, k = K_;
          if constexpr (k == 0) { ee[e & 1] = ex2(acc[p & 3][e] * cg); OPQV(ee[e & 1]); }
          if constexpr (k == 1) { ee[e & 1] = __builtin_amdgcn_rcpf(__builtin_fmaf(ee[e & 1], cu, cu)); OPQV(ee[e & 1]); }
          if constexpr (k == 2) { hv[e & 1] = ee[e & 1] * acc[p & 3][8 + e]; OPQV(hv[e & 1]); }
          if constexpr (k == 3 && (e & 1)) { unsigned pk = cvtpk(hv[0], hv[1]); OPQV(pk); hf[e >> 1] = pk; }
          if constexpr (k == 4 && e == 7) st16n(hf, rs, lane16 + (p & 3) * 1024, hb + (p >> 2) * 4096);
          if constexpr (k == 5 && e == 7) KEEP(hf);
        };
        SB();
        sfor<0, 336>([&](auto I) {
          constexpr int g = I;
          constexpr bool P4 = g >= 224;
          constexpr int T = g < 96 ? g % 3 : (P4 ? 7 : 3 + (g - 96) / 32), s = g < 96 ? g / 3 : (g - 96) % 32, u = g - 224;
          constexpr int left = g < 96 ? 96 - g : (P4 ? 16 - u % 16 : 32 - s);
          if constexpr (left == 4 && g < 328) {
            BAR();
            RING_LATE(aread, g + 4);
          }
          if constexpr (g == 96) STAMP(18);
          if constexpr (g == 128) STAMP(20);
          if constexpr (g == 160) STAMP(19);
          if constexpr (g == 192) STAMP(21);
          if constexpr (g == 224) STAMP(22);
          if constexpr (g == 240) STAMP(25);
          if constexpr (g == 272) STAMP(26);
          if constexpr (g == 304) STAMP(27);
          if constexpr (sched::poll_a(g)) fpa = poll_ld(rs, lane < 8 ? lane * 8 : OOB, fh_poll);
          if constexpr (sched::poll_b(g)) fpb = poll_ld(rs, lane < 8 ? lane * 8 : OOB, fh_poll);
          if constexpr (!P4) {
            if constexpr (g >= 96 && s == 0) { const f32x16 z = {}; acc[T & 3] = mfma8(agn, st.aug, z); }
            acc[T & 3] = mfma16(ar[rslot(g)], x[s], acc[T & 3]); OPQV(acc[T & 3]);
          } else if constexpr (u == 1) { const f32x16 z = {}; c1 = mfma16(ar[rslot(g)], hr[0], z); OPQV(c1); }
          else if constexpr (u & 1) { c1 = mfma16(ar[rslot(g)], hr[(u >> 1) % D4], c1); OPQV(c1); }
          else { c0 = mfma16(ar[rslot(g)], hr[(u >> 1) % D4], c0); OPQV(c0); }
          if constexpr ((left > 6 || left <= 4) && rnext(g) < 336) ar[rslot(g)] = aread(IC(rnext(g)));
          if constexpr (g < 96 && T == 0 && s + D3 < 32) x[s + D3] = xload(IC(s + D3));
          if constexpr (g < 24 && T == 1) sp[s] = ld8(rs, h * 2048 + r * 8 + s * 256, OFF_ST + (layer * 128 + tb3) * 4096u);
          if constexpr (g == 50) { st = make_stats(sp, h); cg = st.rstd * -LOG2E; cu = __builtin_amdgcn_rcpf(st.rstd); OPQV(cg); OPQV(cu); OPQV(st.aug); }
          if constexpr (g == 52 || g == 56 || g == 60) agn = *(LAS v2u*)(L + aga + ((g - 52) / 4) * 256);
          if constexpr (g == 55 || g == 59 || g == 63) { constexpr int t = (g - 55) / 4; acc[t] = mfma8(agn, st.aug, acc[t]); OPQV(acc[t]); }
          if constexpr (left == 8 && g < 200) agn = *(LAS v2u*)(L + aga + (g < 96 ? 3 : T + 1) * 256);
          if constexpr (sched::flag(g) > 0) {
            asm volatile("s_waitcnt vmcnt(%0)" :: "i"(sched::younger(sched::store_at(sched::flag(g) - 1), g)) : "memory");
            set_flag(rs, fh_mine, tg, sched::flag(g), lane, RO, 256);
          }
          if constexpr (sched::check(g) > 0) {
            if (!flags_ok(sched::check(g) == 2 ? fpa : fpb, tg, sched::check(g), lane, 8)) wait_flags(rs, fh_poll, tg, sched::check(g), lane, 8);
          }
          if constexpr (g >= 96) sfor<0, 56>([&](auto N) { constexpr int k = g - sched::epi_at(N); if constexpr (k >= 0 && k < 6) epi(N, IC(k)); });
          if constexpr (g == sched::RES) { const unsigned xr1 = OFF_X1 + layer * (2 * MB) + xfo; r0 = ld16(rs, lane16, xr1); r1 = ld16(rs, lane16 + 1024, xr1); }
          if constexpr (sched::hload(g) >= 0) {
            constexpr int q = sched::hload(g), c = q >> 3;
            hr[q % D4] = ld16(rs, lane16 + (c & 3) * 1024, hb4 + (c >> 2) * 4096u + (q & 7) * 7168u);
          }
          if constexpr (g >= 216 && g < 224) {
            constexpr int i = g - 216;
            const unsigned rv = i < 4 ? r0[i & 3] : r1[i & 3];
            float a0 = bflo(rv), a1 = bfhi(rv); OPQV(a0); OPQV(a1); c0[2 * i] = a0; c0[2 * i + 1] = a1;
          }
          SB();
        });
        unsigned sown = R4S + cw * 4096 + lane16; OPQV(sown);
#pragma unroll
        for (int g = 0; g < 4; ++g) *(LAS f32x4*)(L + sown + g * 1024) = f32x4{c1[4 * g], c1[4 * g + 1], c1[4 * g + 2], c1[4 * g + 3]};
        OPQV(c0);
        STAMP(28);
        BAR();
        STAMP(29);
#pragma unroll
        for (int g = 0; g < 4; ++g) { const f32x4 t = *(LAS f32x4*)(L + (sown ^ 4096u) + g * 1024); c0[4 * g] += t[0]; c0[4 * g + 1] += t[1]; c0[4 * g + 2] += t[2]; c0[4 * g + 3] += t[3]; }
        v4u y0, y1;
        pack_tile(c0, y0, y1);
        if (layer == 0) {
          st16(y0, rs, lane16, OFF_X2 + xfo); st16(y1, rs, lane16 + 1024, OFF_X2 + xfo);
          st8(tile_stats(c0), rs, h ? OOB : r * 8, OFF_ST + ((64 + tb2) * 16 + 2 * nblk + kh) * 256u);
          VMWAIT(0);
          set_flag(rs, (FL_X + 64 + tb2) * 128u + (2 * nblk + kh) * 8, tg, 1u, lane, RX, 192);
        } else {
          const rsrc_t ro = __builtin_amdgcn_make_buffer_rsrc((void*)kargs()->out, 0, SEQ * DM * 2, 0x00020000);
          const unsigned a0 = y0.x, a1 = y0.y, a2 = y0.z, a3 = y0.w, b0 = y1.x, b1 = y1.y, b2 = y1.z, b3 = y1.w;
          const auto p0 = __builtin_amdgcn_permlane32_swap(a0, a2, false, false), p1 = __builtin_amdgcn_permlane32_swap(a1, a3, false, false);
          const auto p2 = __builtin_amdgcn_permlane32_swap(b0, b2, false, false), p3 = __builtin_amdgcn_permlane32_swap(b1, b3, false, false);
          const unsigned vo = r * 1024 + h * 16, so = tb2 * 32768u + nblk * 128u + kh * 64u;
          const v4u z0 = {p0[0], p1[0], p0[1], p1[1]}, z1 = {p2[0], p3[0], p2[1], p3[1]};
          __builtin_amdgcn_raw_buffer_store_b128(z0, ro, vo, so, OUTAUX); __builtin_amdgcn_raw_buffer_store_b128(z1, ro, vo + 32, so, OUTAUX);
          asm volatile("s_nop 1" :: "v"(z0), "v"(z1) : "memory");
        }
        STAMP(30);
      }
      if (layer0 == 1) break;
      STAMP(32);
      { v4u xr[32]; Stats st0; st0.rstd = 0.f; st0.mean = 0.f; st0.aug = v2u{0u, 0u}; qkv(std::false_type{}, xr, st0, 0); }
    }
  } else {
   {
    const int layer0 = 0;
    STAMP(31);
    ROLES(); LANE_CONSTS();
    const int un = grp * 16 + mem, g = lane >> 4, q = (lane & 15) >> 2, pp = lane & 3;
    const rsrc_t rx = __builtin_amdgcn_make_buffer_rsrc((void*)a.x, 0, SEQ * DM * 2, 0x00020000);
    const int ucb = un >> 5, uk = un & 31;
    const bool uw = cw == 0;
    if (uw) {
      const rsrc_t rw = __builtin_amdgcn_make_buffer_rsrc((void*)a.wqkv, 0, DM * 3072, 0x00020000);
      const unsigned uvo = (lane >> 3) * 3072 + (lane & 7) * 16, uso = 16 * uk * 3072 + ucb * 128;
#pragma unroll
      for (int i = 0; i < 6; ++i) dma1p(rw, lb + S_Q + i * 1024u, uso + (i >> 1) * 1024u + (i & 1) * 8u * 3072u, uvo);
    }
    if (cw == 3) {
      unsigned long long pc; unsigned len;
      asm volatile("s_getpc_b64 %0\n.Lmk_here:\n\ts_mov_b32 %1, .Lmk_last-.Lmk_here" : "=s"(pc), "=s"(len));
      const rsrc_t rc = __builtin_amdgcn_make_buffer_rsrc((void*)(pc & ~127ull), 0, len & ~15u, 0x00020000);
      dma1p(rc, lb + S_DUMMY, (blockIdx.x >> 3) * 2048u, lane16); dma1p(rc, lb + S_DUMMY + 1024u, (blockIdx.x >> 3) * 2048u + 1024u, lane16);
    }
    const unsigned token = 8 * un + 2 * cw;
    v2u ep; v4u row0, row1;
    asm volatile("s_nop 4\n\tbuffer_load_dwordx2 %0, off, %3, 0 sc1\n\tbuffer_load_dwordx4 %1, %4, %5, %6 offen\n\tbuffer_load_dwordx4 %2, %4, %5, %6 offen offset:1024"
                 : "=&v"(ep), "=&v"(row0), "=&v"(row1) : "s"(rs), "v"(lane16), "s"(rx), "s"(token * 1024u) : "memory");
    asm volatile("s_waitcnt vmcnt(0)" : "+v"(ep), "+v"(row0), "+v"(row1) :: "memory");
    MAKE_TAG(ep);
    STAMP(12);
    if (uw) {
      const unsigned ta = S_Q + (4 * (g >> 1) + q) * 128 + (g & 1) * 32 + pp * 8;
      v4u f[6];
#pragma unroll
      for (int i = 0; i < 6; ++i) f[i] = trfrag(L, ta + (i >> 1) * 2048 + (i & 1) * 64, 1024);
#pragma unroll
      for (int i = 0; i < 6; ++i) st16(f[i], rs, lane16, OFF_WT + (16 * (i >> 1) + 2 * ucb + (i & 1)) * 32768u + uk * 1024u);
#pragma unroll
      for (int i = 0; i < 6; ++i)
        __builtin_amdgcn_raw_buffer_store_b32(__float_as_uint(xhalf_add(sum8(f[i], 0.f))), rs, h ? OOB : r * 4,
                                              OFF_CSP + (16 * (i >> 1) + 2 * ucb + (i & 1)) * 4096u + uk * 128u, 16);
    }
    {
      const unsigned t32 = (tg.lo >> 16) | (tg.hi << 16);
      const unsigned a0 = row0.x, a1 = row0.y, a2 = row0.z, a3 = row0.w, b0 = row1.x, b1 = row1.y, b2 = row1.z, b3 = row1.w;
      const float s0 = wave_add(sum8(row0, 0.f)), s1 = wave_add(dot2(a0, a0, dot2(a1, a1, dot2(a2, a2, dot2(a3, a3, 0.f)))));
      const float s2 = wave_add(sum8(row1, 0.f)), s3 = wave_add(dot2(b0, b0, dot2(b1, b1, dot2(b2, b2, dot2(b3, b3, 0.f)))));
      const float v = (lane & 2) ? ((lane & 1) ? s3 : s2) : ((lane & 1) ? s1 : s0);
      st8(v2u{__float_as_uint(v), t32}, rs, lane < 4 ? lane * 8 : OOB, OFF_ST0 + token * 16u);
    }
    if (uw) { VMWAIT(1); set_flag(rs, (FL_Q + ucb * 2) * 128u + uk * 8u, tg, 1u, lane, RQ, 16); }
    STAMP(13);
    STAMP(14);
    if (cw == 3) { wait_flags(rs, (FL_Q + (grp % RQ) * 16 + (mem >> 1) * 2) * 128u, tg, 1u, lane, 32); relay_set(L, tg, 1u); }
    relay_wait(L, tg, 1u);
    STAMP(0);
    WQKV_DMA(dma4, 0); WQKV_DMA(dma4, 1);
    VMWAIT(8);
    STAMP(1);
    BAR();
    STAMP(2);
    const int uty = un < 112 ? 0 : (un < 224 ? 1 : 2);
    const int pcb = uty == 0 ? un >> 2 : (uty == 1 ? (un - 112) / 14 : (un - 224) >> 2), pkq = uty == 1 ? (un - 112) % 14 : un & 3, pks = 0; (void)pks;
    if (cw == 3) {
      if (uty == 0) {
        f32x2 cs = {0.f, 0.f};
#pragma unroll
        for (int i = 0; i < 4; ++i) { cs[0] += *(LAS float*)(L + S_PART + (i * 128 + lane) * 4); cs[1] += *(LAS float*)(L + S_PART + (i * 128 + 64 + lane) * 4); }
        __builtin_amdgcn_raw_buffer_store_b32(__float_as_uint(cs[0]), rs, lane16, OFF_AUG + pcb * 2048u + pkq * 4u, 16);
        __builtin_amdgcn_raw_buffer_store_b32(__float_as_uint(cs[1]), rs, lane16 + 1024, OFF_AUG + pcb * 2048u + pkq * 4u, 16);
      }
    }
    unsigned cp[16];
    AUGQ_LOAD(cp);
    AUGQ_PUT(cp, S_AUGQ);
    VMWAIT(0);
    WQKV_DMA(dma4, 2);
    BAR();
    if (cw == 3) set_flag(rs, (FL_P + (uty == 0 ? 0 : (uty == 1 ? 28 : 36)) + pcb) * 128u + pkq * 8u, tg, 1u, lane);
    VMWAIT(0);
    BAR();
    if (cw < 2) {
      const unsigned c0 = (7 * mem) >> 2, nw = 4 + (cw ? 14 : 4 * (((7 * mem + 6) >> 2) - c0 + 1)), l4 = lane - 4;
      wait_words(rs, lane < 4 ? (36 + nblk) * 128 + lane * 8 : ((unsigned)lane < nw ? (cw ? (28 + nblk) * 128 + l4 * 8 : (c0 + (l4 >> 2)) * 128 + (l4 & 3) * 8) : OOB),
                 FL_P * 128u, tg, 1u, (unsigned)lane < nw);
#pragma unroll
      for (int i = 0; i < 8; ++i) dma4(rs, lb + R0 + cw * 32768u + i * 4096u, OFF_WO + nblk * 65536u + cw * 32768u + i * 4096u, lane16);
    }
   }
    for (int layer0 = 0;; ++layer0) {
      ROLES(); LANE_CONSTS();
      const unsigned lw = cw;
      const unsigned gu = OFF_WGU + mem * 7 * 32768u, wd = OFF_WD + nblk * 7 * 32768u;
      unsigned wlo = lw * 8192u + lane16, whi = wlo + R2; OPQV(wlo); OPQV(whi);
      v4u sa[4][8];
      auto stage = [&](auto B, unsigned src, auto PACED) {
        constexpr int b = B;
        sfor<0, 8>([&](auto I) {
          constexpr int i = I;
          sa[b][i] = ld16(rs, lane16 + (i & 3) * 1024, src + lw * 8192u + (i >> 2) * 4096u);
          if constexpr (PACED) asm volatile(DMA_W ::: "memory"); else SB();
        });
      };
      auto put = [&](auto B, unsigned base) { constexpr int b = B; sfor<0, 8>([&](auto I) { constexpr int i = I; *(LAS v4u*)(L + base + i * 1024) = sa[b][i]; if constexpr (PUTGAP > 0) { SB(); asm volatile("s_nop %0" :: "i"(PUTGAP)); SB(); } }); };
      auto sload = [&](auto N) { constexpr int n = N; if constexpr (n < 24) sa[n >> 3][n & 7] = ld16(rs, lane16 + (n & 3) * 1024, (n < 16 ? gu + (2 + (n >> 3)) * 32768u : wd + 3 * 32768u) + lw * 8192u + ((n >> 2) & 1) * 4096u); };
      f32x16 cml;
      sfor<0, 8>([&](auto I) { CAUSAL2(cml, 2 * I); });
      attention(layer0, cml, [&](auto K) { constexpr int k = K; if constexpr (k < 5) sfor<5 * k, 5 * k + 5>(sload); }, [&] { sfor<0, 24>(sload); });
      dma4_paced(rs, lb + R2 + lw * 8192u, gu + lw * 8192u, lane16);
      dma4_paced(rs, lb + R2 + lw * 8192u + 4096u, gu + lw * 8192u + 4096u, lane16);
      ready_wait(L, S_ATT, lane, tg, 1u + layer); ready_wait(L, S_ATT + 32, lane, tg, 1u + layer);
      ready_set(L, S_RDY, lw, tg, 4u + layer);
      stage(IC(3), wd + 4 * 32768u, IC(1));
      {
        const v4u p0 = ld16(rs, r * 16 + lw * 1024, OFF_AUG + mem * 7 * 512u), p1 = ld16(rs, lw < 3 ? r * 16 + lw * 1024 + 512 : OOB, OFF_AUG + mem * 7 * 512u);
        dma4_paced(rs, lb + R3 + lw * 8192u, gu + 32768u + lw * 8192u, lane16);
        dma4_paced(rs, lb + R3 + lw * 8192u + 4096u, gu + 32768u + lw * 8192u + 4096u, lane16);
        *(LAS v2u*)(L + R4V + lw * 512 + r * 8) = make_aug(p0);
        *(LAS v2u*)(L + R4V + lw * 512 + 256 + r * 8) = make_aug(p1);
      }
      STAMP(14); BAR();
      put(IC(0), wlo); put(IC(1), wlo + R1);
      VMWAIT(0);
      ready_set(L, S_RDY, lw, tg, 1u + layer);
      STAMP(17);
      stage(IC(0), wd + 5 * 32768u, IC(1));
      stage(IC(1), wd + 6 * 32768u, IC(1));
      STAMP(18); BAR();
      dma_tile(rs, lb + R2, gu + 4 * 32768u, lw, lane16);
      dma_tile(rs, lb + R3, gu + 5 * 32768u, lw, lane16);
      VMWAIT(8);
      dma4(rs, lb + R0 + lw * 8192u, gu + 6 * 32768u + lw * 8192u, lane16);
      STAMP(20); BAR();
      dma4(rs, lb + R0 + lw * 8192u + 4096u, gu + 6 * 32768u + lw * 8192u + 4096u, lane16);
      dma_tile(rs, lb + R1, wd + 0 * 32768u, lw, lane16);
      VMWAIT(16); STAMP(19); BAR();
      dma_tile(rs, lb + R2, wd + 1 * 32768u, lw, lane16);
      VMWAIT(16); STAMP(21); BAR();
      dma_tile(rs, lb + R3, wd + 2 * 32768u, lw, lane16);
      VMWAIT(16); STAMP(22); BAR();
      put(IC(2), wlo);
      VMWAIT(8); STAMP(25); BAR();
      const unsigned fh = (FL_H + (nblk % RO) * 256 + (layer * 64 + tb2) * 2 + kh) * 128u, fo = lane < 8 ? lane * 8 : OOB;
      put(IC(3), wlo + R1);
      const v2u f5 = poll_ld(rs, fo, fh);
      VMWAIT(1);
      BAR();
      const v2u f6 = poll_ld(rs, fo, fh);
      put(IC(0), whi);
      if (!flags_ok(f5, tg, 5u, lane, 8)) wait_flags(rs, fh, tg, 5u, lane, 8);
      STAMP(26); BAR();
      const v2u f7 = poll_ld(rs, fo, fh);
      put(IC(1), whi + 32768u);
      if (!flags_ok(f6, tg, 6u, lane, 8)) wait_flags(rs, fh, tg, 6u, lane, 8);
      BAR();
      if (!flags_ok(f7, tg, 7u, lane, 8)) wait_flags(rs, fh, tg, 7u, lane, 8);
      STAMP(27); BAR();
      BAR();
      STAMP(28); BAR();
      if (layer0 == 1) {
        if ((grp | mem | cw) == 0) {
#pragma unroll
          for (int i = 0; i < 4; ++i) wait_flags(rs, FL_SEEN * 128u + i * 512u, tg, 1u, lane, 64);
          set_flag(rs, FL_EPOCH * 128u, tg, 0u, lane);
        }
        break;
      }
      WQKV_DMA(dma4_paced, 0);
      unsigned cp[16];
      AUGQ_LOAD(cp);
      WQKV_DMA(dma4_paced, 1);
      ready_set(L, S_RDY, lw, tg, 3u);
      AUGQ_PUT(cp, S_AUGQ1);
      WQKV_DMA(W2DMA, 2);
      VMWAIT(8);
      STAMP(34); BAR();
      VMWAIT(0);
      BAR();
      WO_DMA();
    }
  }
  {
    unsigned lo = 0xffffffffu;
    for (int i = 0; i < 4; ++i) { const unsigned v = __builtin_amdgcn_raw_buffer_load_b32(rs, (threadIdx.x & 63) * 4 + i * 256, 10 * MB + 768 * KB, 16); const unsigned d = (unsigned)rts - v; lo = (int)d > (int)(lo == 0xffffffffu ? 0 : lo) || lo == 0xffffffffu ? d : lo; }
    for (int m = 32; m >= 1; m >>= 1) { const unsigned o = __shfl_xor(lo, m); lo = (int)o > (int)lo ? o : lo; }
    const unsigned back = __builtin_amdgcn_readfirstlane(lo);
    while (__builtin_amdgcn_s_memrealtime() - rts + ((int)back > 0 && back < 3000 ? back : 0) < SPINTO) __builtin_amdgcn_s_sleep(2);
  }
  STAMP_OUT();
}

extern "C" void kernel_launch(void* const* d_in, const int* in_sizes, int n_in, void* d_out, int out_size, void* d_ws, size_t ws_size,
                              hipStream_t stream) {
  Args a;
  a.ws = (unsigned char*)d_ws; a.out = (u16*)d_out;
  a.x = (const u16*)d_in[0]; a.wqkv = (const u16*)d_in[1]; a.wo = (const u16*)d_in[2];
  a.wg = (const u16*)d_in[4]; a.wu = (const u16*)d_in[5]; a.wd = (const u16*)d_in[6];
  (void)hipFuncSetAttribute((const void*)main_k, hipFuncAttributeMaxDynamicSharedMemorySize, LDS_BYTES);
  main_k<<<256, 512, LDS_BYTES, stream>>>(a);
}
```

```cpp
#include <hip/hip_runtime.h>
#include <stdint.h>
#include <type_traits>

typedef unsigned short u16;
typedef float f32x16 __attribute__((ext_vector_type(16)));
typedef float f32x4 __attribute__((ext_vector_type(4)));
typedef float f32x2 __attribute__((ext_vector_type(2)));
typedef __bf16 bf16x8 __attribute__((ext_vector_type(8)));
typedef __bf16 bf16x2 __attribute__((ext_vector_type(2)));
typedef short s16x4 __attribute__((ext_vector_type(4)));
typedef unsigned v4u __attribute__((ext_vector_type(4)));
typedef unsigned v2u __attribute__((ext_vector_type(2)));
#define GAS __attribute__((address_space(1)))
#define LAS __attribute__((address_space(3)))
#define DI __device__ __forceinline__
#define RLX_AGENT __ATOMIC_RELAXED, __HIP_MEMORY_SCOPE_AGENT
#define IC(n) std::integral_constant<int, (n)>{}
typedef __amdgpu_buffer_rsrc_t rsrc_t;

constexpr int SEQ = 2048, DM = 512, FF = 1792;
constexpr unsigned KB = 1024u, MB = 1u << 20;
constexpr unsigned FL_EPOCH = 0;
constexpr unsigned FL_SEEN = 2;
constexpr unsigned FL_P = 32;
#define RQV 4
#define RXV 4
#define ROV 2
constexpr unsigned RQ = RQV, RX = RXV, RO = ROV;
constexpr unsigned FL_Q = FL_P + 44;
constexpr unsigned FL_X = FL_Q + RQ * 16;
constexpr unsigned FL_O = FL_X + RX * 192;
constexpr unsigned FL_H = FL_O + RO * 256;
constexpr unsigned FL_KV = FL_H + RO * 256;
constexpr unsigned FL_LINES = FL_KV + 2 * 16 * 16;
static_assert(FL_LINES * 128 <= 768 * KB, "flags");
constexpr unsigned OFF_AUG = 768 * KB;
constexpr unsigned OFF_ST0 = 896 * KB;
constexpr unsigned OFF_CSP = 10 * MB;
constexpr unsigned OFF_WT = 1 * MB, OFF_WO = 2 * MB + 512 * KB, OFF_WGU = 3 * MB, OFF_WD = 6 * MB + 512 * KB;
constexpr unsigned OFF_ST = 9 * MB;
constexpr unsigned OFF_X1 = 11 * MB, OFF_X2 = 15 * MB, OFF_O = 17 * MB, OFF_KV = 21 * MB, OFF_H = 29 * MB;
constexpr unsigned WS_BYTES = 43 * MB;
constexpr unsigned OOB = 0x40000000u;
constexpr unsigned R0 = 0, R1 = 32 * KB, R2 = 64 * KB, R3 = 96 * KB, R4V = 128 * KB, R4S = 144 * KB, LDS_BYTES = 160 * KB;
constexpr unsigned KVS = R3, S_ONES = R4S, S_KMAX = R4S + 2048, S_RELAY = R4S + 2304, S_DONE = R4S + 2368, S_AUGQ = R4S + 2560, S_RSTD = R4S + 3584,
                   S_Q = R4S + 4096, S_RDY = R4V + 4096, S_ATT = R4S + 2432, S_AUGQ1 = R3, S_PART = R4S + 12288, S_DUMMY = R4S + 14336;
constexpr float NEG_BIG = -1.0e30f;
constexpr float LOG2E = 1.4426950408889634f;
constexpr float SAFE2 = 10000.0f;
constexpr unsigned SPIN_CAP = 1u << 18;
#define D1V 8
#define D2V 8
#define D3V 8
#define D4V 10
#define OUTAUX 0
#define CODEPF 1
#define XDELAY 3
#define SDELAY 0
#define W2DMA dma4_paced
#define PRIO 1
#define WODMA dma4
#define PDELAY 0
#define F7AT 32
#define RSWAP 1
#define FA 25
#define F5 17
#define F6 10
#define PB 12
#define PA 11
#define PUTGAP 6
#define KVEARLY 0
#define ODELAY 0
constexpr int D1 = D1V, D2 = D2V, D3 = D3V, D4 = D4V;

template <int B, int E, class F> DI void sfor(F&& f) {
  if constexpr (B < E) { f(std::integral_constant<int, B>{}); sfor<B + 1, E>(f); }
}
DI bf16x8 as_bf8(v4u v) { return __builtin_bit_cast(bf16x8, v); }
DI bf16x2 as_bf2(unsigned v) { return __builtin_bit_cast(bf16x2, v); }
DI s16x4 as_s4(v2u v) { return __builtin_bit_cast(s16x4, v); }
DI f32x16 mfma16(v4u a, v4u b, f32x16 c) { return __builtin_amdgcn_mfma_f32_32x32x16_bf16(as_bf8(a), as_bf8(b), c, 0, 0, 0); }
DI f32x16 mfma8(v2u a, v2u b, f32x16 c) { return __builtin_amdgcn_mfma_f32_32x32x8bf16_1k(as_s4(a), as_s4(b), c, 0, 0, 0); }
DI unsigned cvtpk(float lo, float hi) { f32x2 f = {lo, hi}; bf16x2 p = __builtin_convertvector(f, bf16x2); return __builtin_bit_cast(unsigned, p); }
DI float bflo(unsigned v) { return __uint_as_float(v << 16); }
DI float bfhi(unsigned v) { return __uint_as_float(v & 0xffff0000u); }
DI float dot2(unsigned a, unsigned b, float c) { return __builtin_amdgcn_fdot2_f32_bf16(as_bf2(a), as_bf2(b), c, false); }
DI float ex2(float v) { return __builtin_amdgcn_exp2f(v); }
DI float xhalf_add(float v) { auto r = __builtin_amdgcn_permlane32_swap(__float_as_uint(v), __float_as_uint(v), false, false); return __uint_as_float(r[0]) + __uint_as_float(r[1]); }
DI float xhalf_max(float v) { auto r = __builtin_amdgcn_permlane32_swap(__float_as_uint(v), __float_as_uint(v), false, false); return fmaxf(__uint_as_float(r[0]), __uint_as_float(r[1])); }
DI bool all_lanes(bool p) { return __builtin_amdgcn_ballot_w64(!p) == 0ull; }
DI unsigned umax(unsigned a, unsigned b) { return a > b ? a : b; }
DI unsigned row_umax(unsigned v) {
  v = umax(v, (unsigned)__builtin_amdgcn_update_dpp(0, (int)v, 0x121, 0xf, 0xf, true));
  v = umax(v, (unsigned)__builtin_amdgcn_update_dpp(0, (int)v, 0x122, 0xf, 0xf, true));
  v = umax(v, (unsigned)__builtin_amdgcn_update_dpp(0, (int)v, 0x124, 0xf, 0xf, true));
  v = umax(v, (unsigned)__builtin_amdgcn_update_dpp(0, (int)v, 0x128, 0xf, 0xf, true));
  return v;
}
DI v4u ld16(rsrc_t rs, unsigned voff, unsigned soff) { return __builtin_amdgcn_raw_buffer_load_b128(rs, voff, soff, 16); }
DI v4u ld16p(rsrc_t rs, unsigned voff, unsigned soff) { return __builtin_amdgcn_raw_buffer_load_b128(rs, voff, soff, 0); }
DI v2u ld8p(rsrc_t rs, unsigned voff, unsigned soff) { return __builtin_bit_cast(v2u, __builtin_amdgcn_raw_buffer_load_b64(rs, voff, soff, 0)); }
DI v2u ld8(rsrc_t rs, unsigned voff, unsigned soff) { return __builtin_bit_cast(v2u, __builtin_amdgcn_raw_buffer_load_b64(rs, voff, soff, 16)); }
DI void st16(v4u v, rsrc_t rs, unsigned voff, unsigned soff) {
  __builtin_amdgcn_raw_buffer_store_b128(v, rs, voff, soff, 16);
  asm volatile("s_nop 1" :: "v"(v) : "memory");
}
DI void st16n(v4u v, rsrc_t rs, unsigned voff, unsigned soff) { __builtin_amdgcn_raw_buffer_store_b128(v, rs, voff, soff, 16); }
#define KEEP(x) asm volatile("" :: "v"(x))
DI void st8(v2u v, rsrc_t rs, unsigned voff, unsigned soff) { __builtin_amdgcn_raw_buffer_store_b64(v, rs, voff, soff, 16); }
DI v4u lds16(LAS unsigned char* L, unsigned off) { return *(LAS v4u*)(L + off); }
#define SB() __builtin_amdgcn_sched_barrier(0)
#define OPQV(x) asm volatile("" : "+v"(x))
#define OPQS(x) asm volatile("" : "+s"(x))
#define LANE_CONSTS() int lane; asm volatile("v_mbcnt_lo_u32_b32 %0, -1, 0\n\tv_mbcnt_hi_u32_b32 %0, -1, %0" : "=v"(lane)); const int r = lane & 31, h = lane >> 5; const unsigned lane16 = lane * 16; (void)r; (void)h
#define BAR() asm volatile("s_waitcnt lgkmcnt(0)\n\ts_barrier" ::: "memory")
#define VMWAIT(n) asm volatile("s_waitcnt vmcnt(" #n ")" ::: "memory")
#define DMA_HEAD "s_nop 4\n\ts_mov_b32 m0, %0\n\ts_nop 0\n\t"
#define DMA_LD(o) "buffer_load_dwordx4 %1, %2, %3 offen offset:" #o " sc1 lds\n\t"
#define DMA_ARGS :: "s"(lds), "v"(voff), "s"(rs), "s"(soff) : "memory"
DI void dma4(rsrc_t rs, unsigned lds, unsigned soff, unsigned voff) { asm volatile(DMA_HEAD DMA_LD(0) DMA_LD(1024) DMA_LD(2048) DMA_LD(3072) DMA_ARGS); }
DI void dma2(rsrc_t rs, unsigned lds, unsigned soff, unsigned voff) { asm volatile(DMA_HEAD DMA_LD(0) DMA_LD(1024) DMA_ARGS); }
DI void dma1p(rsrc_t rs, unsigned lds, unsigned soff, unsigned voff) { asm volatile(DMA_HEAD "buffer_load_dwordx4 %1, %2, %3 offen lds" DMA_ARGS); }
#define PACE 6
#define STR2(x) #x
#define STR(x) STR2(x)
#define DMA_W "s_waitcnt vmcnt(" STR(PACE) ")\n\t"
static_assert(PACE <= 8, "some hand-offs count on the pacing waits");
DI void dma4_paced(rsrc_t rs, unsigned lds, unsigned soff, unsigned voff) {
  asm volatile(DMA_HEAD DMA_LD(0) DMA_W DMA_LD(1024) DMA_W DMA_LD(2048) DMA_W DMA_LD(3072) DMA_W DMA_ARGS);
}
DI void dma_tile(rsrc_t rs, unsigned lds, unsigned soff, unsigned lw, unsigned lane16) {
  dma4(rs, lds + lw * 8192u, soff + lw * 8192u, lane16);
  dma4(rs, lds + lw * 8192u + 4096u, soff + lw * 8192u + 4096u, lane16);
}
extern "C" __device__ long long wsatomic_add64(long long, rsrc_t, int, int, int) __asm("llvm.amdgcn.raw.ptr.buffer.atomic.add.i64");
struct Tag { unsigned lo, hi; };
DI v2u poll_ld(rsrc_t rs, unsigned voff, unsigned soff) {
  long long z = 0; OPQV(z);
  return __builtin_bit_cast(v2u, wsatomic_add64(z, rs, voff, soff, 0));
}
DI bool word_ok(v2u f, Tag t, unsigned target) { const unsigned a = f.x, b = f.y; return b == t.hi && (a - t.lo - target) <= 0xffffu - target; }
DI bool flags_ok(v2u f, Tag t, unsigned target, int lane, int n) { return all_lanes(word_ok(f, t, target) || lane >= n); }
DI v2u wait_words(rsrc_t rs, unsigned vo, unsigned soff, Tag t, unsigned target, bool act) {
  v2u f = {0u, 0u};
#pragma nounroll
  for (unsigned spins = 0; spins < SPIN_CAP; ++spins) {
    f = poll_ld(rs, vo, soff);
    if (all_lanes(word_ok(f, t, target) || !act)) break;
  }
  asm volatile("" ::: "memory");
  return f;
}
DI v2u wait_flags(rsrc_t rs, unsigned soff, Tag t, unsigned target, int lane, int n) {
  return wait_words(rs, lane < n ? lane * 8 : OOB, soff, t, target, lane < n);
}
DI void relay_set(LAS unsigned char* L, Tag t, unsigned id) { *(volatile LAS v2u*)(L + S_RELAY) = v2u{t.lo | id, t.hi}; }
DI bool relay_get(LAS unsigned char* L, Tag t, unsigned id) {
  const v2u v = *(volatile LAS v2u*)(L + S_RELAY);
  const unsigned a = v.x, b = v.y;
  return __builtin_amdgcn_readfirstlane(a) == (t.lo | id) && __builtin_amdgcn_readfirstlane(b) == t.hi;
}
DI void relay_wait(LAS unsigned char* L, Tag t, unsigned id) {
#pragma nounroll
  for (unsigned spins = 0; spins < 64 * SPIN_CAP && !relay_get(L, t, id); ++spins) __builtin_amdgcn_s_sleep(1);
  asm volatile("" ::: "memory");
}
DI void ready_set(LAS unsigned char* L, unsigned at, int lw, Tag t, unsigned id) {
  asm volatile("s_waitcnt lgkmcnt(0)" ::: "memory");
  *(volatile LAS v2u*)(L + at + lw * 8) = v2u{t.lo | id, t.hi};
}
DI void ready_wait(LAS unsigned char* L, unsigned at, int lane, Tag t, unsigned id) {
#pragma nounroll
  for (unsigned spins = 0; spins < 64 * SPIN_CAP; ++spins) {
    const v2u d = *(volatile LAS v2u*)(L + at + (lane & 3) * 8);
    const unsigned d0 = d.x, d1 = d.y;
    if (all_lanes(d0 == (t.lo | id) && d1 == t.hi)) break;
    __builtin_amdgcn_s_sleep(1);
  }
  asm volatile("" ::: "memory");
}
DI void swap64(rsrc_t rs, unsigned voff, unsigned soff, v2u v) {
  asm volatile("s_nop 4\n\tbuffer_atomic_swap_x2 %0, %1, %2, %3 offen\n\ts_nop 1" :: "v"(v), "v"(voff), "s"(rs), "s"(soff) : "memory");
}
DI void set_flag(rsrc_t rs, unsigned off, Tag t, unsigned payload, int lane, unsigned nrep = 1, unsigned rstep = 0) {
  swap64(rs, (unsigned)lane < nrep ? lane * rstep * 128u : OOB, off, v2u{t.lo | payload, t.hi});
}

struct Stats { float rstd, mean; v2u aug; };
DI Stats finish_stats(float s1, float s2, int h) {
  const float mean = s1 * (1.0f / DM);
  const float var = fmaxf(s2 * (1.0f / DM) - mean * mean, 0.0f);
  Stats st; st.rstd = __builtin_amdgcn_rsqf(var + 1e-5f); st.mean = mean;
  const float nm = h ? 0.0f : -mean;
  const unsigned hi = cvtpk(nm, nm) & 0xffffu;
  const float lo = nm - __uint_as_float(hi << 16);
  const unsigned w = hi | (cvtpk(lo, lo) << 16);
  st.aug.x = w; st.aug.y = w;
  return st;
}
DI Stats make_stats(const v2u (&sp)[8], int h) {
  float s1 = 0.f, s2 = 0.f;
#pragma unroll
  for (int i = 0; i < 8; ++i) { const unsigned a = sp[i].x, b = sp[i].y; s1 += __uint_as_float(a); s2 += __uint_as_float(b); }
  return finish_stats(xhalf_add(s1), xhalf_add(s2), h);
}
DI float xrow_add(float v) { auto r = __builtin_amdgcn_permlane16_swap(__float_as_uint(v), __float_as_uint(v), false, false); return __uint_as_float(r[0]) + __uint_as_float(r[1]); }
DI float wave_add(float v) {
  v += __int_as_float(__builtin_amdgcn_update_dpp(0, __float_as_int(v), 0x121, 0xf, 0xf, true));
  v += __int_as_float(__builtin_amdgcn_update_dpp(0, __float_as_int(v), 0x122, 0xf, 0xf, true));
  v += __int_as_float(__builtin_amdgcn_update_dpp(0, __float_as_int(v), 0x124, 0xf, 0xf, true));
  v += __int_as_float(__builtin_amdgcn_update_dpp(0, __float_as_int(v), 0x128, 0xf, 0xf, true));
  return xhalf_add(xrow_add(v));
}
DI v2u trd(LAS unsigned char* L, unsigned off) { return __builtin_bit_cast(v2u, __builtin_amdgcn_ds_read_tr16_b64_v4i16((LAS s16x4*)(L + off))); }
DI v4u trfrag(LAS unsigned char* L, unsigned off, unsigned second) { const v2u a = trd(L, off), b = trd(L, off + second); return v4u{a.x, a.y, b.x, b.y}; }
DI float sum8(v4u f, float c) {
  const unsigned a = f.x, b = f.y, d = f.z, e = f.w;
  return dot2(a, 0x3f803f80u, dot2(b, 0x3f803f80u, dot2(d, 0x3f803f80u, dot2(e, 0x3f803f80u, c))));
}
DI v2u split_aug(float cs) {
  const unsigned hi = cvtpk(cs, cs);
  const float lo = cs - __uint_as_float(hi << 16);
  return v2u{hi, cvtpk(lo, lo)};
}
DI float sum4(v4u p) {
  const unsigned a = p.x, b = p.y, c = p.z, d = p.w;
  return (__uint_as_float(a) + __uint_as_float(b)) + (__uint_as_float(c) + __uint_as_float(d));
}
DI v2u make_aug(v4u p) { return split_aug(sum4(p)); }

DI float max16(const f32x16& s, float m) {
  m = fmaxf(fmaxf(m, s[0]), s[1]);   m = fmaxf(fmaxf(m, s[2]), s[3]);
  float n = fmaxf(s[4], s[5]);       n = fmaxf(fmaxf(n, s[6]), s[7]);
  m = fmaxf(fmaxf(m, s[8]), s[9]);   n = fmaxf(fmaxf(n, s[10]), s[11]);
  m = fmaxf(fmaxf(m, s[12]), s[13]); n = fmaxf(fmaxf(n, s[14]), s[15]);
  return fmaxf(m, n);
}
DI void exp_pv(const f32x16& s, v4u v0, v4u v1, f32x16& o) {
  v4u p0, p1;
  p0.x = cvtpk(ex2(s[0]), ex2(s[1])); p0.y = cvtpk(ex2(s[2]), ex2(s[3])); p0.z = cvtpk(ex2(s[4]), ex2(s[5])); p0.w = cvtpk(ex2(s[6]), ex2(s[7]));
  o = mfma16(v0, p0, o);
  p1.x = cvtpk(ex2(s[8]), ex2(s[9])); p1.y = cvtpk(ex2(s[10]), ex2(s[11])); p1.z = cvtpk(ex2(s[12]), ex2(s[13])); p1.w = cvtpk(ex2(s[14]), ex2(s[15]));
  o = mfma16(v1, p1, o);
}
DI void attn_general(LAS unsigned char* L, v4u q, const f32x16& cm, int bm, int bf0, int nf, unsigned kaddr, unsigned vbase, unsigned vstep,
                     float& m, f32x16& o) {
  const f32x16 zero = {};
  float mx = max16(mfma16(lds16(L, kaddr + bm * 4096), q, cm), m);
#pragma nounroll
  for (int b = 0; b < nf; ++b) mx = max16(mfma16(lds16(L, kaddr + (bf0 + b) * 4096), q, zero), mx);
  mx = xhalf_max(mx);
  const float f = ex2(m - mx);
#pragma unroll
  for (int i = 0; i < 16; ++i) o[i] *= f;
  m = mx;
  f32x16 c;
#pragma unroll
  for (int i = 0; i < 16; ++i) c[i] = cm[i] - mx;
  unsigned va = vbase + vstep * bm;
  exp_pv(mfma16(lds16(L, kaddr + bm * 4096), q, c), lds16(L, va), lds16(L, va + 1024), o);
#pragma unroll
  for (int i = 0; i < 16; ++i) c[i] = -mx;
#pragma nounroll
  for (int b = 0; b < nf; ++b) {
    va = vbase + vstep * (bf0 + b);
    exp_pv(mfma16(lds16(L, kaddr + (bf0 + b) * 4096), q, c), lds16(L, va), lds16(L, va + 1024), o);
  }
}
template <int K, int KMAX, int S0, int DIR, class H>
DI void attn_chain(LAS unsigned char* L, v4u q, int n, unsigned kaddr, unsigned va, unsigned vstep, const f32x16& s, v4u v0, v4u v1, v4u kn,
                   f32x16& o, H&& hook) {
  if constexpr (K == KMAX) { hook(IC(K)); exp_pv(s, v0, v1, o); }
  else {
    const f32x16 zero = {};
    f32x16 sn = mfma16(kn, q, zero);
    v4u w0 = lds16(L, va), w1 = lds16(L, va + 1024);
    if constexpr (K + 1 < KMAX) kn = lds16(L, kaddr + (S0 + DIR * (K + 1)) * 4096);
    hook(IC(K));
    SB();
    exp_pv(s, v0, v1, o);
    asm volatile("" : "+v"(sn), "+v"(w0), "+v"(w1), "+v"(kn), "+v"(o));
    if ((DIR < 0 && K < 4) || n > K) attn_chain<K + 1, KMAX, S0, DIR>(L, q, n, kaddr, DIR > 0 ? va + vstep : va - vstep, vstep, sn, w0, w1, kn, o, hook);
  }
}

DI f32x16 unpack_tile(v4u r0, v4u r1) {
  f32x16 k;
  k[0] = bflo(r0.x); k[1] = bfhi(r0.x); k[2] = bflo(r0.y); k[3] = bfhi(r0.y); k[4] = bflo(r0.z); k[5] = bfhi(r0.z); k[6] = bflo(r0.w); k[7] = bfhi(r0.w);
  k[8] = bflo(r1.x); k[9] = bfhi(r1.x); k[10] = bflo(r1.y); k[11] = bfhi(r1.y); k[12] = bflo(r1.z); k[13] = bfhi(r1.z); k[14] = bflo(r1.w); k[15] = bfhi(r1.w);
  return k;
}
DI void pack_tile(const f32x16& k, v4u& y0, v4u& y1) {
  y0.x = cvtpk(k[0], k[1]); y0.y = cvtpk(k[2], k[3]); y0.z = cvtpk(k[4], k[5]); y0.w = cvtpk(k[6], k[7]);
  y1.x = cvtpk(k[8], k[9]); y1.y = cvtpk(k[10], k[11]); y1.z = cvtpk(k[12], k[13]); y1.w = cvtpk(k[14], k[15]);
}
DI v2u tile_stats(const f32x16& k) {
  f32x2 a = {k[0], k[1]}, b = a * a;
#pragma unroll
  for (int i = 1; i < 8; ++i) { const f32x2 t = {k[2 * i], k[2 * i + 1]}; a += t; b += t * t; }
  return v2u{__float_as_uint(xhalf_add(a[0] + a[1])), __float_as_uint(xhalf_add(b[0] + b[1]))};
}

constexpr int rslot(int f) { return RSWAP ? (f % 6) ^ ((f / 6) & 1) : f % 6; }
constexpr int rnext(int u) { return !RSWAP ? u + 6 : (u & 1) ? u + 5 : u + 7; }
#define RING_FILL(rd) sfor<0, 6>([&](auto I_) { constexpr int f_ = I_ ^ 1; ar[f_] = rd(IC(f_)); })
#define RING_LATE(rd, B) do { ar[rslot((B) + 1)] = rd(IC((B) + 1)); ar[rslot(B)] = rd(IC(B)); } while (0)
namespace sched {
constexpr int T4 = 128, T5 = 160, T6 = 192, P4 = 224, RES = T6 + 19;
constexpr int epi_at(int n) { return n < 40 ? 98 + 2 * n : (n < 48 ? T6 + 2 + 2 * (n - 40) : P4 + 1 + (3 * (n - 48)) / 2); }
constexpr int store_at(int p) { return epi_at(8 * p + 7) + 4; }
constexpr int flag(int g) { return g == T4 + FA ? 2 : g == T5 + FA ? 4 : g == T6 + F5 ? 5 : g == P4 + F6 ? 6 : g == P4 + F7AT ? 7 : 0; }
constexpr int hload(int g) { return g >= T6 + 21 && g < T6 + 21 + D4 ? g - T6 - 21 : (g > P4 && (g & 1) && (g - P4) / 2 + D4 < 56 ? (g - P4) / 2 + D4 : -1); }
constexpr int need(int c) { return P4 + 2 * (8 * c - D4) - 1; }
constexpr int check(int g) { return g == T6 + 18 ? 2 : g == need(2) ? 4 : 0; }
constexpr bool poll_a(int g) { return g == T5 + PA; }
constexpr bool poll_b(int g) { return g == T6 + PB; }
constexpr int pre(int g) { return (poll_a(g) ? 1 : 0) + (poll_b(g) ? 1 : 0); }
constexpr int post(int g) { return (g == RES ? 2 : 0) + (hload(g) >= 0 ? 1 : 0); }
constexpr bool is_store(int g) { for (int p = 0; p < 7; ++p) if (store_at(p) == g) return true; return false; }
constexpr int younger(int gs, int gw) {
  int n = post(gs) + pre(gw);
  for (int g = gs + 1; g < gw; ++g) n += pre(g) + (flag(g) ? 1 : 0) + (is_store(g) ? 1 : 0) + post(g);
  return n;
}
static_assert(D4 <= 10 && need(2) > P4, "chunk c is first requested behind the barrier that ends chunk c - 2");
}
struct Args { unsigned char* ws; u16* out; const u16 *x, *wqkv, *wo, *wg, *wu, *wd; };
#define STAMP(id) do {} while (0)
#define STAMP_DECL() do {} while (0)
#define STAMP_OUT() do {} while (0)

typedef const __attribute__((address_space(4))) Args* kargs_t;
DI kargs_t kargs() { kargs_t p = (kargs_t)__builtin_amdgcn_kernarg_segment_ptr(); OPQS(p); return p; }
__global__ void __launch_bounds__(512, 2) main_k(Args a) {
  extern __shared__ __attribute__((aligned(16))) unsigned char smem[];
  LAS unsigned char* L = (LAS unsigned char*)smem;
  const unsigned lb = (unsigned)(size_t)L;
  const int w0 = __builtin_amdgcn_readfirstlane(threadIdx.x >> 6);
  STAMP_DECL();
  const int xcd = blockIdx.x & 7, idx = blockIdx.x >> 3;
  const int grp0 = (xcd & ~1) * 2 + (idx >> 3), mem0 = ((idx & 4) << 1) | ((xcd & 1) << 2) | (idx & 3);
  const rsrc_t rs = __builtin_amdgcn_make_buffer_rsrc(a.ws, 0, WS_BYTES, 0x00020000);
#define UNI(x) (x)
#define ROLES() int w = w0, grp = grp0, mem = mem0, layer = UNI(layer0); OPQS(w); OPQS(grp); OPQS(mem); OPQS(layer); Tag tg = {(unsigned)UNI(tg0.lo), (unsigned)UNI(tg0.hi)}; OPQS(tg.lo); OPQS(tg.hi); \
  const int nblk = mem & 7, cw = w & 3, th = cw >> 1, kh = cw & 1;              \
  const int tb2 = 4 * grp + 2 * (mem >> 3) + th, tb3 = 4 * grp + cw;            \
  (void)nblk; (void)kh; (void)tb2; (void)tb3
  Tag tg0 = {0u, 0u};
  f32x16 cmc;
#define CAUSAL2(cm, i) do { float a0 = (((i) & 3) + 8 * ((i) >> 2) + 4 * h <= r) ? 0.0f : NEG_BIG, a1 = ((((i) + 1) & 3) + 8 * ((i) >> 2) + 4 * h <= r) ? 0.0f : NEG_BIG; \
    OPQV(a0); OPQV(a1); cm[i] = a0; cm[(i) + 1] = a1; } while (0)
#define MAKE_TAG(ep) do { const unsigned e0 = __builtin_amdgcn_readfirstlane((ep).x) & 0xffff0000u, e1 = __builtin_amdgcn_readfirstlane((ep).y); \
    tg.lo = e0 + 0x10000u; tg.hi = e1 + (tg.lo == 0u ? 1u : 0u); tg0 = tg; } while (0)
#define AUGQ_LOAD(cp) do { const unsigned tile_ = (cw == 2 ? 0 : 16 + 16 * cw) + mem;     _Pragma("unroll") for (int i_ = 0; i_ < 16; ++i_) \
    cp[i_] = __builtin_amdgcn_raw_buffer_load_b32(rs, cw < 3 ? h * 2048 + r * 4 + i_ * 128 : OOB, OFF_CSP + tile_ * 4096u, 16); } while (0)
#define AUGQ_PUT(cp, at) do { float s_ = 0.f; _Pragma("unroll") for (int i_ = 0; i_ < 16; ++i_) s_ += __uint_as_float(cp[i_]); \
    *(LAS v2u*)(L + (at) + cw * 256 + r * 8) = split_aug(xhalf_add(s_)); } while (0)

  auto qkv = [&](auto FIRST_, v4u (&x)[32], Stats st, int layer0) {
    constexpr bool FIRST = FIRST_;
    ROLES(); LANE_CONSTS();
    unsigned lhi = lane16 + R2; OPQV(lhi);
    auto wread = [&](auto U) -> v4u { constexpr int u = U; if constexpr (u < 64) return lds16(L, (u & 1) * 32768 + (u >> 1) * 1024 + lane16); else return lds16(L, (u - 64) * 1024 + lhi); };
    const unsigned xb = OFF_X2 + tb3 * 32768u;
    auto xload = [&](auto I) -> v4u { constexpr int i = I; return ld16(rs, lane16 + (i & 3) * 1024, xb + (i >> 2) * 4096u); };
    v4u ar[6];
    if constexpr (!FIRST) {
      sfor<0, 8>([&](auto I) { CAUSAL2(cmc, 2 * I); });
      __builtin_amdgcn_s_sleep(PDELAY);
      wait_flags(rs, (FL_X + (mem % RX) * 192 + 64 + tb3) * 128u, tg, 1u, lane, 16);
      STAMP(34);
      sfor<0, D1>([&](auto I) { constexpr int i = I; x[i] = xload(I); });
      ready_wait(L, S_RDY, lane, tg, 3u);
    }
    RING_FILL(wread);
    constexpr int lay = FIRST ? 0 : 1;
    const unsigned kvb = OFF_KV + lay * (4 * MB) + (mem * 64 + tb3) * 4096u, kvo = grp < 15 ? lane16 : OOB;
    unsigned own = KVS + (8 + cw) * 4096 + lane16; OPQV(own);
    f32x16 ck = {}, cv = {}, cq;
    v2u sp[8], ag;
    f32x4 rr[4];
    v4u kvf[4];
    float k2 = 0.f;
    unsigned kr, k16;
    SB();
    sfor<0, 96>([&](auto I) {
      constexpr int u = I, s = u < 64 ? u >> 1 : u - 64;
      if constexpr (FIRST && u == 28) VMWAIT(0);
      if constexpr (u == 28 || u == 60) { BAR(); RING_LATE(wread, u + 4); }
      if constexpr (u == 64) { const f32x16 z = {}; cq = mfma8(ag, st.aug, z); }
      if constexpr (u >= 64) { cq = mfma16(ar[rslot(u)], x[s], cq); OPQV(cq); }
      else if constexpr (u & 1) { cv = mfma16(x[s], ar[rslot(u)], cv); OPQV(cv); }
      else { ck = mfma16(ar[rslot(u)], x[s], ck); OPQV(ck); }
      if constexpr (rnext(u) < 96 && u != 26 && u != 27 && u != 58 && u != 59) ar[rslot(u)] = wread(IC(rnext(u)));
      if constexpr (!FIRST) {
        if constexpr (u < 64 && (u & 1) && s + D1 < 32) x[s + D1] = xload(IC(s + D1));
        if constexpr (u < 16 && !(u & 1)) sp[s] = ld8(rs, h * 2048 + r * 8 + s * 256, OFF_ST + (64 + tb3) * 4096u);
        if constexpr (u == 36) { st = make_stats(sp, h); OPQV(st.aug); }
      }
      if constexpr (u == 32) { unsigned o2 = 0x3f803f80u; OPQV(o2); *(LAS v4u*)(L + S_ONES + (lane & 1) * 1024) = v4u{o2, o2, o2, o2}; }
      if constexpr (u == 38) *(LAS float*)(L + S_RSTD + cw * 128 + r * 4) = st.rstd;
      if constexpr (u == 40 || u == 45 || u == 50) ag = *(LAS v2u*)(L + (FIRST ? S_AUGQ : S_AUGQ1) + ((u - 40) / 5) * 256 + r * 8);
      if constexpr (u == 43) { ck = mfma8(ag, st.aug, ck); OPQV(ck); }
      if constexpr (u == 48) { cv = mfma8(st.aug, ag, cv); OPQV(cv); }
      if constexpr (u == 52) {
#pragma unroll
        for (int g = 0; g < 4; ++g) rr[g] = *(LAS f32x4*)(L + S_RSTD + cw * 128 + (8 * g + 4 * h) * 4);
      }
      if constexpr (u >= 66 && u < 86 && (u - 66) % 5 < 4) {
        constexpr int n = (u - 66) / 5, i = (n & 1) * 4 + (u - 66) % 5;
        float a0, a1;
        if constexpr (n < 2) { a0 = ck[2 * i] * st.rstd; OPQV(a0); a1 = ck[2 * i + 1] * st.rstd; OPQV(a1); ck[2 * i] = a0; ck[2 * i + 1] = a1; }
        else { a0 = cv[2 * i] * rr[i >> 1][(2 * i) & 3]; OPQV(a0); a1 = cv[2 * i + 1] * rr[i >> 1][(2 * i + 1) & 3]; OPQV(a1); }
        unsigned pk = cvtpk(a0, a1); OPQV(pk);
        kvf[n][i & 3] = pk;
      }
      if constexpr (u >= 70 && u <= 85 && (u - 70) % 5 == 0) {
        st16n(kvf[(u - 70) / 5], rs, kvo + ((u - 70) / 5) * 1024, kvb);
        *(LAS v4u*)(L + own + ((u - 70) / 5) * 1024) = kvf[(u - 70) / 5];
      }
      if constexpr (u >= 71 && u <= 86 && (u - 71) % 5 == 0) KEEP(kvf[(u - 71) / 5]);
      if constexpr (u >= 76 && u <= 90 && !(u & 1)) { constexpr int i = u - 76; k2 = fmaxf(fmaxf(k2, __builtin_fabsf(ck[i])), __builtin_fabsf(ck[i + 1])); OPQV(k2); }
      if constexpr (u == 91) { kr = __float_as_uint(k2); kr = umax(kr, (unsigned)__builtin_amdgcn_update_dpp(0, (int)kr, 0x121, 0xf, 0xf, true)); kr = umax(kr, (unsigned)__builtin_amdgcn_update_dpp(0, (int)kr, 0x122, 0xf, 0xf, true)); OPQV(kr); }
      if constexpr (u == 92) { kr = umax(kr, (unsigned)__builtin_amdgcn_update_dpp(0, (int)kr, 0x124, 0xf, 0xf, true)); kr = umax(kr, (unsigned)__builtin_amdgcn_update_dpp(0, (int)kr, 0x128, 0xf, 0xf, true)); OPQV(kr); }
      if constexpr (u == 94) {
        const unsigned km0 = (umax(umax(__builtin_amdgcn_readlane(kr, 0), __builtin_amdgcn_readlane(kr, 16)), umax(__builtin_amdgcn_readlane(kr, 32), __builtin_amdgcn_readlane(kr, 48))) >> 16) + 1u;
        k16 = km0 < 0x7fc0u ? km0 : 0x7fc0u;
      }
      if constexpr (u == 95 && !FIRST) asm volatile(".Lmk_last:");
      SB();
    });
    STAMP(FIRST ? 3 : 35);
    *(LAS unsigned*)(L + S_KMAX + (8 + cw) * 4) = k16 << 16;
    const float qs = st.rstd * (0.25f * LOG2E);
#pragma unroll
    for (int i = 0; i < 16; ++i) cq[i] *= qs;
    v4u f0, f1;
    pack_tile(cq, f0, f1);
    *(LAS v4u*)(L + S_Q + cw * 2048 + lane16) = f0; *(LAS v4u*)(L + S_Q + cw * 2048 + 1024 + lane16) = f1;
    ready_set(L, S_DONE, cw, tg, 1u + lay);
    VMWAIT(0);
    set_flag(rs, (FL_KV + (lay * 16 + mem) * 16 + grp) * 128u + cw * 8, tg, k16, lane);
  };
#define WQKV_DMA(F, part) do { if ((part) < 2) { F(rs, lb + R0 + (part) * 16384u + cw * 4096u, OFF_WT + (16 + mem) * 32768u + (part) * 16384u + cw * 4096u, lane16); \
                                                 F(rs, lb + R1 + (part) * 16384u + cw * 4096u, OFF_WT + (32 + mem) * 32768u + (part) * 16384u + cw * 4096u, lane16); } \
    else { F(rs, lb + R2 + cw * 8192u, OFF_WT + mem * 32768u + cw * 8192u, lane16); F(rs, lb + R2 + cw * 8192u + 4096u, OFF_WT + mem * 32768u + cw * 8192u + 4096u, lane16); } } while (0)

#define WO_DMA() do { _Pragma("unroll") for (int i_ = 0; i_ < 4; ++i_) \
    WODMA(rs, lb + R0 + cw * 16384u + i_ * 4096u, OFF_WO + nblk * 65536u + cw * 16384u + i_ * 4096u, lane16); } while (0)
    auto attention = [&](int layer0, const f32x16 cm, auto&& hook, auto&& nohook) {
      ROLES(); LANE_CONSTS();
      const int hd = w >> 2, tb = w < 4 ? w : 7 - w;
      const bool up = (lane & 16) != 0;
      unsigned kaddr = KVS + hd * 1024 + lane16; OPQV(kaddr);
      const unsigned vstep = up ? 0u : 4096u, vbase = up ? S_ONES : KVS + 2048 + (h * 32 + hd * 16 + (lane & 15)) * 16;
      STAMP(4);
      if (w >= 6 && grp > 0) {
        const int p = w - 6;
        const bool have = grp - 2 + p >= 0;
        v2u f = {0u, 0u};
        if (have) f = wait_flags(rs, (FL_KV + (layer * 16 + mem) * 16 + grp - 2 + p) * 128u, tg, 1u, lane, 4);
#pragma unroll
        for (int i = 0; i < 4; ++i)
          dma4(rs, lb + KVS + (4 * p + i) * 4096u, OFF_KV + layer * (4 * MB) + (mem * 64 + 4 * (grp - 2 + p) + i) * 4096u, have ? lane16 : OOB);
        if (lane < 4) *(LAS unsigned*)(L + S_KMAX + (4 * p + lane) * 4) = f.x << 16;
        __builtin_amdgcn_s_setprio(1);
        STAMP(7);
      }
      ready_wait(L, S_DONE, lane, tg, 1u + layer);
      STAMP(5);
      const v4u qf = lds16(L, S_Q + tb * 2048 + hd * 1024 + lane16);
      const unsigned km = *(LAS unsigned*)(L + S_KMAX + (8 + (lane & 3)) * 4);
      unsigned vm = vbase + vstep * (8 + tb);
      v4u kd = lds16(L, kaddr + (8 + tb) * 4096), kn = lds16(L, kaddr + 8 * 4096), v0 = lds16(L, vm), v1 = lds16(L, vm + 1024);
      f32x16 s0 = mfma16(kd, qf, cm); OPQV(s0);
      const unsigned q0 = qf.x, q1 = qf.y, q2_ = qf.z, q3 = qf.w;
      const float q2 = xhalf_add(dot2(q0, q0, dot2(q1, q1, 0.f)) + dot2(q2_, q2_, dot2(q3, q3, 0.f))) * 16.5f;
      const float ko = __uint_as_float(row_umax(km));
      bool ok = all_lanes(q2 * ko * ko <= SAFE2);
      f32x16 o = {};
      if (ok) attn_chain<0, 3, 8, 1>(L, qf, tb, kaddr, vbase + vstep * 8, vstep, s0, v0, v1, kn, o, [&](auto) {});
      STAMP(6);
      f32x16 cmo;
#pragma unroll
      for (int i = 0; i < 16; ++i) cmo[i] = grp == 1 ? NEG_BIG : NEG_BIG - cm[i];
      OPQV(cmo);
      vm = vbase + vstep * tb;
      if (w >= 6) VMWAIT(0);
      STAMP(8);
      BAR();
      STAMP(9);
      const int np = grp == 1 ? 4 : 7 - tb;
      if (tb < 2) __builtin_amdgcn_s_setprio(1); else __builtin_amdgcn_s_setprio(0);
      if (grp > 0) {
        const unsigned kp = *(LAS unsigned*)(L + S_KMAX + (lane & 7) * 4);
        kd = lds16(L, kaddr + tb * 4096); kn = lds16(L, kaddr + 7 * 4096); v0 = lds16(L, vm); v1 = lds16(L, vm + 1024);
        s0 = mfma16(kd, qf, cmo); OPQV(s0);
        const float kq = __uint_as_float(row_umax(kp));
        ok = ok && all_lanes(q2 * kq * kq <= SAFE2);
      }
      if (ok) {
        if (grp > 0) attn_chain<0, 7, 7, -1>(L, qf, np, kaddr, vbase + vstep * 7, vstep, s0, v0, v1, kn, o, hook);
        else nohook();
      } else {
        nohook();
        float m = NEG_BIG;
#pragma unroll
        for (int i = 0; i < 16; ++i) o[i] = 0.f;
        attn_general(L, qf, cm, 8 + tb, 8, tb, kaddr, vbase, vstep, m, o);
        if (grp > 0) attn_general(L, qf, cmo, tb, 8 - np, np, kaddr, vbase, vstep, m, o);
      }
      __builtin_amdgcn_s_setprio(0);
      STAMP(10);
      const float il = __builtin_amdgcn_rcpf(o[8]);
      v4u of;
      of.x = cvtpk(o[0] * il, o[1] * il); of.y = cvtpk(o[2] * il, o[3] * il); of.z = cvtpk(o[4] * il, o[5] * il); of.w = cvtpk(o[6] * il, o[7] * il);
      const int head = 2 * mem + hd, Tq = 4 * grp + tb;
      st16(of, rs, lane16, OFF_O + layer * (2 * MB) + (Tq * 32 + head) * 1024u);
      VMWAIT(0);
      set_flag(rs, (FL_O + (layer * 64 + Tq) * 2 + (head >> 4)) * 128u + (head & 15) * 8, tg, 1u, lane, RO, 256);
      ready_set(L, S_ATT + (w >> 2) * 32, cw, tg, 1u + layer);
      STAMP(11);
    };
  if (w0 < 4) {
   {
    const int layer0 = 0;
    STAMP(31);
    ROLES(); LANE_CONSTS();
    const rsrc_t rx = __builtin_amdgcn_make_buffer_rsrc((void*)a.x, 0, SEQ * DM * 2, 0x00020000);
    v2u ep;
    asm volatile("s_nop 4\n\tbuffer_load_dwordx2 %0, off, %1, 0 sc1" : "=&v"(ep) : "s"(rs) : "memory");
    const unsigned xv0 = (lane >> 3) * 1024 + (((lane & 7) ^ (lane >> 4)) * 16), xv1 = (xv0 ^ 64u) + 7168, xv2 = xv0 + 14336, xv3 = xv1 + 14336;
    auto xround = [&](int rho) {
      asm volatile("s_nop 4\n\ts_mov_b32 m0, %0\n\ts_nop 0\n\tbuffer_load_dwordx4 %1, %5, %6 offen lds\n\tbuffer_load_dwordx4 %2, %5, %6 offen offset:1024 lds\n\t"
                   "buffer_load_dwordx4 %3, %5, %6 offen offset:2048 lds\n\tbuffer_load_dwordx4 %4, %5, %6 offen offset:3072 lds"
                   :: "s"(lb + KVS + w * 12288u + (rho % 3) * 4096u), "v"(xv0), "v"(xv1), "v"(xv2), "v"(xv3), "s"(rx), "s"(tb3 * 32768u + rho * 128u) : "memory");
    };
    __builtin_amdgcn_s_sleep(XDELAY);
    const int un = grp * 16 + mem, g = lane >> 4, q = (lane & 15) >> 2, pp = lane & 3;
    const int uty = un < 112 ? 0 : (un < 224 ? 1 : 2);
    const int pcb = uty == 0 ? un >> 2 : (uty == 1 ? (un - 112) / 14 : (un - 224) >> 2), pkq = uty == 1 ? (un - 112) % 14 : un & 3, pks = 8 * pkq + 2 * cw;
    auto prep_load = [&] {
      const unsigned pld = uty == 0 ? FF * 2 : 1024;
      const rsrc_t ra = __builtin_amdgcn_make_buffer_rsrc((void*)(uty == 0 ? a.wg : (uty == 1 ? a.wd : a.wo)), 0, uty == 2 ? DM * 1024 : FF * 1024, 0x00020000);
      const rsrc_t rb = __builtin_amdgcn_make_buffer_rsrc((void*)a.wu, 0, FF * 1024, 0x00020000);
      const unsigned pvo = (lane >> 3) * pld + (lane & 7) * 16, pso = 16 * pks * pld + pcb * 128;
#pragma unroll
      for (int i = 0; i < 4; ++i) dma1p(ra, lb + R2 + cw * 8192u + i * 1024u, pso + 8 * i * pld, pvo);
      if (uty == 0) {
#pragma unroll
        for (int i = 0; i < 4; ++i) dma1p(rb, lb + R2 + cw * 8192u + 4096u + i * 1024u, pso + 8 * i * pld, pvo);
      }
    };
    xround(0);
    prep_load();
    xround(1); xround(2);
    unsigned xa[4];
#pragma unroll
    for (int i = 0; i < 4; ++i) { xa[i] = KVS + w * 12288 + (r >> 3) * 1024 + (r & 7) * 128 + (((2 * i) ^ ((r >> 1) & 7)) << 4) + h * 8; OPQV(xa[i]); }
    sfor<0, 8>([&](auto I) { CAUSAL2(cmc, 2 * I); });
    v4u x0[32];
    SB();
    sfor<0, 8>([&](auto RHO) {
      constexpr int rho = RHO;
      if constexpr (rho == 0) { if (uty == 0) VMWAIT(16); else VMWAIT(12); asm volatile("" : "+v"(ep) :: "memory"); }
      else if constexpr (rho < 6) VMWAIT(8);
      else if constexpr (rho == 6) VMWAIT(4);
      else VMWAIT(0);
      v2u t[8];
#pragma unroll
      for (int i = 0; i < 4; ++i) { t[2 * i] = *(LAS v2u*)(L + xa[i] + (rho % 3) * 4096); t[2 * i + 1] = *(LAS v2u*)(L + (xa[i] ^ 16u) + (rho % 3) * 4096); }
      asm volatile("s_waitcnt lgkmcnt(0)" : "+v"(t[0]), "+v"(t[1]), "+v"(t[2]), "+v"(t[3]), "+v"(t[4]), "+v"(t[5]), "+v"(t[6]), "+v"(t[7]) :: "memory");
      if constexpr (rho + 3 < 8) xround(rho + 3);
#pragma unroll
      for (int i = 0; i < 4; ++i) { const unsigned a0 = t[2 * i].x, a1 = t[2 * i].y, a2 = t[2 * i + 1].x, a3 = t[2 * i + 1].y; x0[4 * rho + i] = v4u{a0, a1, a2, a3}; }
      SB();
    });
    MAKE_TAG(ep);
    STAMP(0);
    const unsigned t32 = (tg.lo >> 16) | (tg.hi << 16);
    v2u sgr = {0u, 0u};
    __builtin_amdgcn_s_sleep(SDELAY);
#pragma nounroll
    for (unsigned spins = 0; spins < SPIN_CAP; ++spins) {
      sgr = ld8(rs, r * 16 + h * 8, OFF_ST0 + tb3 * 512u);
      if (all_lanes(sgr.y == t32)) break;
      asm volatile("s_sleep 4" ::: "memory");
    }
    const unsigned sv = sgr.x;
    const auto svs = __builtin_amdgcn_permlane32_swap(sv, sv, false, false);
    const Stats st = finish_stats(__uint_as_float(svs[0]), __uint_as_float(svs[1]), h);
    {
      const unsigned ta = R2 + cw * 8192 + (4 * (g >> 1) + q) * 128 + pp * 8;
      if (uty == 0) {
        v4u f[8];
#pragma unroll
        for (int i = 0; i < 8; ++i) f[i] = trfrag(L, ta + (g & 1) * 4096 + (i >> 2) * 2048 + (i & 3) * 32, 1024);
#pragma unroll
        for (int i = 0; i < 8; ++i) st16(f[i], rs, lane16 + (i >> 2) * 1024, OFF_WGU + ((4 * pcb + (i & 3)) * 32 + pks) * 1024u);
#pragma unroll
        for (int tl = 0; tl < 4; ++tl) *(LAS float*)(L + S_PART + ((cw * 4 + tl) * 32 + r) * 4) = xhalf_add(sum8(f[tl], sum8(f[4 + tl], 0.f)));
      } else {
        v4u f[4];
#pragma unroll
        for (int i = 0; i < 4; ++i) f[i] = trfrag(L, ta + (g & 1) * 32 + (i >> 1) * 2048 + (i & 1) * 64, 1024);
#pragma unroll
        for (int i = 0; i < 4; ++i) {
          const int ks = pks + (i >> 1), mm = ks / 7, cc = ks % 7;
          st16(f[i], rs, lane16, uty == 1 ? OFF_WD + (pcb * 224 + cc * 32 + (mm >> 3) * 16 + (mm & 7) * 2 + (i & 1)) * 1024u : OFF_WO + ((pcb * 2 + (i & 1)) * 32 + ks) * 1024u);
        }
      }
    }
    if (w == 3) set_flag(rs, FL_SEEN * 128u + (grp * 16 + mem) * 8u, tg, 1u, lane);
    STAMP(1);
    BAR();
    STAMP(2);
    qkv(std::true_type{}, x0, st, 0);
   }
    for (int layer0 = 0;; ++layer0) {
      attention(layer0, cmc, [&](auto) {}, [&] {});
      {
        ROLES(); LANE_CONSTS();
        const unsigned xfo = (tb2 * 32 + 4 * nblk + 2 * kh) * 1024u;
        v4u r0, r1;
        if (layer) { r0 = ld16(rs, lane16, OFF_X2 + xfo); r1 = ld16(rs, lane16 + 1024, OFF_X2 + xfo); }
        else {
          const rsrc_t rx = __builtin_amdgcn_make_buffer_rsrc((void*)kargs()->x, 0, SEQ * DM * 2, 0x00020000);
          const unsigned vo = r * 1024 + h * 8, so = tb2 * 32768u + nblk * 128u + kh * 64u;
          const v2u t0 = ld8p(rx, vo, so), t1 = ld8p(rx, vo + 16, so), t2 = ld8p(rx, vo + 32, so), t3 = ld8p(rx, vo + 48, so);
          r0 = v4u{t0.x, t0.y, t1.x, t1.y}; r1 = v4u{t2.x, t2.y, t3.x, t3.y};
        }
        unsigned wa0 = R0 + kh * (16384 + 32768) + lane16, wa1 = wa0 ^ 32768u; OPQV(wa0); OPQV(wa1);
        auto wread = [&](auto U) -> v4u { constexpr int u = U; return lds16(L, ((u & 1) ? wa1 : wa0) + (u >> 1) * 1024); };
        v4u ar[6];
        f32x16 c0 = unpack_tile(r0, r1), c1 = {};
        OPQV(c0);
        __builtin_amdgcn_s_sleep(ODELAY);
        wait_flags(rs, (FL_O + (nblk % RO) * 256 + (layer * 64 + tb2) * 2 + kh) * 128u, tg, 1u, lane, 16);
        STAMP(13);
        const unsigned ob = OFF_O + layer * (2 * MB) + (tb2 * 32 + 16 * kh) * 1024u;
        auto oload = [&](auto I) -> v4u { constexpr int i = I; return ld16(rs, lane16 + (i & 3) * 1024, ob + (i >> 2) * 4096u); };
        v4u ofr[16];
        sfor<0, D2>([&](auto I) { constexpr int i = I; ofr[i] = oload(I); });
        ready_wait(L, S_RDY, lane, tg, 4u + layer);
        STAMP(12);
        RING_FILL(wread);
        SB();
        sfor<0, 32>([&](auto I) {
          constexpr int u = I;
          if constexpr (u & 1) { c1 = mfma16(ar[rslot(u)], ofr[u >> 1], c1); OPQV(c1); } else { c0 = mfma16(ar[rslot(u)], ofr[u >> 1], c0); OPQV(c0); }
          if constexpr (rnext(u) < 32) ar[rslot(u)] = wread(IC(rnext(u)));
          if constexpr ((u & 1) && (u >> 1) + D2 < 16) ofr[(u >> 1) + D2] = oload(IC((u >> 1) + D2));
          SB();
        });
        unsigned sown = R4S + cw * 4096 + lane16; OPQV(sown);
#pragma unroll
        for (int g = 0; g < 4; ++g) *(LAS f32x4*)(L + sown + g * 1024) = f32x4{c1[4 * g], c1[4 * g + 1], c1[4 * g + 2], c1[4 * g + 3]};
        OPQV(c0);
        STAMP(14);
        BAR();
        STAMP(15);
#pragma unroll
        for (int g = 0; g < 4; ++g) { const f32x4 t = *(LAS f32x4*)(L + (sown ^ 4096u) + g * 1024); c0[4 * g] += t[0]; c0[4 * g + 1] += t[1]; c0[4 * g + 2] += t[2]; c0[4 * g + 3] += t[3]; }
        v4u y0, y1;
        pack_tile(c0, y0, y1);
        const unsigned xo = OFF_X1 + layer * (2 * MB) + xfo;
        st16(y0, rs, lane16, xo); st16(y1, rs, lane16 + 1024, xo);
        st8(tile_stats(c0), rs, h ? OOB : r * 8, OFF_ST + ((layer * 128 + tb2) * 16 + 2 * nblk + kh) * 256u);
        VMWAIT(0);
        set_flag(rs, (FL_X + layer * 128 + tb2) * 128u + (2 * nblk + kh) * 8, tg, 1u, lane, RX, 192);
      }
      {
        ROLES(); LANE_CONSTS();
        STAMP(16);
        v2u fpa, fpb;
        v4u ar[6];
        unsigned lhi = lane16 + R2; OPQV(lhi);
        unsigned lp0 = lane16 + kh * (16384 + 1024), lp1 = lp0 ^ 1024u;
        unsigned lp0h = lp0 + R2, lp1h = lp1 + R2;
        OPQV(lp0); OPQV(lp1); OPQV(lp0h); OPQV(lp1h);
        auto aread = [&](auto G) -> v4u {
          constexpr int g = G;
          if constexpr (g < 224) {
            constexpr int T = g < 96 ? g % 3 : 3 + (g - 96) / 32, s = g < 96 ? g / 3 : (g - 96) % 32, sl = (T + 2) & 3;
            return lds16(L, (sl & 1) * 32768 + s * 1024 + (sl < 2 ? lane16 : lhi));
          } else {
            constexpr int u = g - 224, c = u / 16, i = (u % 16) >> 1, sl = (c + 1) & 3;
            return lds16(L, (sl & 1) * 32768 + i * 2048 + (sl < 2 ? ((u & 1) ? lp1 : lp0) : ((u & 1) ? lp1h : lp0h)));
          }
        };
        unsigned aga = R4V + r * 8; OPQV(aga);
        __builtin_amdgcn_s_sleep(PDELAY);
        wait_flags(rs, (FL_X + (mem % RX) * 192 + layer * 128 + tb3) * 128u, tg, 1u, lane, 16);
        STAMP(17);
        v2u sp[8];
        const unsigned xb = OFF_X1 + layer * (2 * MB) + tb3 * 32768u;
        auto xload = [&](auto I) -> v4u { constexpr int i = I; return ld16(rs, lane16 + (i & 3) * 1024, xb + (i >> 2) * 4096u); };
        v4u x[32];
        sfor<0, D3>([&](auto I) { constexpr int i = I; x[i] = xload(I); });
        ready_wait(L, S_RDY, lane, tg, 1u + layer);
        STAMP(24);
        RING_FILL(aread);
        SB();
        const unsigned hb = OFF_H + layer * (7 * MB) + (tb3 * 112 + mem * 7) * 1024u;
        const unsigned fh_mine = (FL_H + (layer * 64 + tb3) * 2 + (mem >> 3)) * 128u + (mem & 7) * 8;
        const unsigned fh_poll = (FL_H + (nblk % RO) * 256 + (layer * 64 + tb2) * 2 + kh) * 128u;
        const unsigned xfo = (tb2 * 32 + 4 * nblk + 2 * kh) * 1024u;
        const unsigned hb4 = OFF_H + layer * (7 * MB) + (tb2 * 112 + 56 * kh) * 1024u;
        Stats st;
        float cg, cu;
        f32x16 acc[4] = {};
        f32x16 c0 = {}, c1;
        v2u agn;
        v4u hr[D4], r0, r1;
        float ee[2], hv[2]; v4u hf;
        auto epi = [&](auto N_, auto K_) {
          constexpr int p = N_ / 8, e = N_ % 8, k = K_;
          if constexpr (k == 0) { ee[e & 1] = ex2(acc[p & 3][e] * cg); OPQV(ee[e & 1]); }
          if constexpr (k == 1) { ee[e & 1] = __builtin_amdgcn_rcpf(__builtin_fmaf(ee[e & 1], cu, cu)); OPQV(ee[e & 1]); }
          if constexpr (k == 2) { hv[e & 1] = ee[e & 1] * acc[p & 3][8 + e]; OPQV(hv[e & 1]); }
          if constexpr (k == 3 && (e & 1)) { unsigned pk = cvtpk(hv[0], hv[1]); OPQV(pk); hf[e >> 1] = pk; }
          if constexpr (k == 4 && e == 7) st16n(hf, rs, lane16 + (p & 3) * 1024, hb + (p >> 2) * 4096);
          if constexpr (k == 5 && e == 7) KEEP(hf);
        };
        SB();
        sfor<0, 336>([&](auto I) {
          constexpr int g = I;
          constexpr bool P4 = g >= 224;
          constexpr int T = g < 96 ? g % 3 : (P4 ? 7 : 3 + (g - 96) / 32), s = g < 96 ? g / 3 : (g - 96) % 32, u = g - 224;
          constexpr int left = g < 96 ? 96 - g : (P4 ? 16 - u % 16 : 32 - s);
          if constexpr (left == 4 && g < 328) {
            BAR();
            RING_LATE(aread, g + 4);
          }
          if constexpr (g == 96) STAMP(18);
          if constexpr (g == 128) STAMP(20);
          if constexpr (g == 160) STAMP(19);
          if constexpr (g == 192) STAMP(21);
          if constexpr (g == 224) STAMP(22);
          if constexpr (g == 240) STAMP(25);
          if constexpr (g == 272) STAMP(26);
          if constexpr (g == 304) STAMP(27);
          if constexpr (sched::poll_a(g)) fpa = poll_ld(rs, lane < 8 ? lane * 8 : OOB, fh_poll);
          if constexpr (sched::poll_b(g)) fpb = poll_ld(rs, lane < 8 ? lane * 8 : OOB, fh_poll);
          if constexpr (!P4) {
            if constexpr (g >= 96 && s == 0) { const f32x16 z = {}; acc[T & 3] = mfma8(agn, st.aug, z); }
            acc[T & 3] = mfma16(ar[rslot(g)], x[s], acc[T & 3]); OPQV(acc[T & 3]);
          } else if constexpr (u == 1) { const f32x16 z = {}; c1 = mfma16(ar[rslot(g)], hr[0], z); OPQV(c1); }
          else if constexpr (u & 1) { c1 = mfma16(ar[rslot(g)], hr[(u >> 1) % D4], c1); OPQV(c1); }
          else { c0 = mfma16(ar[rslot(g)], hr[(u >> 1) % D4], c0); OPQV(c0); }
          if constexpr ((left > 6 || left <= 4) && rnext(g) < 336) ar[rslot(g)] = aread(IC(rnext(g)));
          if constexpr (g < 96 && T == 0 && s + D3 < 32) x[s + D3] = xload(IC(s + D3));
          if constexpr (g < 24 && T == 1) sp[s] = ld8(rs, h * 2048 + r * 8 + s * 256, OFF_ST + (layer * 128 + tb3) * 4096u);
          if constexpr (g == 50) { st = make_stats(sp, h); cg = st.rstd * -LOG2E; cu = __builtin_amdgcn_rcpf(st.rstd); OPQV(cg); OPQV(cu); OPQV(st.aug); }
          if constexpr (g == 52 || g == 56 || g == 60) agn = *(LAS v2u*)(L + aga + ((g - 52) / 4) * 256);
          if constexpr (g == 55 || g == 59 || g == 63) { constexpr int t = (g - 55) / 4; acc[t] = mfma8(agn, st.aug, acc[t]); OPQV(acc[t]); }
          if constexpr (left == 8 && g < 200) agn = *(LAS v2u*)(L + aga + (g < 96 ? 3 : T + 1) * 256);
          if constexpr (sched::flag(g) > 0) {
            asm volatile("s_waitcnt vmcnt(%0)" :: "i"(sched::younger(sched::store_at(sched::flag(g) - 1), g)) : "memory");
            set_flag(rs, fh_mine, tg, sched::flag(g), lane, RO, 256);
          }
          if constexpr (sched::check(g) > 0) {
            if (!flags_ok(sched::check(g) == 2 ? fpa : fpb, tg, sched::check(g), lane, 8)) wait_flags(rs, fh_poll, tg, sched::check(g), lane, 8);
          }
          if constexpr (g >= 96) sfor<0, 56>([&](auto N) { constexpr int k = g - sched::epi_at(N); if constexpr (k >= 0 && k < 6) epi(N, IC(k)); });
          if constexpr (g == sched::RES) { const unsigned xr1 = OFF_X1 + layer * (2 * MB) + xfo; r0 = ld16(rs, lane16, xr1); r1 = ld16(rs, lane16 + 1024, xr1); }
          if constexpr (sched::hload(g) >= 0) {
            constexpr int q = sched::hload(g), c = q >> 3;
            hr[q % D4] = ld16(rs, lane16 + (c & 3) * 1024, hb4 + (c >> 2) * 4096u + (q & 7) * 7168u);
          }
          if constexpr (g >= 216 && g < 224) {
            constexpr int i = g - 216;
            const unsigned rv = i < 4 ? r0[i & 3] : r1[i & 3];
            float a0 = bflo(rv), a1 = bfhi(rv); OPQV(a0); OPQV(a1); c0[2 * i] = a0; c0[2 * i + 1] = a1;
          }
          SB();
        });
        unsigned sown = R4S + cw * 4096 + lane16; OPQV(sown);
#pragma unroll
        for (int g = 0; g < 4; ++g) *(LAS f32x4*)(L + sown + g * 1024) = f32x4{c1[4 * g], c1[4 * g + 1], c1[4 * g + 2], c1[4 * g + 3]};
        OPQV(c0);
        STAMP(28);
        BAR();
        STAMP(29);
#pragma unroll
        for (int g = 0; g < 4; ++g) { const f32x4 t = *(LAS f32x4*)(L + (sown ^ 4096u) + g * 1024); c0[4 * g] += t[0]; c0[4 * g + 1] += t[1]; c0[4 * g + 2] += t[2]; c0[4 * g + 3] += t[3]; }
        v4u y0, y1;
        pack_tile(c0, y0, y1);
        if (layer == 0) {
          st16(y0, rs, lane16, OFF_X2 + xfo); st16(y1, rs, lane16 + 1024, OFF_X2 + xfo);
          st8(tile_stats(c0), rs, h ? OOB : r * 8, OFF_ST + ((64 + tb2) * 16 + 2 * nblk + kh) * 256u);
          VMWAIT(0);
          set_flag(rs, (FL_X + 64 + tb2) * 128u + (2 * nblk + kh) * 8, tg, 1u, lane, RX, 192);
        } else {
          const rsrc_t ro = __builtin_amdgcn_make_buffer_rsrc((void*)kargs()->out, 0, SEQ * DM * 2, 0x00020000);
          const unsigned a0 = y0.x, a1 = y0.y, a2 = y0.z, a3 = y0.w, b0 = y1.x, b1 = y1.y, b2 = y1.z, b3 = y1.w;
          const auto p0 = __builtin_amdgcn_permlane32_swap(a0, a2, false, false), p1 = __builtin_amdgcn_permlane32_swap(a1, a3, false, false);
          const auto p2 = __builtin_amdgcn_permlane32_swap(b0, b2, false, false), p3 = __builtin_amdgcn_permlane32_swap(b1, b3, false, false);
          const unsigned vo = r * 1024 + h * 16, so = tb2 * 32768u + nblk * 128u + kh * 64u;
          const v4u z0 = {p0[0], p1[0], p0[1], p1[1]}, z1 = {p2[0], p3[0], p2[1], p3[1]};
          __builtin_amdgcn_raw_buffer_store_b128(z0, ro, vo, so, OUTAUX); __builtin_amdgcn_raw_buffer_store_b128(z1, ro, vo + 32, so, OUTAUX);
          asm volatile("s_nop 1" :: "v"(z0), "v"(z1) : "memory");
        }
        STAMP(30);
      }
      if (layer0 == 1) break;
      STAMP(32);
      { v4u xr[32]; Stats st0; st0.rstd = 0.f; st0.mean = 0.f; st0.aug = v2u{0u, 0u}; qkv(std::false_type{}, xr, st0, 0); }
    }
  } else {
   {
    const int layer0 = 0;
    STAMP(31);
    ROLES(); LANE_CONSTS();
    const int un = grp * 16 + mem, g = lane >> 4, q = (lane & 15) >> 2, pp = lane & 3;
    const rsrc_t rx = __builtin_amdgcn_make_buffer_rsrc((void*)a.x, 0, SEQ * DM * 2, 0x00020000);
    const int ucb = un >> 5, uk = un & 31;
    const bool uw = cw == 0;
    if (uw) {
      const rsrc_t rw = __builtin_amdgcn_make_buffer_rsrc((void*)a.wqkv, 0, DM * 3072, 0x00020000);
      const unsigned uvo = (lane >> 3) * 3072 + (lane & 7) * 16, uso = 16 * uk * 3072 + ucb * 128;
#pragma unroll
      for (int i = 0; i < 6; ++i) dma1p(rw, lb + S_Q + i * 1024u, uso + (i >> 1) * 1024u + (i & 1) * 8u * 3072u, uvo);
    }
    if (cw == 3) {
      unsigned long long pc; unsigned len;
      asm volatile("s_getpc_b64 %0\n.Lmk_here:\n\ts_mov_b32 %1, .Lmk_last-.Lmk_here" : "=s"(pc), "=s"(len));
      const rsrc_t rc = __builtin_amdgcn_make_buffer_rsrc((void*)(pc & ~127ull), 0, len & ~15u, 0x00020000);
      dma1p(rc, lb + S_DUMMY, (blockIdx.x >> 3) * 2048u, lane16); dma1p(rc, lb + S_DUMMY + 1024u, (blockIdx.x >> 3) * 2048u + 1024u, lane16);
    }
    const unsigned token = 8 * un + 2 * cw;
    v2u ep; v4u row0, row1;
    asm volatile("s_nop 4\n\tbuffer_load_dwordx2 %0, off, %3, 0 sc1\n\tbuffer_load_dwordx4 %1, %4, %5, %6 offen\n\tbuffer_load_dwordx4 %2, %4, %5, %6 offen offset:1024"
                 : "=&v"(ep), "=&v"(row0), "=&v"(row1) : "s"(rs), "v"(lane16), "s"(rx), "s"(token * 1024u) : "memory");
    asm volatile("s_waitcnt vmcnt(0)" : "+v"(ep), "+v"(row0), "+v"(row1) :: "memory");
    MAKE_TAG(ep);
    STAMP(12);
    if (uw) {
      const unsigned ta = S_Q + (4 * (g >> 1) + q) * 128 + (g & 1) * 32 + pp * 8;
      v4u f[6];
#pragma unroll
      for (int i = 0; i < 6; ++i) f[i] = trfrag(L, ta + (i >> 1) * 2048 + (i & 1) * 64, 1024);
#pragma unroll
      for (int i = 0; i < 6; ++i) st16(f[i], rs, lane16, OFF_WT + (16 * (i >> 1) + 2 * ucb + (i & 1)) * 32768u + uk * 1024u);
#pragma unroll
      for (int i = 0; i < 6; ++i)
        __builtin_amdgcn_raw_buffer_store_b32(__float_as_uint(xhalf_add(sum8(f[i], 0.f))), rs, h ? OOB : r * 4,
                                              OFF_CSP + (16 * (i >> 1) + 2 * ucb + (i & 1)) * 4096u + uk * 128u, 16);
    }
    {
      const unsigned t32 = (tg.lo >> 16) | (tg.hi << 16);
      const unsigned a0 = row0.x, a1 = row0.y, a2 = row0.z, a3 = row0.w, b0 = row1.x, b1 = row1.y, b2 = row1.z, b3 = row1.w;
      const float s0 = wave_add(sum8(row0, 0.f)), s1 = wave_add(dot2(a0, a0, dot2(a1, a1, dot2(a2, a2, dot2(a3, a3, 0.f)))));
      const float s2 = wave_add(sum8(row1, 0.f)), s3 = wave_add(dot2(b0, b0, dot2(b1, b1, dot2(b2, b2, dot2(b3, b3, 0.f)))));
      const float v = (lane & 2) ? ((lane & 1) ? s3 : s2) : ((lane & 1) ? s1 : s0);
      st8(v2u{__float_as_uint(v), t32}, rs, lane < 4 ? lane * 8 : OOB, OFF_ST0 + token * 16u);
    }
    if (uw) { VMWAIT(1); set_flag(rs, (FL_Q + ucb * 2) * 128u + uk * 8u, tg, 1u, lane, RQ, 16); }
    STAMP(13);
    STAMP(14);
    if (cw == 3) { wait_flags(rs, (FL_Q + (grp % RQ) * 16 + (mem >> 1) * 2) * 128u, tg, 1u, lane, 32); relay_set(L, tg, 1u); }
    relay_wait(L, tg, 1u);
    STAMP(0);
    WQKV_DMA(dma4, 0); WQKV_DMA(dma4, 1);
    VMWAIT(8);
    STAMP(1);
    BAR();
    STAMP(2);
    const int uty = un < 112 ? 0 : (un < 224 ? 1 : 2);
    const int pcb = uty == 0 ? un >> 2 : (uty == 1 ? (un - 112) / 14 : (un - 224) >> 2), pkq = uty == 1 ? (un - 112) % 14 : un & 3, pks = 0; (void)pks;
    if (cw == 3) {
      if (uty == 0) {
        f32x2 cs = {0.f, 0.f};
#pragma unroll
        for (int i = 0; i < 4; ++i) { cs[0] += *(LAS float*)(L + S_PART + (i * 128 + lane) * 4); cs[1] += *(LAS float*)(L + S_PART + (i * 128 + 64 + lane) * 4); }
        __builtin_amdgcn_raw_buffer_store_b32(__float_as_uint(cs[0]), rs, lane16, OFF_AUG + pcb * 2048u + pkq * 4u, 16);
        __builtin_amdgcn_raw_buffer_store_b32(__float_as_uint(cs[1]), rs, lane16 + 1024, OFF_AUG + pcb * 2048u + pkq * 4u, 16);
      }
    }
    unsigned cp[16];
    AUGQ_LOAD(cp);
    AUGQ_PUT(cp, S_AUGQ);
    VMWAIT(0);
    WQKV_DMA(dma4, 2);
    BAR();
    if (cw == 3) set_flag(rs, (FL_P + (uty == 0 ? 0 : (uty == 1 ? 28 : 36)) + pcb) * 128u + pkq * 8u, tg, 1u, lane);
    VMWAIT(0);
    BAR();
    if (cw < 2) {
      const unsigned c0 = (7 * mem) >> 2, nw = 4 + (cw ? 14 : 4 * (((7 * mem + 6) >> 2) - c0 + 1)), l4 = lane - 4;
      wait_words(rs, lane < 4 ? (36 + nblk) * 128 + lane * 8 : ((unsigned)lane < nw ? (cw ? (28 + nblk) * 128 + l4 * 8 : (c0 + (l4 >> 2)) * 128 + (l4 & 3) * 8) : OOB),
                 FL_P * 128u, tg, 1u, (unsigned)lane < nw);
#pragma unroll
      for (int i = 0; i < 8; ++i) dma4(rs, lb + R0 + cw * 32768u + i * 4096u, OFF_WO + nblk * 65536u + cw * 32768u + i * 4096u, lane16);
    }
   }
    for (int layer0 = 0;; ++layer0) {
      ROLES(); LANE_CONSTS();
      const unsigned lw = cw;
      const unsigned gu = OFF_WGU + mem * 7 * 32768u, wd = OFF_WD + nblk * 7 * 32768u;
      unsigned wlo = lw * 8192u + lane16, whi = wlo + R2; OPQV(wlo); OPQV(whi);
      v4u sa[4][8];
      auto stage = [&](auto B, unsigned src, auto PACED) {
        constexpr int b = B;
        sfor<0, 8>([&](auto I) {
          constexpr int i = I;
          sa[b][i] = ld16(rs, lane16 + (i & 3) * 1024, src + lw * 8192u + (i >> 2) * 4096u);
          if constexpr (PACED) asm volatile(DMA_W ::: "memory"); else SB();
        });
      };
      auto put = [&](auto B, unsigned base) { constexpr int b = B; sfor<0, 8>([&](auto I) { constexpr int i = I; *(LAS v4u*)(L + base + i * 1024) = sa[b][i]; if constexpr (PUTGAP > 0) { SB(); asm volatile("s_nop %0" :: "i"(PUTGAP)); SB(); } }); };
      auto sload = [&](auto N) { constexpr int n = N; if constexpr (n < 24) sa[n >> 3][n & 7] = ld16(rs, lane16 + (n & 3) * 1024, (n < 16 ? gu + (2 + (n >> 3)) * 32768u : wd + 3 * 32768u) + lw * 8192u + ((n >> 2) & 1) * 4096u); };
      f32x16 cml;
      sfor<0, 8>([&](auto I) { CAUSAL2(cml, 2 * I); });
      attention(layer0, cml, [&](auto K) { constexpr int k = K; if constexpr (k < 5) sfor<5 * k, 5 * k + 5>(sload); }, [&] { sfor<0, 24>(sload); });
      dma4_paced(rs, lb + R2 + lw * 8192u, gu + lw * 8192u, lane16);
      dma4_paced(rs, lb + R2 + lw * 8192u + 4096u, gu + lw * 8192u + 4096u, lane16);
      ready_wait(L, S_ATT, lane, tg, 1u + layer); ready_wait(L, S_ATT + 32, lane, tg, 1u + layer);
      ready_set(L, S_RDY, lw, tg, 4u + layer);
      stage(IC(3), wd + 4 * 32768u, IC(1));
      {
        const v4u p0 = ld16(rs, r * 16 + lw * 1024, OFF_AUG + mem * 7 * 512u), p1 = ld16(rs, lw < 3 ? r * 16 + lw * 1024 + 512 : OOB, OFF_AUG + mem * 7 * 512u);
        dma4_paced(rs, lb + R3 + lw * 8192u, gu + 32768u + lw * 8192u, lane16);
        dma4_paced(rs, lb + R3 + lw * 8192u + 4096u, gu + 32768u + lw * 8192u + 4096u, lane16);
        *(LAS v2u*)(L + R4V + lw * 512 + r * 8) = make_aug(p0);
        *(LAS v2u*)(L + R4V + lw * 512 + 256 + r * 8) = make_aug(p1);
      }
      STAMP(14); BAR();
      put(IC(0), wlo); put(IC(1), wlo + R1);
      VMWAIT(0);
      ready_set(L, S_RDY, lw, tg, 1u + layer);
      STAMP(17);
      stage(IC(0), wd + 5 * 32768u, IC(1));
      stage(IC(1), wd + 6 * 32768u, IC(1));
      STAMP(18); BAR();
      dma_tile(rs, lb + R2, gu + 4 * 32768u, lw, lane16);
      dma_tile(rs, lb + R3, gu + 5 * 32768u, lw, lane16);
      VMWAIT(8);
      dma4(rs, lb + R0 + lw * 8192u, gu + 6 * 32768u + lw * 8192u, lane16);
      STAMP(20); BAR();
      dma4(rs, lb + R0 + lw * 8192u + 4096u, gu + 6 * 32768u + lw * 8192u + 4096u, lane16);
      dma_tile(rs, lb + R1, wd + 0 * 32768u, lw, lane16);
      VMWAIT(16); STAMP(19); BAR();
      dma_tile(rs, lb + R2, wd + 1 * 32768u, lw, lane16);
      VMWAIT(16); STAMP(21); BAR();
      dma_tile(rs, lb + R3, wd + 2 * 32768u, lw, lane16);
      VMWAIT(16); STAMP(22); BAR();
      put(IC(2), wlo);
      VMWAIT(8); STAMP(25); BAR();
      const unsigned fh = (FL_H + (nblk % RO) * 256 + (layer * 64 + tb2) * 2 + kh) * 128u, fo = lane < 8 ? lane * 8 : OOB;
      put(IC(3), wlo + R1);
      const v2u f5 = poll_ld(rs, fo, fh);
      VMWAIT(1);
      BAR();
      const v2u f6 = poll_ld(rs, fo, fh);
      put(IC(0), whi);
      if (!flags_ok(f5, tg, 5u, lane, 8)) wait_flags(rs, fh, tg, 5u, lane, 8);
      STAMP(26); BAR();
      const v2u f7 = poll_ld(rs, fo, fh);
      put(IC(1), whi + 32768u);
      if (!flags_ok(f6, tg, 6u, lane, 8)) wait_flags(rs, fh, tg, 6u, lane, 8);
      BAR();
      if (!flags_ok(f7, tg, 7u, lane, 8)) wait_flags(rs, fh, tg, 7u, lane, 8);
      STAMP(27); BAR();
      BAR();
      STAMP(28); BAR();
      if (layer0 == 1) {
        if ((grp | mem | cw) == 0) {
#pragma unroll
          for (int i = 0; i < 4; ++i) wait_flags(rs, FL_SEEN * 128u + i * 512u, tg, 1u, lane, 64);
          set_flag(rs, FL_EPOCH * 128u, tg, 0u, lane);
        }
        break;
      }
      WQKV_DMA(dma4_paced, 0);
      unsigned cp[16];
      AUGQ_LOAD(cp);
      WQKV_DMA(dma4_paced, 1);
      ready_set(L, S_RDY, lw, tg, 3u);
      AUGQ_PUT(cp, S_AUGQ1);
      WQKV_DMA(W2DMA, 2);
      VMWAIT(8);
      STAMP(34); BAR();
      VMWAIT(0);
      BAR();
      WO_DMA();
    }
  }
  STAMP_OUT();
}

extern "C" void kernel_launch(void* const* d_in, const int* in_sizes, int n_in, void* d_out, int out_size, void* d_ws, size_t ws_size,
                              hipStream_t stream) {
  Args a;
  a.ws = (unsigned char*)d_ws; a.out = (u16*)d_out;
  a.x = (const u16*)d_in[0]; a.wqkv = (const u16*)d_in[1]; a.wo = (const u16*)d_in[2];
  a.wg = (const u16*)d_in[4]; a.wu = (const u16*)d_in[5]; a.wd = (const u16*)d_in[6];
  (void)hipFuncSetAttribute((const void*)main_k, hipFuncAttributeMaxDynamicSharedMemorySize, LDS_BYTES);
  main_k<<<256, 512, LDS_BYTES, stream>>>(a);
}
```
